```python
import torch

def _softmax(x, axis=-1):
    return torch.softmax(x, dim=axis)

def _gelu(x):
    return torch.nn.functional.gelu(x, approximate='tanh')

def _silu(x):
    return torch.nn.functional.silu(x)

def _sigmoid(x):
    return torch.sigmoid(x)

def _layernorm(x, axis=-1, eps=1e-05):
    m = x.mean(dim=axis, keepdim=True)
    v = x.var(dim=axis, keepdim=True, unbiased=False)
    return (x - m) / torch.sqrt(v + eps)

def _scatter_add(tgt, idx, src, axis=0):
    return tgt.clone().index_add_(axis, idx, src)

def _segment_sum(src, seg, n):
    out = torch.zeros((n,) + src.shape[1:], dtype=src.dtype)
    return out.index_add_(0, seg, src)

def _pad(x, pw):
    flat = tuple((v for pair in reversed(pw) for v in pair))
    return torch.nn.functional.pad(x, flat)
kernel_name = 'recurrent_scan__memory_aug_recurrent_mem_tr__glu_gated_swiglu__prompt'

def setup_inputs():

    _w = lambda a: (lambda t: t.to(torch.bfloat16) if t.is_floating_point() else t)(torch.as_tensor(a))

    def _A_setup():
        B, S, Dm = (1, 2048, 640)
        H, Dh = (32, 128)
        M, seg = (8, 32)
        seg = min(seg, S)
        S = S // seg * seg
        return {'x': _w((lambda *s: torch.randn(*s, dtype=torch.bfloat16) * 0.1)(B, S, Dm)), 'mem0': _w((lambda *s: torch.randn(*s, dtype=torch.bfloat16) * 1.6)(B, M, Dm)), 'Wq': _w((lambda *s: torch.randn(*s, dtype=torch.bfloat16) * 0.8)(Dm, H, Dh) * Dm ** (-0.5)), 'Wk': _w((lambda *s: torch.randn(*s, dtype=torch.bfloat16) * 0.8)(Dm, H, Dh) * Dm ** (-0.5)), 'Wv': _w((lambda *s: torch.randn(*s, dtype=torch.bfloat16) * 1.6)(Dm, H, Dh) * Dm ** (-0.5)), 'Wo': _w((lambda *s: torch.randn(*s, dtype=torch.bfloat16) * 1.6)(H, Dh, Dm) * (H * Dh) ** (-0.5)), 'seg_len': seg}
    _A_in = _A_setup()

    def _B_setup():
        B, S, D, F = (1, 2048, 640, 1728)
        return {'x': _w((lambda *s: torch.randn(*s, dtype=torch.bfloat16) * 0.1)(B, S, D)), 'w1': _w((lambda *s: torch.randn(*s, dtype=torch.bfloat16) * 0.4)(D, F) * (2.0 / D) ** 0.5), 'w3': _w((lambda *s: torch.randn(*s, dtype=torch.bfloat16) * 0.4)(D, F) * (2.0 / D) ** 0.5), 'w2': _w((lambda *s: torch.randn(*s, dtype=torch.bfloat16) * 0.4)(F, D) * (2.0 / F) ** 0.5)}
    _B_in = _B_setup()
    return {'A_x': _A_in['x'], 'A_mem0': _A_in['mem0'], 'A_Wq': _A_in['Wq'], 'A_Wk': _A_in['Wk'], 'A_Wv': _A_in['Wv'], 'A_Wo': _A_in['Wo'], 'A_seg_len': _A_in['seg_len'], 'B_x': _B_in['x'], 'B_w1': _B_in['w1'], 'B_w3': _B_in['w3'], 'B_w2': _B_in['w2']}

def reference(A_x, A_mem0, A_Wq, A_Wk, A_Wv, A_Wo, A_seg_len, B_x, B_w1, B_w3, B_w2):

    def _A_ref(x, mem0, Wq, Wk, Wv, Wo, seg_len):
        B, S, Dm = x.shape
        H, Dh = (Wq.shape[1], Wq.shape[2])
        M = mem0.shape[1]
        scale = Dh ** (-0.5)
        n_seg = S // seg_len
        L = M + seg_len
        idx = torch.arange(L)
        mask = (idx[:, None] < M) | (idx[:, None] >= idx[None, :])
        mem = mem0
        outs = []
        for s in range(n_seg):
            xs = x[:, s * seg_len:(s + 1) * seg_len, :].clone()
            h = torch.concatenate([mem, xs], axis=1)
            q = torch.einsum('btd,dhe->bhte', h, Wq)
            k = torch.einsum('btd,dhe->bhte', h, Wk)
            v = torch.einsum('btd,dhe->bhte', h, Wv)
            scores = torch.einsum('bhqe,bhke->bhqk', q, k) * scale
            scores = torch.where(mask[None, None], scores, -1000000000.0)
            p = _softmax(scores, axis=-1)
            o = torch.einsum('bhqk,bhke->bhqe', p, v)
            o = torch.einsum('bhte,hed->btd', o, Wo)
            mem = o[:, :M, :].clone()
            outs.append(o[:, M:, :])
        return torch.concatenate(outs, axis=1)

    def _B_ref(x, w1, w3, w2):
        gate = torch.einsum('bsd,df->bsf', x, w1)
        up = torch.einsum('bsd,df->bsf', x, w3)
        h = _silu(gate) * up
        out = torch.einsum('bsf,fd->bsd', h, w2)
        return out
    x = A_x
    xn = _layernorm(x, axis=-1)
    s = _A_ref(xn, A_mem0, A_Wq, A_Wk, A_Wv, A_Wo, A_seg_len)
    g = _silu(_B_ref(xn, B_w1, B_w3, B_w2))
    return x + s * g
```

```cpp
#include <hip/hip_runtime.h>
#include <stdint.h>
#define FIXED

typedef __bf16 bf16x8 __attribute__((ext_vector_type(8)));
typedef __bf16 bf16x16 __attribute__((ext_vector_type(16)));
typedef __bf16 bf16x2 __attribute__((ext_vector_type(2)));
typedef float f32x2 __attribute__((ext_vector_type(2)));
typedef float f32x4 __attribute__((ext_vector_type(4)));
typedef unsigned u32x2 __attribute__((ext_vector_type(2)));
typedef unsigned u32x4 __attribute__((ext_vector_type(4)));
typedef unsigned u32x8 __attribute__((ext_vector_type(8)));
typedef short s16x4 __attribute__((ext_vector_type(4)));
#define LAS __attribute__((address_space(3)))
#define DEV __device__ __forceinline__

constexpr int NSEG = 64;
constexpr unsigned MB = 1u << 20;
constexpr unsigned OFF_COMM = 0;
constexpr unsigned OFF_CTL = 4 * MB;
constexpr unsigned OFF_XN = 5 * MB;
constexpr unsigned OFF_PT = 14 * MB;
constexpr unsigned OFF_HH = 20 * MB;
constexpr unsigned OFF_BLK = 32 * MB;
constexpr unsigned OFF_DBG = 240 * MB;
constexpr unsigned WS_BYTES = 248 * MB;
constexpr unsigned OOB = 0xF0000000u;
constexpr unsigned C_MEM1 = 16384, C_PM = 16384 + 2048, C_PX = 2 * 16384, C_Z = 4 * 16384;
constexpr unsigned CTL_POL = 65536;
constexpr unsigned CTL_RL = 65536 + 4096;
constexpr unsigned CTL_HP = 131072;
constexpr unsigned SENT = 0xFFFFFFFFu;
constexpr int SPIN_CAP = 1 << 20;
constexpr int SC1 = 16;

typedef __amdgpu_buffer_rsrc_t rsrc_t;
DEV u32x4 ld128(rsrc_t r, unsigned vo, unsigned so) { return __builtin_amdgcn_raw_buffer_load_b128(r, vo, so, SC1); }
DEV u32x2 ld64(rsrc_t r, unsigned vo, unsigned so) { return __builtin_amdgcn_raw_buffer_load_b64(r, vo, so, SC1); }
DEV unsigned ld32(rsrc_t r, unsigned vo, unsigned so) { return __builtin_amdgcn_raw_buffer_load_b32(r, vo, so, SC1); }
DEV void st128(rsrc_t r, u32x4 v, unsigned vo, unsigned so) { __builtin_amdgcn_raw_buffer_store_b128(v, r, vo, so, SC1); }
DEV void st64(rsrc_t r, u32x2 v, unsigned vo, unsigned so) { __builtin_amdgcn_raw_buffer_store_b64(v, r, vo, so, SC1); }
DEV void st32(rsrc_t r, unsigned v, unsigned vo, unsigned so) { __builtin_amdgcn_raw_buffer_store_b32(v, r, vo, so, SC1); }
DEV unsigned pk(float a, float b) { bf16x2 v = {(__bf16)a, (__bf16)b}; return __builtin_bit_cast(unsigned, v); }
DEV unsigned pks(float a, float b) { return min(pk(a, b), 0xFFFFFFFEu); }
DEV unsigned mx4(u32x4 v) { return max(max(v.x, v.y), max(v.z, v.w)); }
DEV f32x4 mfma(u32x4 a, u32x4 b, f32x4 c) { return __builtin_amdgcn_mfma_f32_16x16x32_bf16(__builtin_bit_cast(bf16x8, a), __builtin_bit_cast(bf16x8, b), c, 0, 0, 0); }
DEV f32x4 smf(u32x4 a, u32x8 b, f32x4 c, int idx) { return __builtin_amdgcn_smfmac_f32_16x16x64_bf16(__builtin_bit_cast(bf16x8, a), __builtin_bit_cast(bf16x16, b), c, idx, 0, 0); }
DEV u32x4 lo4(u32x8 v) { return __builtin_shufflevector(v, v, 0, 1, 2, 3); }
DEV u32x4 hi4(u32x8 v) { return __builtin_shufflevector(v, v, 4, 5, 6, 7); }
DEV u32x8 cat8(u32x4 a, u32x4 b) { return __builtin_shufflevector(a, b, 0, 1, 2, 3, 4, 5, 6, 7); }
DEV u32x4 cat4(u32x2 a, u32x2 b) { return __builtin_shufflevector(a, b, 0, 1, 2, 3); }
DEV u32x2 trread(unsigned lds_addr) { return __builtin_bit_cast(u32x2, __builtin_amdgcn_ds_read_tr16_b64_v4i16((LAS s16x4*)(uintptr_t)lds_addr)); }
DEV u32x4 ldsr128(unsigned a) { return *(LAS u32x4*)(uintptr_t)a; }
DEV void ldsw128(unsigned a, u32x4 v) { *(LAS u32x4*)(uintptr_t)a = v; }
DEV void ldsw64(unsigned a, u32x2 v) { *(LAS u32x2*)(uintptr_t)a = v; }
#define BAR() asm volatile("s_waitcnt lgkmcnt(0)\n\ts_barrier" ::: "memory")
#define VMWAIT(n) asm volatile("s_waitcnt vmcnt(" #n ")" ::: "memory")
#define LGWAIT() asm volatile("s_waitcnt lgkmcnt(0)" ::: "memory")
#define CFENCE() asm volatile("" ::: "memory")
template <int N> DEV void vmwait() { asm volatile("s_waitcnt vmcnt(%0)" :: "n"(N) : "memory"); }
DEV void glds(const void* sbase, unsigned voff, unsigned ldst) {
  unsigned keep;
  asm volatile("s_mov_b32 %0, m0\n\ts_mov_b32 m0, %3\n\ts_nop 0\n\tglobal_load_lds_dwordx4 %1, %2\n\ts_mov_b32 m0, %0" : "=&s"(keep) : "v"(voff), "s"(sbase), "s"(ldst) : "memory");
}
constexpr unsigned CTL_XP = 196608;
DEV void glds_sc1(const void* sbase, unsigned voff, unsigned ldst) {
  unsigned keep;
  asm volatile("s_mov_b32 %0, m0\n\ts_mov_b32 m0, %3\n\ts_nop 0\n\tglobal_load_lds_dwordx4 %1, %2 sc1\n\ts_mov_b32 m0, %0" : "=&s"(keep) : "v"(voff), "s"(sbase), "s"(ldst) : "memory");
}
DEV void set_m0(unsigned v) { asm volatile("s_mov_b32 m0, %0" :: "s"(v) : "memory"); }
template <int OFF> DEV void glds_off(const void* sbase, unsigned voff) { asm volatile("global_load_lds_dwordx4 %0, %1 offset:%2 sc1" :: "v"(voff), "s"(sbase), "n"(OFF) : "memory"); }
template <int V> struct IC { static constexpr int value = V; constexpr operator int() const { return V; } };
template <int N, int I = 0, class F> DEV void static_for(F&& f) { if constexpr (I < N) { f(IC<I>{}); static_for<N, I + 1>(f); } }
DEV float fold32(float a, float b) {
  auto r = __builtin_amdgcn_permlane32_swap(__builtin_bit_cast(unsigned, a), __builtin_bit_cast(unsigned, b), false, false);
  return __builtin_bit_cast(float, (unsigned)r[0]) + __builtin_bit_cast(float, (unsigned)r[1]);
}
DEV float xmax16(float v) { auto r = __builtin_amdgcn_permlane16_swap(__builtin_bit_cast(unsigned, v), __builtin_bit_cast(unsigned, v), false, false); return fmaxf(__builtin_bit_cast(float, (unsigned)r[0]), __builtin_bit_cast(float, (unsigned)r[1])); }
DEV float xmax32(float v) { auto r = __builtin_amdgcn_permlane32_swap(__builtin_bit_cast(unsigned, v), __builtin_bit_cast(unsigned, v), false, false); return fmaxf(__builtin_bit_cast(float, (unsigned)r[0]), __builtin_bit_cast(float, (unsigned)r[1])); }
DEV float xadd16(float v) { auto r = __builtin_amdgcn_permlane16_swap(__builtin_bit_cast(unsigned, v), __builtin_bit_cast(unsigned, v), false, false); return __builtin_bit_cast(float, (unsigned)r[0]) + __builtin_bit_cast(float, (unsigned)r[1]); }
DEV float xadd32(float v) { auto r = __builtin_amdgcn_permlane32_swap(__builtin_bit_cast(unsigned, v), __builtin_bit_cast(unsigned, v), false, false); return __builtin_bit_cast(float, (unsigned)r[0]) + __builtin_bit_cast(float, (unsigned)r[1]); }
DEV float bf2f(unsigned short u) { return __builtin_bit_cast(float, (unsigned)u << 16); }
DEV float rbf(float x) { return (float)(__bf16)x; }

DEV void smf_a(f32x4& c, u32x4 a, const u32x8& w, int idx) { asm volatile("v_smfmac_f32_16x16x64_bf16 %0, %1, %2, %3" : "+v"(c) : "v"(a), "a"(w), "v"(idx)); }
DEV void smf_v(f32x4& c, u32x4 a, const u32x8& w, int idx) { asm volatile("v_smfmac_f32_16x16x64_bf16 %0, %1, %2, %3" : "+v"(c) : "v"(a), "v"(w), "v"(idx)); }
template <int I> DEV void smf_w(f32x4& c, u32x4 a, const u32x8 (&w)[40], int idx) { if constexpr (I < 32) smf_a(c, a, w[I], idx); else smf_v(c, a, w[I], idx); }
DEV void mf_va(f32x4& c, u32x4 a, u32x4 b) { asm volatile("v_mfma_f32_16x16x32_bf16 %0, %1, %2, %0" : "+v"(c) : "v"(a), "a"(b)); }
DEV void mf_vv(f32x4& c, u32x4 a, u32x4 b) { asm volatile("v_mfma_f32_16x16x32_bf16 %0, %1, %2, %0" : "+v"(c) : "v"(a), "v"(b)); }
template <bool A> DEV void pin_w(u32x8& x, u32x8& y) {
  if constexpr (A) asm volatile("s_waitcnt lgkmcnt(0)" : "+a"(x), "+a"(y) :: "memory"); else asm volatile("s_waitcnt lgkmcnt(0)" : "+v"(x), "+v"(y) :: "memory");
}
template <bool A> DEV void pin_w4(u32x4& x, u32x4& y, u32x4& z, u32x4& u) {
  if constexpr (A) asm volatile("s_waitcnt lgkmcnt(0)" : "+a"(x), "+a"(y), "+a"(z), "+a"(u) :: "memory"); else asm volatile("s_waitcnt lgkmcnt(0)" : "+v"(x), "+v"(y), "+v"(z), "+v"(u) :: "memory");
}
DEV void pin2(f32x4& x, f32x4& y) { asm volatile("" : "+v"(x), "+v"(y)); }
DEV void pin2n(f32x4& x, f32x4& y) { asm volatile("s_nop 3" : "+v"(x), "+v"(y)); }
DEV unsigned pin1(unsigned x) { asm volatile("" :: "v"(x)); return x; }
#define NOP(n) asm volatile("s_nop " #n)
#define PIN(x) asm volatile("" : "+v"(x))
#define PINS(x) asm volatile("" : "+s"(x))
DEV unsigned xcc_id() { return (unsigned)__builtin_amdgcn_s_getreg(20 | (3 << 11)) & 0xfu; }
DEV unsigned rtc() { unsigned long long t; asm volatile("s_memrealtime %0\n\ts_waitcnt lgkmcnt(0)" : "=s"(t) :: "memory"); return (unsigned)t; }
constexpr unsigned CTL_CUR = 65536 + 12288 + 1024;
constexpr unsigned OFF_REP = 8 * MB;
constexpr int MAXWAIT = 400 * 8;
#define D1_INIT 830
#define D2_INIT 500
struct Sched { int j0, jbad; unsigned base; int T, D1; unsigned c0, v0; int R; unsigned ct, tt; };
DEV unsigned now8() { return rtc() << 3; }
DEV unsigned cycles() { unsigned long long t; asm volatile("s_memtime %0\n\ts_waitcnt lgkmcnt(0)" : "=s"(t) :: "memory"); return (unsigned)t; }
DEV void clk_init(Sched& c) { c.j0 = 0; c.jbad = 0; c.base = 0; c.T = D1_INIT + D2_INIT; c.D1 = D1_INIT; c.v0 = rtc(); c.c0 = cycles(); c.R = 24 * 256; c.ct = c.c0; c.tt = 0; }
DEV void clk_update(Sched& c) {
  const unsigned v = rtc() - c.v0, y = cycles() - c.c0;
  if (v > 300) c.R = __builtin_amdgcn_readfirstlane((int)((float)y * 256.f * __builtin_amdgcn_rcpf((float)v)));
}
DEV unsigned tau_of(const Sched& c, int j) { return c.base + (j >> 1) * c.T + ((j & 1) ? c.D1 : 0); }
DEV void adopt_cur(Sched& c, u32x4 v, int j) {
  const int j0 = __builtin_amdgcn_readfirstlane(v.x);
  if (j0 > c.j0 && j0 <= j) { c.j0 = j0; c.base = __builtin_amdgcn_readfirstlane(v.y); c.T = __builtin_amdgcn_readfirstlane(v.z); c.D1 = __builtin_amdgcn_readfirstlane(v.w); }
}
DEV bool wait_until(unsigned tau) {
  int d = (int)(tau - now8());
  if (d <= 0 || d > MAXWAIT) return false;
  while ((int)(tau - now8()) > 0) {}
  return true;
}
#define NOP16 "s_nop 0\n\ts_nop 0\n\ts_nop 0\n\ts_nop 0\n\ts_nop 0\n\ts_nop 0\n\ts_nop 0\n\ts_nop 0\n\ts_nop 0\n\ts_nop 0\n\ts_nop 0\n\ts_nop 0\n\ts_nop 0\n\ts_nop 0\n\ts_nop 0\n\ts_nop 0\n\t"
#define FK 96
DEV unsigned est_cycle(unsigned tau, int R, unsigned c0, int best) {
  unsigned long long v, c; unsigned t; int rem;
  asm volatile(
    "1:\n\t"
    "s_memrealtime %0\n\ts_memtime %1\n\ts_waitcnt lgkmcnt(0)\n\t"
    "s_lshl_b32 %2, s94, 3\n\t"
    "s_sub_i32 %2, %5, %2\n\t"
    "s_mul_i32 %2, %2, %6\n\t"
    "s_ashr_i32 %2, %2, 11\n\t"
    "s_sub_i32 %3, s96, %7\n\t"
    "s_add_i32 %2, %2, %3\n\t"
    "s_min_i32 %4, %4, %2\n\t"
    "s_sub_i32 %2, %2, 100\n\t"
    "s_max_i32 %4, %4, %2\n\t"
    "s_sub_i32 %3, %4, %3\n\t"
    "s_cmp_gt_i32 %3, 520\n\t"
    "s_cbranch_scc1 1b"
    : "=&{s[94:95]}"(v), "=&{s[96:97]}"(c), "=&s"(t), "=&s"(rem), "+s"(best)
    : "s"(tau), "s"(R), "s"(c0) : "memory", "scc");
  return c0 + best;
}
DEV void release(unsigned cs) {
  unsigned long long c, pc; int rem;
  asm volatile(
    "1:\n\t"
    "s_memtime %0\n\ts_waitcnt lgkmcnt(0)\n\t"
    "s_sub_i32 %2, %3, s96\n\t"
    "s_cmp_gt_i32 %2, %4\n\t"
    "s_cbranch_scc1 1b\n\t"
    "s_sub_i32 %2, %2, %5\n\t"
    "s_max_i32 %2, %2, 0\n\t"
    "s_and_b32 %2, %2, -4\n\t"
    "s_sub_i32 %2, 140, %2\n\t"
    "s_getpc_b64 %1\n\t"
    "s_add_u32 s98, s98, %2\n\t"
    "s_addc_u32 s99, s99, 0\n\t"
    "s_setpc_b64 %1\n\t"
    NOP16 NOP16 "s_barrier"
    : "=&{s[96:97]}"(c), "=&{s[98:99]}"(pc), "=&s"(rem)
    : "s"(cs), "n"(FK + 124), "n"(FK) : "memory", "scc");
}
#define DRIFT 10
#define LATE_THR 120
constexpr unsigned L_TM = 61440;
DEV void mark_overrun(Sched& c, int j, unsigned t) { if ((int)(t - tau_of(c, j + 1)) > LATE_THR + 40) c.jbad = j; }
template <class F>
DEV unsigned trigger(rsrc_t rs, Sched& c, int j, unsigned hold, F&& poll) {
  unsigned cs, timed = 0;
  for (;;) {
    const unsigned tau = tau_of(c, j), now = now8();
    const int d = (int)(tau - now);
    const bool ok = c.j0 > c.jbad && d > -30 * 8 && d <= MAXWAIT;
    if (ok && d <= 0) { cs = cycles(); break; }
    if (ok && d <= 50 * 8) {
      const unsigned dt = tau - c.tt;
      const int prior = dt < 4096 ? (int)((dt * (unsigned)c.R) >> 11) + DRIFT : 0x7fffffff;
      cs = est_cycle(__builtin_amdgcn_readfirstlane(tau), __builtin_amdgcn_readfirstlane(c.R), __builtin_amdgcn_readfirstlane(c.ct), __builtin_amdgcn_readfirstlane(prior));
      c.ct = cs; c.tt = tau; timed = 1;
      break;
    }
    if (!ok && (unsigned)(hold - now) > (unsigned)MAXWAIT) { poll(); cs = cycles(); break; }
    if (d > 100 * 8 || !ok) { adopt_cur(c, ld128(rs, 0, OFF_CTL + CTL_CUR), j); CFENCE(); }
  }
  ldsw64(L_TM + (j & 2) * 8, (u32x2){cs, timed});
  return __builtin_amdgcn_readfirstlane(cs);
}
#define STAMP(k)
#define STV(k) stdummy
#define STAMP_DECL unsigned stdummy
#define STAMP_OUT(e, s)

#define BFAR 1
DEV unsigned blk_addr(int blk, unsigned pol, unsigned far) { return OFF_BLK + blk * 16384 + (((pol >> ((blk >> 7) & 1)) ^ far) & 1) * 8192; }
DEV unsigned z_base(int s, unsigned pol) { const int pg = s / 3; return blk_addr(10240 + pg * 128 + (s - 3 * pg) * 40, pol, 0); }
DEV unsigned kq_addr(int isq, int seg, int h, unsigned pol) { return blk_addr(isq * 2048 + seg * 32 + h, pol, 1 - BFAR); }
DEV unsigned b_addr(int seg, int cb, unsigned pol) { return blk_addr((seg * 40 + cb) * 4, pol, BFAR); }

#define EV(slot)
struct Args {
  const unsigned char* x; const unsigned char* mem0; const unsigned char* wq; const unsigned char* wk; const unsigned char* wv; const unsigned char* wo;
  const unsigned char* w1; const unsigned char* w3; const unsigned char* w2; unsigned char* out; unsigned char* ws;
};

enum { K_P, K_V, K_QK, K_VB, K_F1 };
constexpr unsigned L_XB0 = 120 * 1024, L_XB1 = 80 * 1024;
template <int KIND, class WT, class LATE>
DEV void load_weights(WT& w, const unsigned char* baseA, const unsigned char* baseB, int wave, int lane, int cmask, LATE&& late) {
  constexpr int NG = 20;
  constexpr bool WO = KIND == K_V || KIND == K_VB;
  constexpr unsigned SA = KIND == K_F1 ? 3456 : 8192, SB = WO ? 1280 : SA;
  constexpr int MA = (KIND == K_P || KIND == K_V) ? 2 : 0, MBB = KIND == K_P ? 2 : KIND == K_VB ? 1 : 0;
  const int gB = lane >> 4, q = (lane >> 2) & 3, pp = lane & 3;
  unsigned vo[2][4], rd[2][2];
#pragma unroll
  for (int m = 0; m < 2; ++m) {
    const int mode = m ? MBB : MA;
#pragma unroll
    for (int p = 0; p < 4; ++p) {
      const int key = mode == 0 ? (gB | (p >> 1) << 2) : mode == 1 ? (gB | (p & 1) << 2) : ((gB >> 1) | p << 1);
      vo[m][p] = (wave * 16 + p * 4 + gB) * (m ? SB : SA) + (((lane & 15) ^ (key << 1)) & cmask) * 16;
    }
    const int key = mode == 2 ? ((gB & 1) | (q >> 1) << 1 | (q & 1) << 2) : (q | (gB & 1) << 2);
    const int row = mode == 0 ? 8 * gB + q : mode == 1 ? 4 * gB + q : 16 * (gB >> 1) + 4 * (q >> 1) + 2 * (gB & 1) + 8 * (q & 1);
#pragma unroll
    for (int nt = 0; nt < 2; ++nt) rd[m][nt] = row * 256 + ((4 * wave + 2 * nt + (pp >> 1)) ^ (key << 1)) * 16 + (pp & 1) * 8;
  }
  auto issue = [&](int g) {
    const unsigned char* src = g < 10 ? baseA + (size_t)g * 64 * SA : WO ? baseB + (size_t)((g - 10) & 1) * 64 * SB + ((g - 10) >> 1) * 256 : baseB + (size_t)(g - 10) * 64 * SB;
#pragma unroll
    for (int p = 0; p < 4; ++p) glds(src, vo[g >= 10][p], (g % 7) * 16384 + wave * 4096 + p * 1024);
  };
#pragma unroll
  for (int g = 0; g < 7; ++g) issue(g);
  static_for<NG>([&](auto G) {
    constexpr int g = G, mode = g < 10 ? MA : MBB;
    if constexpr (g == 0) vmwait<24>(); else if constexpr (g < NG - 5) vmwait<20>(); else if constexpr (g == NG - 5) late();
    if constexpr (g <= NG - 5) BAR();
    if constexpr (g >= 1 && g + 6 < NG) issue(g + 6);
    constexpr unsigned phs = mode == 2 ? 256 : mode == 1 ? 16 * 256 : 4 * 256, sl = (g % 7) * 16384;
#pragma unroll
    for (int nt = 0; nt < 2; ++nt) {
      const unsigned rb = rd[g >= 10][nt] + sl;
      u32x2 a = trread(rb), b = trread(rb + phs), c = trread(rb + 8192), d = trread(rb + 8192 + phs);
      if constexpr (KIND == K_P || KIND == K_V) w[g * 2 + nt] = cat8(cat4(a, b), cat4(c, d));
      else { w[g * 4 + nt * 2] = cat4(a, b); w[g * 4 + nt * 2 + 1] = cat4(c, d); }
    }
    if constexpr (KIND == K_P || KIND == K_V) pin_w<g < 16>(w[g * 2], w[g * 2 + 1]);
    else pin_w4<g < 16>(w[g * 4], w[g * 4 + 1], w[g * 4 + 2], w[g * 4 + 3]);
  });
}
DEV void load_w2(u32x4 (&w)[56], const unsigned char* base, int wave, int lane) {
  const int gB = lane >> 4, q = (lane >> 2) & 3, pp = lane & 3;
  const unsigned ring = wave * 32768;
  const unsigned vo = (lane >> 3) * 1280 + ((lane & 7) ^ (((lane >> 4) & 3) << 1)) * 16;
  unsigned rd[4];
#pragma unroll
  for (int c = 0; c < 4; ++c) rd[c] = ring + (4 * gB + q) * 128 + ((2 * c + (pp >> 1)) ^ ((((gB & 1) << 1) | (q >> 1)) << 1)) * 16 + (pp & 1) * 8;
  auto issue = [&](int g) {
    const unsigned char* src = base + (size_t)min(wave * 7 + g, 26) * 64 * 1280;
#pragma unroll
    for (int p = 0; p < 8; ++p) glds(src + p * 8 * 1280, vo, ring + (g & 3) * 8192 + p * 1024);
  };
#pragma unroll
  for (int g = 0; g < 4; ++g) issue(g);
  static_for<7>([&](auto G) {
    constexpr int g = G;
    vmwait<8 * (6 - g < 3 ? 6 - g : 3)>();
#pragma unroll
    for (int c = 0; c < 4; ++c) {
      const unsigned rb = rd[c] + (g & 3) * 8192;
      w[g * 8 + c * 2] = cat4(trread(rb), trread(rb + 2048)); w[g * 8 + c * 2 + 1] = cat4(trread(rb + 4096), trread(rb + 6144));
    }
    pin_w4<true>(w[g * 8], w[g * 8 + 1], w[g * 8 + 2], w[g * 8 + 3]); pin_w4<true>(w[g * 8 + 4], w[g * 8 + 5], w[g * 8 + 6], w[g * 8 + 7]);
    if constexpr (g + 4 < 7) issue(g + 4);
  });
}

DEV unsigned prog_off(int type, int h, int par, int wave) { return OFF_CTL + (((type * 32 + h) * 2 + par) * 4 + wave) * 128; }
DEV unsigned hprog_off(int rep, int slice) { return OFF_CTL + CTL_HP + (rep * 56 + slice) * 128; }
DEV float silu(float x) { return x * __builtin_amdgcn_rcpf(1.f + __builtin_amdgcn_exp2f(-1.4426950408889634f * x)); }
DEV u32x2 pk4(f32x4 v) { return (u32x2){pk(v[0], v[1]), pk(v[2], v[3])}; }

template <int I, int HI, bool WA, bool FIRST, int N>
DEV void mf_w(f32x4& c, const u32x4 (&w)[N], u32x4 x) {
  const u32x4 f = w[2 * I + HI];
  if constexpr (FIRST) {
    if constexpr (I < 32) { if constexpr (WA) asm volatile("v_mfma_f32_16x16x32_bf16 %0, %1, %2, 0" : "=&v"(c) : "a"(f), "v"(x)); else asm volatile("v_mfma_f32_16x16x32_bf16 %0, %2, %1, 0" : "=&v"(c) : "a"(f), "v"(x)); }
    else { if constexpr (WA) asm volatile("v_mfma_f32_16x16x32_bf16 %0, %1, %2, 0" : "=&v"(c) : "v"(f), "v"(x)); else asm volatile("v_mfma_f32_16x16x32_bf16 %0, %2, %1, 0" : "=&v"(c) : "v"(f), "v"(x)); }
  } else {
    if constexpr (I < 32) { if constexpr (WA) asm volatile("v_mfma_f32_16x16x32_bf16 %0, %1, %2, %0" : "+v"(c) : "a"(f), "v"(x)); else asm volatile("v_mfma_f32_16x16x32_bf16 %0, %2, %1, %0" : "+v"(c) : "a"(f), "v"(x)); }
    else { if constexpr (WA) asm volatile("v_mfma_f32_16x16x32_bf16 %0, %1, %2, %0" : "+v"(c) : "v"(f), "v"(x)); else asm volatile("v_mfma_f32_16x16x32_bf16 %0, %2, %1, %0" : "+v"(c) : "v"(f), "v"(x)); }
  }
}

DEV void issue_x(const unsigned char* ws, int seg, unsigned buf, int wave, int lane) {
#pragma unroll
  for (int i = 0; i < 10; ++i) glds_sc1(ws + OFF_XN + (size_t)seg * 40960 + (wave * 10 + i) * 1024, lane * 16, buf + (wave * 10 + i) * 1024);
}
template <int NT, class MID>
DEV void xproj(f32x4 (&acc)[NT][2], const u32x4 (&w)[80], unsigned xa, MID&& mid) {
  u32x4 xr[3][2];
#pragma unroll
  for (int k = 0; k < 2; ++k) { xr[k][0] = ldsr128(xa + k * 2048); xr[k][1] = ldsr128(xa + k * 2048 + 1024); }
  static_for<20>([&](auto K) {
    constexpr int ks = K;
    static_for<2 * NT>([&](auto J) {
      constexpr int j = J, t = j >> 1, wi = ((t >> 1) * 10 + (ks >> 1)) * 2 + (t & 1);
      mf_w<wi, ks & 1, true, ks == 0>(acc[t][j & 1], w, xr[ks % 3][j & 1]);
      if constexpr (ks + 2 < 20 && j == 1) { CFENCE(); xr[(ks + 2) % 3][0] = ldsr128(xa + (ks + 2) * 2048); CFENCE(); }
      if constexpr (ks + 2 < 20 && j == NT / 2 + 1) { CFENCE(); xr[(ks + 2) % 3][1] = ldsr128(xa + (ks + 2) * 2048 + 1024); CFENCE(); }
      if constexpr (j == (NT == 4 ? 5 : 0)) mid(K);
    });
  });
}
template <int NT> DEV void acc_wait(f32x4 (&c)[NT][2]) {
  if constexpr (NT == 4) asm volatile("s_nop 7" : "+v"(c[0][0]), "+v"(c[0][1]), "+v"(c[1][0]), "+v"(c[1][1]), "+v"(c[2][0]), "+v"(c[2][1]), "+v"(c[3][0]), "+v"(c[3][1]));
  else asm volatile("s_nop 7" : "+v"(c[0][0]), "+v"(c[0][1]), "+v"(c[1][0]), "+v"(c[1][1]));
}

template <int KIND>
DEV void role_x(const Args& a, rsrc_t rs, int id, int seg0, int dseg, unsigned po, int wave, int lane, unsigned pol) {
  constexpr int NST = KIND == K_QK ? 4 : KIND == K_F1 ? 2 : 10;
  u32x4 w[80];
  auto late = [&]() {
    if constexpr (KIND != K_F1) {
      int budget = SPIN_CAP;
      for (;;) { const unsigned v = ld32(rs, lane * 128, OFF_CTL + CTL_XP); CFENCE(); if (__all(v == 1) || --budget <= 0) break; }
    }
    VMWAIT(0);
    issue_x(a.ws, seg0, L_XB0, wave, lane);
  };
  if constexpr (KIND == K_QK) load_weights<K_QK>(w, a.wq + id * 256, a.wk + id * 256, wave, lane, 15, late);
  else if constexpr (KIND == K_F1) load_weights<K_F1>(w, a.w1 + (id >> 2) * 256, a.w3 + (id >> 2) * 256, wave, lane, id >= 52 ? 7 : 15, late);
  else load_weights<K_VB>(w, a.wv + id * 256, a.wo + (size_t)id * 128 * 1280, wave, lane, 15, late);
  VMWAIT(0);
  EV(KIND == K_F1 ? 4 : 1);
  const unsigned l16 = lane * 16, fvo = lane == 0 ? po : OOB;
  const unsigned svo = (KIND == K_F1 && id > 53) ? OOB : l16, bvo = l16 + (id >> 3) * 16384 + (id & 7) * 1024;
  int it = 0;
  for (int seg = seg0; seg < NSEG; seg += dseg, ++it) {
    const unsigned buf = (it & 1) ? L_XB1 : L_XB0;
    vmwait<NST>();
    BAR();
    const bool more = seg + dseg < NSEG;
    const unsigned char* xsrc = a.ws + OFF_XN + (size_t)(more ? seg + dseg : seg) * 40960 + wave * 10240;
    const unsigned xdst = L_XB0 + L_XB1 - buf + wave * 10240;
    const unsigned char *xs0 = xsrc, *xs1 = xsrc + 4096, *xs2 = xsrc + 8192;
    PINS(xs0); PINS(xs1); PINS(xs2);
    auto mid = [&](auto K) {
      if constexpr (K % 5 == 0 && K <= 10) set_m0(xdst + (K / 5) * 4096);
      if constexpr (K % 5 != 0 && K <= 12) glds_off<(K % 5 - 1) * 1024>(K < 5 ? xs0 : K < 10 ? xs1 : xs2, l16);
      if constexpr (K == 16) { if (it > 1) { vmwait<10>(); st32(rs, it, fvo, 0); } }
    };
    if constexpr (KIND != K_VB) {
      f32x4 acc[4][2];
      xproj<4>(acc, w, buf + l16, mid);
      acc_wait(acc);
      if constexpr (KIND == K_QK) {
#pragma unroll
        for (int m = 0; m < 2; ++m)
#pragma unroll
          for (int tt = 0; tt < 2; ++tt) st128(rs, cat4(pk4(acc[2 * m][tt]), pk4(acc[2 * m + 1][tt])), l16, kq_addr(1 - m, seg, id, pol) + (tt * 4 + wave) * 1024);
      } else {
#pragma unroll
        for (int tt = 0; tt < 2; ++tt) {
          f32x4 hv[2];
#pragma unroll
          for (int t = 0; t < 2; ++t)
#pragma unroll
            for (int r = 0; r < 4; ++r) hv[t][r] = rbf(silu(rbf(acc[t][tt][r]))) * rbf(acc[2 + t][tt][r]);
          st128(rs, cat4(pk4(hv[0]), pk4(hv[1])), svo, OFF_HH + (seg * 54 + id) * 2048 + tt * 1024);
        }
      }
    } else {
      f32x4 acc[2][2];
      xproj<2>(acc, w, buf + l16, mid);
      acc_wait(acc);
#pragma unroll
      for (int tt = 0; tt < 2; ++tt) ldsw128((wave * 2 + tt) * 1024 + l16, cat4(pk4(acc[0][tt]), pk4(acc[1][tt])));
      BAR();
      u32x4 vf[4][2];
#pragma unroll
      for (int ks = 0; ks < 4; ++ks) { vf[ks][0] = ldsr128((ks * 2) * 1024 + l16); vf[ks][1] = ldsr128((ks * 2 + 1) * 1024 + l16); }
      f32x4 c[3][2];
      static_for<12>([&](auto CT) {
        constexpr int ct = CT;
        u32x4 o;
        static_for<4>([&](auto KS) {
          constexpr int ks = KS, wi = 20 + ((ct >> 1) * 2 + (ks >> 1)) * 2 + (ct & 1);
          if constexpr (ct < 10) { mf_w<wi, ks & 1, false, ks == 0>(c[ct % 3][0], w, vf[ks][0]); mf_w<wi, ks & 1, false, ks == 0>(c[ct % 3][1], w, vf[ks][1]); }
          if constexpr (ct >= 2) {
            constexpr int e = (ct - 2) % 3;
            if constexpr (ks == 0) { if constexpr (ct == 11) pin2n(c[e][0], c[e][1]); else pin2(c[e][0], c[e][1]); }
            o[ks] = pk(c[e][ks >> 1][2 * (ks & 1)], c[e][ks >> 1][2 * (ks & 1) + 1]); pin1(o[ks]);
          }
        });
        if constexpr (ct >= 2) { CFENCE(); st128(rs, o, bvo, b_addr(seg, ((ct - 2) >> 1) * 8 + wave * 2 + (ct & 1), pol)); CFENCE(); }
      });
    }
    if (it == 0) { vmwait<0>(); st32(rs, 1, fvo, 0); EV(KIND == K_F1 ? 5 : 2); }
  }
  VMWAIT(0);
  st32(rs, it, fvo, 0);
  EV(KIND == K_F1 ? 6 : 3);
  BAR();
}

#define REPL 4
DEV unsigned slot_base(int s, unsigned pol) { return OFF_COMM + (s & 3) * MB + ((pol >> ((s >> 1) & 1)) & 1) * 8192; }
DEV int wmax(int v) {
  v = max(v, __builtin_amdgcn_update_dpp(v, v, 0xB1, 0xf, 0xf, false));
  v = max(v, __builtin_amdgcn_update_dpp(v, v, 0x4E, 0xf, 0xf, false));
  v = max(v, __builtin_amdgcn_update_dpp(v, v, 0x124, 0xf, 0xf, false));
  v = max(v, __builtin_amdgcn_update_dpp(v, v, 0x128, 0xf, 0xf, false));
  return max(max(__builtin_amdgcn_readlane(v, 0), __builtin_amdgcn_readlane(v, 16)), max(__builtin_amdgcn_readlane(v, 32), __builtin_amdgcn_readlane(v, 48)));
}
DEV int wmin(int v) { return -wmax(-v); }
DEV void report(rsrc_t rs, int e, int s, unsigned flags, int lane) {
  unsigned long long v, c;
  asm volatile("s_memrealtime %0\n\ts_memtime %1\n\ts_waitcnt lgkmcnt(0)" : "=s"(v), "=s"(c) :: "memory");
  const u32x2 tm = *(volatile LAS u32x2*)(uintptr_t)(L_TM + (s & 1) * 16);
  st128(rs, (u32x4){(unsigned)s + 1, (unsigned)v << 3, (((unsigned)c - tm.x) << 2) | flags | tm.y, 0}, lane < 8 ? lane * 16 : OOB, OFF_REP + (s * 128 + e) * 128);
}
DEV void sleep_until_turn(rsrc_t rs, Sched& c, int j, int off) {
  for (;;) {
    adopt_cur(c, ld128(rs, 0, OFF_CTL + CTL_CUR), j + 1); CFENCE();
    const int d = (int)(tau_of(c, j) + off - now8());
    if (c.j0 == 0 || d > 200 * MAXWAIT) return;
    if (d < 60 * 8) { wait_until(tau_of(c, j) + off); return; }
    __builtin_amdgcn_s_sleep(12);
  }
}
DEV int probe_seg(int s, int lane) { return min(s + (3 << (lane & 3)), NSEG - 1); }
DEV unsigned probe_ld(rsrc_t rs, int s, int lane) { return ld32(rs, lane < 4 ? probe_seg(s, lane) * 128 : OOB, OFF_CTL + CTL_RL); }
DEV int probe_rdy(unsigned v, int s, int lane) { return wmax(v != 0 ? probe_seg(s, lane) + 1 : 0); }
DEV void role_agg(rsrc_t rs, int lane) {
  int done = 0, budget = SPIN_CAP;
  while (done < NSEG && --budget > 0) {
    int m = NSEG;
#pragma unroll
    for (int i = 0; i < 8; ++i) m = min(m, 2 * (int)ld32(rs, (i * 64 + lane) * 128, OFF_CTL) + ((lane >> 2) & 1));
    CFENCE();
    m = min(wmin(m), NSEG);
    if (m > done) { st32(rs, 1, (lane >= done && lane < m) ? lane * 128 : OOB, OFF_CTL + CTL_RL); done = m; }
    __builtin_amdgcn_s_sleep(16);
  }
}
DEV unsigned rep_off(int j, int lane) { return ((j >> 1) * 128 + ((j & 1) ? 64 + min(lane, 39) : lane)) * 128; }
#define PTSLEEP 2
#define HOLDP 720
#define HOLDC 1080
#define NOPM_(n) asm volatile("s_nop " #n ::: "memory")
#define NOPM(n) NOPM_(n)
#define VSLACK 48
#define PACE 11
#define NOPV(n, x) asm volatile("s_nop " #n : "+v"(x))
template <int N> DEV void zero_acc(f32x4 (&c)[N]) {
#pragma unroll
  for (int t = 0; t < N; ++t) { c[t] = (f32x4){0, 0, 0, 0}; PIN(c[t]); }
}
DEV unsigned memoff(int j) { return j < 8 ? j * 1024 : C_MEM1 + (j - 8) * 1024; }
DEV bool has_nan(f32x4 v) { return __any(__builtin_isunordered(v[0], v[1]) || __builtin_isunordered(v[2], v[3])); }
template <int N> DEV bool has_sent(const u32x4 (&a)[N]) {
  unsigned m = 0;
#pragma unroll
  for (int k = 0; k < N; ++k) m = max(m, mx4(a[k]));
  return __any(m == SENT);
}
DEV int mrow(int i, int half) { return 4 * (i >> 1) + 2 * half + (i & 1); }
DEV unsigned foldpk(f32x4 v) { return pk(v[0] + v[2], v[1] + v[3]); }
DEV void ldsw32(unsigned a, unsigned v) { *(LAS unsigned*)(uintptr_t)a = v; }
DEV void mem_wait(rsrc_t rs, unsigned sb, int lane, int& budget) {
  for (;;) {
    const unsigned u = ld32(rs, lane * 128, sb), v = ld32(rs, C_MEM1 + (lane & 15) * 128, sb);
    CFENCE();
    if (__all(u != SENT && v != SENT) || --budget <= 0) break;
  }
}
template <int NT, int NK>
DEV void project(rsrc_t rs, const unsigned (&vo)[10], unsigned sb, const u32x8 (&w)[40], int idx, u32x4 (&am)[10], f32x4 (&acc)[NT]) {
#pragma unroll
  for (int k = 0; k < 8; ++k) {
    am[k] = ld128(rs, vo[k], sb);
    if (k < 7) NOPM(PACE); else CFENCE();
  }
  static_for<NK>([&](auto K) {
    constexpr int ks = K;
    smf_w<ks * 2>(acc[0], am[ks], w, idx); smf_w<ks * 2 + 1>(acc[1], am[ks], w, idx);
    if constexpr (ks < 2) { CFENCE(); am[8 + ks] = ld128(rs, vo[8 + ks], sb); CFENCE(); }
    if constexpr (NT == 4) { smf_w<20 + ks * 2>(acc[2], am[ks], w, idx); smf_w<20 + ks * 2 + 1>(acc[3], am[ks], w, idx); }
  });
}
#define DPL " row_ror:8 row_mask:0xf bank_mask:0x3\n\t"
#define DPH " row_ror:8 row_mask:0xf bank_mask:0xc\n\t"
#define DPA " quad_perm:[2,3,0,1] row_mask:0xf bank_mask:0xf\n\t"
#define FADD2(d0, d1, c) asm volatile("v_add_f32 %0, %2, %4\n\tv_add_f32 %1, %3, %5" : "=&v"(d0), "=&v"(d1) : "v"(c[0]), "v"(c[1]), "v"(c[2]), "v"(c[3]))

constexpr unsigned P_RING = 16384;
#define ROWRED(op) "s_nop 1\n\t" op "_dpp %7, %6, %6" DPA op "_dpp %8, %6, %6" DPA "s_nop 1\n\tv_permlane16_swap_b32 %7, %8\n\t" op " %6, %7, %8\n\t" op " %7, %7, %8\n\t" \
                   "s_nop 1\n\tv_permlane32_swap_b32 %6, %7\n\t" op " %6, %6, %7\n\t"
#define SM_SELECT "v_cndmask_b32 %4, %24, %13, %26\n\tv_cndmask_b32 %5, %25, %14, %26\n\ts_nop 1\n\t" \
                  "v_cndmask_b32 %0, %20, %15, %26\n\tv_cndmask_b32 %1, %21, %16, %26\n\tv_cndmask_b32 %2, %22, %17, %26\n\tv_cndmask_b32 %3, %23, %18, %26\n\t"
#define SM_EXP "v_fma_f32 %4, %4, %19, -%12\n\tv_fma_f32 %5, %5, %19, -%12\n\tv_exp_f32 %4, %4\n\tv_fma_f32 %0, %0, %19, -%12\n\tv_exp_f32 %5, %5\n\t" \
               "v_fma_f32 %1, %1, %19, -%12\n\tv_exp_f32 %0, %0\n\tv_fma_f32 %2, %2, %19, -%12\n\tv_exp_f32 %1, %1\n\tv_fma_f32 %3, %3, %19, -%12\n\tv_exp_f32 %2, %2\n\t" \
               "v_add_f32 %6, %4, %5\n\tv_exp_f32 %3, %3\n\tv_add_f32 %7, %0, %1\n\tv_add_f32 %6, %6, %2\n\tv_add_f32 %7, %7, %3\n\tv_add_f32 %6, %6, %7\n\t"
#define SM_OUT "v_rcp_f32 %7, %6\n\tv_med3_f32 %8, %6, %27, %28\n\t" \
               "v_mul_f32 %0, %0, %7\n\tv_mul_f32 %1, %1, %7\n\tv_mul_f32 %2, %2, %7\n\tv_mul_f32 %3, %3, %7\n\t" \
               "v_cvt_pk_bf16_f32 v248, |%0|, |%1|\n\tv_cvt_pk_bf16_f32 v249, |%2|, |%3|\n\tv_mul_f32 %4, %4, %7\n\tv_mul_f32 %5, %5, %7\n\t" \
               "v_permlane32_swap_b32 v248, v250\n\tv_cvt_pk_bf16_f32 v252, |%4|, |%5|\n\tv_permlane32_swap_b32 v249, v251\n\tv_cmp_eq_f32 %11, %8, %6\n\tv_permlane16_swap_b32 v252, v253"
#define SM_OPS : "=&v"(x0), "=&v"(x1), "=&v"(x2), "=&v"(x3), "=&v"(y0), "=&v"(y1), "=&v"(sum), "=&v"(t1), "=&v"(t2), "=&{v[248:251]}"(ox), "=&{v[252:253]}"(om), "=&s"(okm), "+v"(mref) \
               : "v"(s0[2]), "v"(s0[3]), "v"(s2[0]), "v"(s2[1]), "v"(s2[2]), "v"(s2[3]), "s"(cs), "v"(s1[0]), "v"(s1[1]), "v"(s1[2]), "v"(s1[3]), "v"(s0[0]), "v"(s0[1]), \
                 "s"(0xCCCCCCCCCCCCCCCCull), "v"(lo), "v"(hi)
template <int CLS>
DEV void p_loop(const Args& a, rsrc_t rs, int h, int wave, int lane, unsigned pol, const u32x8 (&w)[40], Sched& sc) {
  const int n = lane & 15, g = lane >> 4;
  const int idx = (lane & 2) ? 0xEEEE : 0x4444;
  const float NEG = -3.0e38f;
  const unsigned l16 = lane * 16, cpy = (h & (REPL - 1)) * 65536;
  unsigned vo[10];
#pragma unroll
  for (int k = 0; k < 10; ++k) { vo[k] = l16 + memoff(k); PIN(vo[k]); }
  f32x4 i1, i2;
#pragma unroll
  for (int r = 0; r < 4; ++r) {
    const bool tri = (4 * g + r) <= n;
    i1[r] = (wave == 1 && !tri) ? NEG : 0.f;
    i2[r] = (wave == 1 || (wave == 2 && !tri)) ? NEG : 0.f;
  }
  unsigned kvo[2];
#pragma unroll
  for (int tl = 0; tl < 2; ++tl) {
    int tok = 16 * tl + n;
    if (CLS == 0) { const int gp = n >> 2, r = n & 3; tok = 16 * (r >> 1) + 8 * (gp & 1) + 4 * (gp >> 1) + 2 * tl + (r & 1); }
    kvo[tl] = P_RING + (tok >> 4) * 4096 + (g * 16 + (tok & 15)) * 16;
  }
  const unsigned qvo = P_RING + 8192 + (wave == 2 ? 4096 : 0) + l16;
  unsigned wra = (32 * wave + n) * 16 + g * 4, wrq = 2 * wra;
  unsigned tra = (4 * g + ((lane >> 2) & 3)) * 16 + (lane & 1) * 8, trq = (4 * g + ((lane >> 2) & 3)) * 32 + (lane & 3) * 8;
  PIN(wra); PIN(wrq); PIN(tra); PIN(trq);
  unsigned vox[REPL], vom[REPL];
#pragma unroll
  for (int c = 0; c < REPL; ++c) {
    if (CLS == 0) { vox[c] = lane < 32 ? c * 65536 + C_PX + (h >> 4) * 16384 + (h & 15) * 512 + l16 : OOB; vom[c] = lane < 16 ? c * 65536 + C_PM + h * 128 + lane * 8 : OOB; PIN(vox[c]); PIN(vom[c]); }
    else { vox[c] = (h * 2 + (wave - 1)) * 1024 + l16; vom[c] = lane < 16 ? 65536 + (wave - 1) * 8192 + h * 256 + l16 : OOB; }
  }
  const unsigned pvo = prog_off(0, h, (lane >> 2) & 1, lane & 3);
  const float cs = 0.08838834764831845f * 1.4426950408889634f;
  float mref = 0.f, lo = 0x1p-64f, hi = 0x1p64f;
  PIN(lo); PIN(hi);
  int budget = SPIN_CAP, rdy = 0;
  auto fetch = [&](int seg) {
    while (rdy <= seg && --budget > 0) { rdy = wmin(2 * (int)ld32(rs, pvo, 0) + ((lane >> 2) & 1)); CFENCE(); }
    static_for<4>([&](auto J) {
      const unsigned char* src = a.ws + kq_addr(J >> 1, seg, h, pol) + (J & 1) * 4096;
      set_m0(P_RING + (seg & 1) * 16384 + J * 4096); NOP(0);
      glds_off<0>(src, l16); glds_off<1024>(src, l16); glds_off<2048>(src, l16); glds_off<3072>(src, l16);
    });
  };
  u32x4 kx[2][4], qf[4];
  auto pull = [&](int seg) {
    const unsigned rb = (seg & 1) * 16384;
#pragma unroll
    for (int ks = 0; ks < 4; ++ks) { kx[0][ks] = ldsr128(kvo[0] + rb + ks * 1024); kx[1][ks] = ldsr128(kvo[1] + rb + ks * 1024); if (CLS == 1) qf[ks] = ldsr128(qvo + rb + ks * 1024); }
  };
  if (CLS == 2) { fetch(0); VMWAIT(0); }
  BAR();
  EV(2);
  if (CLS == 2) fetch(1); else pull(0);
  unsigned hold = now8(), pend = 0;
  for (int s = 0; s < NSEG; ++s) {
    unsigned sb = slot_base(s, pol), sbr = sb + cpy;
    const float n0 = g >= 2 ? NEG : 0.f;
    f32x4 s0 = {n0, n0, n0, n0}, s1 = i1, s2 = i2;
    PINS(sb); PINS(sbr);
    if (CLS < 2) {
      PIN(s0); PIN(s1); PIN(s2);
#pragma unroll
      for (int ks = 0; ks < 4; ++ks) { PIN(kx[0][ks]); PIN(kx[1][ks]); }
    }
    if (CLS == 1) {
      NOP(1);
#pragma unroll
      for (int ks = 0; ks < 4; ++ks) { mf_vv(s1, kx[0][ks], qf[ks]); mf_vv(s2, kx[1][ks], qf[ks]); }
    }
    unsigned cst = 0;
    if (CLS == 2) cst = trigger(rs, sc, 2 * s, hold, [&]() { mem_wait(rs, sbr, lane, budget); });
    f32x4 acc[4];
    zero_acc(acc);
    STAMP_DECL;
    unsigned good = 2;
    if (CLS == 2) release(cst); else BAR();
    STAMP(0);
    for (;;) {
      u32x4 am[10];
      project<4, 8>(rs, vo, sbr, w, idx, am, acc);
      float a0, a1, b0, b1;
      smf_w<16>(acc[0], am[8], w, idx); smf_w<17>(acc[1], am[8], w, idx); smf_w<18>(acc[0], am[9], w, idx); smf_w<19>(acc[1], am[9], w, idx);
      smf_w<36>(acc[2], am[8], w, idx); smf_w<37>(acc[3], am[8], w, idx); smf_w<38>(acc[2], am[9], w, idx);
      FADD2(a0, a1, acc[0]);
      smf_w<39>(acc[3], am[9], w, idx);
      FADD2(b0, b1, acc[1]);
      STAMP(1);
      asm volatile("ds_write2_b32 %0, %1, %1 offset0:0 offset1:1" :: "v"(wrq), "v"(pk(a0, a1)) : "memory");
      asm volatile("ds_write2_b32 %0, %1, %1 offset0:128 offset1:129" :: "v"(wrq), "v"(pk(b0, b1)) : "memory");
      const bool bad = __any(__builtin_isunordered(a0, a1));
      PIN(acc[2]);
      const unsigned f2 = foldpk(acc[2]);
      pin1(f2);
      PIN(acc[3]);
      ldsw32(wra + 4096, f2); ldsw32(wra + 4096 + 256, foldpk(acc[3]));
      if (__builtin_expect(!bad, 1)) break;
      if ((!(good & 2) && !has_sent(am)) || --budget <= 0) break;
      good = 0;
      zero_acc(acc);
      mem_wait(rs, sbr, lane, budget);
    }
    BAR();
    STAMP(2);
    u32x4 km[4];
#define TRQ(ks) qf[ks] = cat4(trread(trq + (ks) * 1024), trread(trq + (ks) * 1024 + 512))
#define TRK(ks) km[ks] = cat4(trread(tra + 4096 + (ks) * 512), trread(tra + 4096 + (ks) * 512 + 256))
    if (CLS == 0) {
      TRQ(0); TRQ(1); TRK(0); TRK(1); CFENCE();
      mf_vv(s1, kx[0][0], qf[0]); CFENCE(); TRQ(2); CFENCE(); mf_vv(s2, kx[1][0], qf[0]); CFENCE(); TRK(2); CFENCE();
      mf_vv(s1, kx[0][1], qf[1]); CFENCE(); TRQ(3); CFENCE(); mf_vv(s2, kx[1][1], qf[1]); CFENCE(); TRK(3); CFENCE();
      mf_vv(s0, km[0], qf[0]); mf_vv(s1, kx[0][2], qf[2]); mf_vv(s0, km[1], qf[1]); mf_vv(s2, kx[1][2], qf[2]);
      mf_vv(s0, km[2], qf[2]); mf_vv(s1, kx[0][3], qf[3]); mf_vv(s0, km[3], qf[3]); mf_vv(s2, kx[1][3], qf[3]);
      STAMP(3);
      float x0, x1, x2, x3, y0, y1, sum, t1, t2;
      u32x4 ox; u32x2 om; unsigned long long okm;
      asm volatile("s_nop 5\n\t" SM_SELECT SM_EXP ROWRED("v_add_f32") SM_OUT SM_OPS);
      if (__builtin_expect(okm != ~0ull, 0))
        asm volatile(SM_SELECT "v_max3_f32 %6, %4, %5, %0\n\tv_max3_f32 %7, %1, %2, %3\n\tv_max_f32 %6, %6, %7\n\t" ROWRED("v_max_f32") "v_mul_f32 %12, %19, %6\n\t"
                     SM_EXP ROWRED("v_add_f32") SM_OUT SM_OPS);
#pragma unroll
      for (int c = 0; c < REPL; ++c) st128(rs, ox, vox[c], sb);
#pragma unroll
      for (int c = 0; c < REPL; ++c) st64(rs, om, vom[c], sb);
      CFENCE();
      STAMP(4);
      STAMP_OUT(h, s);
      report(rs, h, s, good, lane);
      mref += __builtin_amdgcn_logf(sum);
      if (s >= 1) {
        const unsigned rb = slot_base(s - 1, pol);
#pragma unroll
        for (int c = 0; c < REPL; ++c) { st128(rs, (u32x4){SENT, SENT, SENT, SENT}, vox[c], rb); st64(rs, (u32x2){SENT, SENT}, vom[c], rb); }
      }
      if (s + 1 < NSEG) pull(s + 1);
    } else if (CLS == 1) {
      TRK(0); TRK(1); TRK(2); TRK(3);
#pragma unroll
      for (int ks = 0; ks < 4; ++ks) mf_vv(s0, km[ks], qf[ks]);
      asm volatile("s_nop 7" : "+v"(s0), "+v"(s1), "+v"(s2));
      float mxv = fmaxf(fmaxf(fmaxf(s1[0], s1[1]), fmaxf(s1[2], s1[3])), fmaxf(fmaxf(s2[0], s2[1]), fmaxf(s2[2], s2[3])));
      mxv = fmaxf(fmaxf(fmaxf(s0[0], s0[1]), fmaxf(s0[2], s0[3])), mxv);
      mxv = xmax32(xmax16(mxv));
      const float mb = -mxv * cs;
      float sum = 0.f;
#pragma unroll
      for (int r = 0; r < 4; ++r) {
        s0[r] = __builtin_amdgcn_exp2f(fmaf(s0[r], cs, mb)); s1[r] = __builtin_amdgcn_exp2f(fmaf(s1[r], cs, mb)); s2[r] = __builtin_amdgcn_exp2f(fmaf(s2[r], cs, mb));
        sum += s0[r] + s1[r] + s2[r];
      }
      sum = xadd32(xadd16(sum));
      const float inv = __builtin_amdgcn_rcpf(sum);
      s0 *= inv; s1 *= inv; s2 *= inv;
      const unsigned so = OFF_PT + s * 81920;
      const u32x2 pmv = pk4(s0);
      auto r0 = __builtin_amdgcn_permlane16_swap(pmv.x, pmv.x, false, false);
      auto r1 = __builtin_amdgcn_permlane16_swap(pmv.y, pmv.y, false, false);
      const u32x4 ptx = cat4(pk4(s1), pk4(s2));
      if (s + 1 < NSEG) { pull(s + 1); __builtin_amdgcn_s_sleep(PTSLEEP); }
      st128(rs, ptx, vox[0], so);
      st128(rs, (u32x4){(unsigned)r0[0], (unsigned)r1[0], (unsigned)r0[1], (unsigned)r1[1]}, vom[0], so);
      STAMP(4);
      STAMP_OUT(h, s);
    } else {
      const unsigned tend = now8() + 24 * 8;
      mark_overrun(sc, 2 * s, tend);
      const u32x4 cur = ld128(rs, 0, OFF_CTL + CTL_CUR);
      rdy = max(rdy, probe_rdy(pend, s, lane));
      adopt_cur(sc, cur, 2 * s + 2);
      if (s + 2 < NSEG) fetch(s + 2);
      pend = 0;
      if (rdy <= s + 5) pend = probe_ld(rs, s + 1, lane);
      STAMP_OUT(h, s);
      hold = tend + HOLDP; clk_update(sc);
    }
  }
  VMWAIT(0);
  BAR();
  if (CLS == 2) st128(rs, (u32x4){1, 1, 1, 1}, lane < 8 ? lane * 16 : OOB, OFF_REP + (NSEG * 128 + h) * 128);
  EV(3);
}
DEV void role_p(const Args& a, rsrc_t rs, int h, int wave, int lane, unsigned pol) {
  u32x8 w[40];
  Sched sc; clk_init(sc);
  load_weights<K_P>(w, a.wq + h * 256, a.wk + h * 256, wave, lane, 15, []() { VMWAIT(0); });
  BAR();
  EV(1);
  if (wave == 0) p_loop<0>(a, rs, h, wave, lane, pol, w, sc);
  else if (wave < 3) p_loop<1>(a, rs, h, wave, lane, pol, w, sc);
  else p_loop<2>(a, rs, h, wave, lane, pol, w, sc);
}

DEV void role_v(const Args& a, rsrc_t rs, int h, int wave, int lane, unsigned pol) {
  u32x8 w[40];
  Sched sc; clk_init(sc);
  load_weights<K_V>(w, a.wv + h * 256, a.wo + (size_t)h * 128 * 1280, wave, lane, 15, []() { VMWAIT(0); });
  BAR();
  EV(1);
  const int n = lane & 15, g = lane >> 4;
  const int idx = (lane & 2) ? 0xEEEE : 0x4444;
  unsigned wra = (16 * wave + (n >> 2) * 2 + (n & 1)) * 32 + g * 8 + ((n >> 1) & 1) * 4, tra = (8 * g + ((lane >> 2) & 3)) * 32 + (lane & 3) * 8;
  unsigned zv0 = g < 2 ? (wave * 2 + g) * 16384 + h * 256 + n * 16 : OOB;
  unsigned zv1 = (8 + (g >> 1) * 8 + wave * 2 + (g & 1)) * 16384 + h * 256 + n * 16, zv2 = zv1 + 16 * 16384;
  PIN(wra); PIN(tra); PIN(zv0); PIN(zv1); PIN(zv2);
  unsigned vo[10];
#pragma unroll
  for (int k = 0; k < 10; ++k) { vo[k] = lane * 16 + memoff(k); PIN(vo[k]); }
  int budget = SPIN_CAP;
  unsigned hold = now8();
  for (int s = 0; s < NSEG; ++s) {
    unsigned sb = slot_base(s, pol) + (h & (REPL - 1)) * 65536, zb = z_base(s, pol);
    PINS(sb); PINS(zb);
    f32x4 c[10], acc[2];
    zero_acc(c); zero_acc(acc);
    unsigned cst;
    if (wave == 3) cst = trigger(rs, sc, 2 * s, hold, [&]() { mem_wait(rs, sb, lane, budget); }); else cst = cycles();
    STAMP_DECL;
    unsigned good = 2;
    release(__builtin_amdgcn_readfirstlane(cst));
    STAMP(0);
    for (;;) {
      u32x4 am[10];
      project<2, 10>(rs, vo, sb, w, idx, am, acc);
      STAMP(1);
      asm volatile("s_nop 7" : "+v"(acc[0]), "+v"(acc[1]));
      ldsw32(wra, foldpk(acc[0])); ldsw32(wra + 256, foldpk(acc[1]));
      if (__builtin_expect(!has_nan(acc[0]), 1)) break;
      if ((!(good & 2) && !has_sent(am)) || --budget <= 0) break;
      good = 0;
      zero_acc(acc);
      mem_wait(rs, sb, lane, budget);
    }
    BAR();
    STAMP(2);
    u32x4 va[2];
#pragma unroll
    for (int ks = 0; ks < 2; ++ks) va[ks] = cat4(trread(tra + ks * 1024), trread(tra + ks * 1024 + 128));
    CFENCE(); const u32x4 cur = ld128(rs, 0, OFF_CTL + CTL_CUR); CFENCE();
    u32x4 O0, O1, O2;
#define VMM(t, k) smf_w<20 + ((t) >> 1) * 4 + 2 * (k) + ((t) & 1)>(c[t], va[k], w, idx)
#define VEA(t, T) FADD2(T##0, T##1, c[t])
#define VEC2(R, ra, rb, O) asm volatile("v_cvt_pk_bf16_f32 " ra ", %1, %2\n\tv_cvt_pk_bf16_f32 " rb ", %3, %4" : "+{" R "}"(O) : "v"(ta0), "v"(ta1), "v"(tb0), "v"(tb1))
#define VSW(txt, R, O) asm volatile(txt : "+{" R "}"(O))
    float ta0, ta1, tb0, tb1;
    asm volatile("" : "={v[232:235]}"(O0), "={v[236:239]}"(O1), "={v[240:243]}"(O2));
    VMM(0, 0); VMM(1, 0); VMM(0, 1); VMM(1, 1);
    VMM(2, 0); VMM(3, 0); VMM(2, 1); VMM(3, 1);
    VMM(4, 0); NOP(1); VMM(5, 0); VEA(0, ta); VMM(4, 1); VEA(1, tb); VMM(5, 1); VEC2("v[232:235]", "v232", "v233", O0);
    VMM(6, 0); VEA(2, ta); VMM(7, 0); VEA(3, tb); VMM(6, 1); VSW("v_permlane16_swap_b32 v232, v233", "v[232:235]", O0); VMM(7, 1); VEC2("v[236:239]", "v236", "v237", O1);
    VMM(8, 0); VSW("v_permlane32_swap_b32 v232, v234", "v[232:235]", O0); VMM(9, 0); VSW("v_permlane32_swap_b32 v233, v235", "v[232:235]", O0);
    VMM(8, 1); VEA(4, ta); VMM(9, 1); CFENCE(); st128(rs, O0, zv0, zb); CFENCE(); VEA(5, tb);
    VEC2("v[236:239]", "v238", "v239", O1);
    VEA(6, ta); VEA(7, tb);
    VSW("v_permlane32_swap_b32 v236, v238\n\tv_permlane32_swap_b32 v237, v239", "v[236:239]", O1);
    VEC2("v[240:243]", "v240", "v241", O2);
    VSW("v_permlane16_swap_b32 v236, v237\n\tv_permlane16_swap_b32 v238, v239", "v[236:239]", O1);
    CFENCE(); st128(rs, O1, zv1, zb); CFENCE();
    VEA(8, ta); VEA(9, tb); VEC2("v[240:243]", "v242", "v243", O2);
    VSW("s_nop 0\n\tv_permlane32_swap_b32 v240, v242\n\tv_permlane32_swap_b32 v241, v243\n\ts_nop 1\n\tv_permlane16_swap_b32 v240, v241\n\tv_permlane16_swap_b32 v242, v243", "v[240:243]", O2);
    CFENCE(); st128(rs, O2, zv2, zb);
    CFENCE();
    STAMP(4);
    STAMP_OUT(32 + h, s);
    if (wave == 0) report(rs, 32 + h, s, good, lane);
    if (wave == 3) { const unsigned tend = now8(); hold = tend + HOLDP; mark_overrun(sc, 2 * s, tend - VSLACK); clk_update(sc); adopt_cur(sc, cur, 2 * s + 2); }
  }
  VMWAIT(0);
  BAR();
  if (wave == 0) st128(rs, (u32x4){1, 1, 1, 1}, lane < 8 ? lane * 16 : OOB, OFF_REP + (NSEG * 128 + 32 + h) * 128);
}

#define CLOB_A "a0", "a1", "a2", "a3", "a4", "a5", "a6", "a7", "a8", "a9", "a10", "a11", "a12", "a13", "a14", "a15", "a16", "a17", "a18", "a19", "a20", "a21", "a22", "a23", "a24", "a25", "a26", "a27", "a28", "a29", "a30", "a31", "a32", "a33", "a34", "a35", "a36", "a37", "a38", "a39", "a40", "a41", "a42", "a43", "a44", "a45", "a46", "a47", "a48", "a49", "a50", "a51", "a52", "a53", "a54", "a55", "a56", "a57", "a58", "a59", "a60", "a61", "a62", "a63", "a64", "a65", "a66", "a67", "a68", "a69", "a70", "a71", "a72", "a73", "a74", "a75", "a76", "a77", "a78", "a79", "a80", "a81", "a82", "a83", "a84", "a85", "a86", "a87", "a88", "a89", "a90", "a91", "a92", "a93", "a94", "a95", "a96", "a97", "a98", "a99", "a100", "a101", "a102", "a103", "a104", "a105", "a106", "a107", "a108", "a109", "a110", "a111", "a112", "a113", "a114", "a115", "a116", "a117", "a118", "a119", "a120", "a121", "a122", "a123", "a124", "a125", "a126", "a127"
template <int J> DEV void c_ldb(unsigned l16, const unsigned char* src) {
  asm volatile("global_load_dwordx4 a[%c2:%c2+3], %0, %1 sc1\n\tglobal_load_dwordx4 a[%c2+4:%c2+7], %0, %1 offset:1024 sc1\n\t"
               "global_load_dwordx4 a[%c2+8:%c2+11], %0, %1 offset:2048 sc1\n\tglobal_load_dwordx4 a[%c2+12:%c2+15], %0, %1 offset:3072 sc1"
               :: "v"(l16), "s"(src + (J >> 1) * 16384 + (J & 1) * 4096), "n"(J * 16) : "memory", CLOB_A);
}
template <int J> DEV void c_smf(f32x4& acc, u32x4 p, int idx) { asm volatile("v_smfmac_f32_16x16x64_bf16 %0, %1, a[%c3:%c3+7], %2" : "+{v[248:251]}"(acc) : "v"(p), "v"(idx), "n"(J * 8)); }
#define BDELAY 30
DEV void c_arm(rsrc_t rs, int s, int cb, int lane, unsigned pol) {
  const unsigned zn = z_base(s, pol) + cb * 16384;
#pragma unroll
  for (int j = 0; j < 8; ++j) st128(rs, (u32x4){SENT, SENT, SENT, SENT}, lane * 16 + (j & 3) * 1024, zn + (j >> 2) * 4096);
}
DEV void role_c(const Args& a, rsrc_t rs, int cb, int wave, int lane, unsigned pol) {
  Sched sc; clk_init(sc);
  c_arm(rs, wave, cb, lane, pol);
  VMWAIT(0);
  BAR();
  const unsigned pvo = prog_off(1, lane >> 1, lane & 1, (cb >> 1) & 3);
  if (wave == 3) {
    int rdy = 0;
    for (int s = 0; s < NSEG - 1; ++s) {
      BAR(); BAR();
      unsigned v = 0;
      if (rdy <= s + 4) v = probe_ld(rs, s, lane);
      BAR();
      rdy = max(rdy, probe_rdy(v, s, lane));
      ldsw32(L_TM + 32, rdy);
    }
    return;
  }
  if (wave == 2) {
    const unsigned rvo = (lane >> 4) < REPL ? (lane >> 4) * 65536 + (cb < 32 ? cb * 256 : C_MEM1 + (cb - 32) * 256) + (lane & 15) * 16 : OOB;
    for (int s = 0; s < NSEG - 1; ++s) {
      BAR(); BAR(); VMWAIT(0); BAR();
      st128(rs, (u32x4){SENT, SENT, SENT, SENT}, rvo, slot_base(s, pol));
      if (s + 4 < NSEG) c_arm(rs, s + 4, cb, lane, pol);
    }
    return;
  }
  int budget = SPIN_CAP;
  const unsigned cpy = ((cb / 10) & (REPL - 1)) * 65536;
  auto c_wait = [&](unsigned sb, unsigned zb) {
    for (;;) {
      const unsigned u = ld32(rs, C_PM + (lane & 31) * 128, sb), v = ld32(rs, cb * 16384 + lane * 128, zb);
      CFENCE();
      if (__all(u != SENT && v != SENT) || --budget <= 0) break;
    }
  };
  if (wave == 1) {
    unsigned hold = now8();
    for (int s = 0; s < NSEG - 1; ++s) {
      release(trigger(rs, sc, 2 * s + 1, hold, [&]() { c_wait(slot_base(s, pol) + cpy, z_base(s, pol)); }));
      clk_update(sc);
      BAR();
      hold = now8();
      mark_overrun(sc, 2 * s + 1, hold);
      hold += HOLDC;
      adopt_cur(sc, ld128(rs, 0, OFF_CTL + CTL_CUR), 2 * s + 3);
      if (sc.j0 > sc.jbad) wait_until(tau_of(sc, 2 * s + 2) + BDELAY * 8);
      BAR();
    }
    return;
  }
  const int n = lane & 15, g = lane >> 4;
  const int idx = (lane & 2) ? 0xEEEE : 0x4444;
  const unsigned l16 = lane * 16;
  unsigned pxvo[4], zvo[2], pmvo = C_PM + 2 * g * 128 + n * 8;
#pragma unroll
  for (int i = 0; i < 4; ++i) { pxvo[i] = C_PX + (i >> 1) * 16384 + (i & 1) * 4096 + l16; PIN(pxvo[i]); }
#pragma unroll
  for (int i = 0; i < 2; ++i) { zvo[i] = cb * 16384 + i * 4096 + l16; PIN(zvo[i]); }
  const unsigned mvo = (cb < 32 ? cb * 256 : C_MEM1 + (cb - 32) * 256) + mrow(2 * g + (n >> 3), (n & 7) >> 2) * 16 + (n & 3) * 4;
  unsigned oob = OOB; PIN(oob); PIN(pmvo);
  {
    const unsigned short* m0 = (const unsigned short*)a.mem0 + (2 * g + (n >> 3)) * 640 + cb * 16 + (n & 7);
    const unsigned v = min(m0[0] | (unsigned)m0[8] << 16, 0xFFFFFFFEu);
#pragma unroll
    for (int c = 0; c < REPL; ++c) st32(rs, v, mvo, slot_base(0, pol) + c * 65536);
  }
  int rdy = 0;
  auto fetch = [&](int seg) {
    while (rdy <= seg && --budget > 0) { rdy = wmin(2 * (int)ld32(rs, pvo, 0) + (lane & 1)); CFENCE(); }
    const unsigned char* src = a.ws + b_addr(seg, cb, pol);
    static_for<8>([&](auto J) { c_ldb<J>(l16, src); });
  };
  fetch(0);
  for (int s = 0; s < NSEG - 1; ++s) {
    unsigned sb = slot_base(s, pol) + cpy, zb = z_base(s, pol), sbn[REPL];
#pragma unroll
    for (int c = 0; c < REPL; ++c) { sbn[c] = slot_base(s + 1, pol) + c * 65536; PINS(sbn[c]); }
    PINS(sb); PINS(zb);
    STAMP_DECL;
    unsigned good = 2;
    BAR();
    STAMP(0);
    for (;;) {
      u32x4 z[8], pm[4], px[16];
#pragma unroll
      for (int j = 0; j < 16; ++j) px[j] = ld128(rs, pxvo[j >> 2] + (j & 3) * 1024, sb);
      CFENCE();
#pragma unroll
      for (int j = 0; j < 4; ++j) pm[j] = cat4(ld64(rs, pmvo + j * 1024, sb), ld64(rs, pmvo + j * 1024 + 128, sb));
      CFENCE();
#pragma unroll
      for (int j = 0; j < 8; ++j) { z[j] = ld128(rs, zvo[j >> 2] + (j & 3) * 1024, zb); CFENCE(); }
      f32x4 acc = {0, 0, 0, 0};
      asm volatile("s_nop 1" : "+{v[248:251]}"(acc));
      static_for<16>([&](auto J) { c_smf<J>(acc, px[J], idx); });
#pragma unroll
      for (int j = 0; j < 4; ++j) asm volatile("v_smfmac_f32_16x16x64_bf16 %0, %1, %2, %3" : "+{v[248:251]}"(acc) : "v"(pm[j]), "v"(cat8(z[2 * j], z[2 * j + 1])), "v"(idx));
      STAMP(1);
      unsigned o, vo2; unsigned long long okm;
      asm volatile(
        "s_nop 7\n\t"
        "v_add_f32 v248, v248, v250\n\t"
        "v_add_f32 v250, v249, v251\n\t"
        "v_add_f32 v249, v249, v251\n\t"
        "v_mov_b32_dpp v250, v248" DPL
        "s_nop 0\n\t"
        "v_mov_b32_dpp v248, v249" DPH
        "v_cmp_o_f32 %3, v248, v250\n\t"
        "v_cvt_pk_bf16_f32 %1, v248, v250\n\t"
        "s_nop 0\n\t"
        "v_cndmask_b32 %2, %5, %4, %3"
        : "+{v[248:251]}"(acc), "=&v"(o), "=&v"(vo2), "=&s"(okm) : "v"(mvo), "v"(oob));
#pragma unroll
      for (int c = 0; c < REPL; ++c) st32(rs, o, vo2, sbn[c]);
      CFENCE();
      if (__builtin_expect(okm == ~0ull, 1)) break;
      if ((!(good & 2) && !(has_sent(z) || has_sent(pm) || has_sent(px))) || --budget <= 0) {
#pragma unroll
        for (int c = 0; c < REPL; ++c) st32(rs, min(o, 0xFFFFFFFEu), mvo, sbn[c]);
        break;
      }
      good = 0;
      c_wait(sb, zb);
    }
    STAMP(4);
    STAMP_OUT(64 + cb, s);
    report(rs, 64 + cb, s, good, lane);
    BAR();
    if (s == 0) EV(2); if (s == NSEG - 2) EV(3);
    if (s > 0) rdy = max(rdy, (int)__builtin_amdgcn_readfirstlane(*(volatile LAS unsigned*)(uintptr_t)(L_TM + 32)));
    BAR();
    if (s + 2 < NSEG) fetch(s + 1);
  }
}

DEV void u_scatter(unsigned img, const f32x4 (&acc)[4][2], int wave, int lane) {
  const int n = lane & 15, g = lane >> 4;
#pragma unroll
  for (int c = 0; c < 4; ++c)
#pragma unroll
    for (int tt = 0; tt < 2; ++tt)
#pragma unroll
      for (int r = 0; r < 4; ++r) *(LAS float*)(uintptr_t)(img + wave * 8704 + (tt * 16 + 4 * g + r) * 272 + c * 64 + n * 4) = acc[c][tt][r];
}
DEV void u_gather(unsigned img, f32x4& v0, f32x4& v1, int tid) {
  v0 = (f32x4){0, 0, 0, 0}; v1 = v0;
#pragma unroll
  for (int ww = 0; ww < 4; ++ww) { const unsigned p = img + ww * 8704 + (tid >> 3) * 272 + (tid & 7) * 32; v0 += *(LAS f32x4*)(uintptr_t)p; v1 += *(LAS f32x4*)(uintptr_t)(p + 16); }
}
#define UOFF 240
#define UOFF_LAST 240
#define UPACE 20
DEV void role_u(const Args& a, rsrc_t rs, int wid, int wave, int lane, unsigned pol) {
  if (wid >= 120) return;
  const int cq = wid % 10, tid = wave * 64 + lane;
  const unsigned l16 = lane * 16;
  u32x4 w[56];
  load_w2(w, a.w2 + cq * 128, wave, lane);
  BAR();
  EV(7);
  unsigned pvo = OOB;
  if (lane < 32) pvo = prog_off(1, wave * 8 + (lane >> 2), lane & 1, (cq * 2 + ((lane >> 1) & 1)) & 3);
  const int nks = wave < 3 ? 14 : 12;
  int budget = SPIN_CAP;
  Sched sc; clk_init(sc);
  const unsigned tu0 = rtc();
  for (int seg = wid / 10; seg < NSEG; seg += 12) {
    while ((int)(rtc() - tu0) < seg * UPACE) __builtin_amdgcn_s_sleep(8);
    const int rep = seg % 9;
    if (lane >= 48) pvo = lane - 48 < nks ? hprog_off(rep, wave * 14 + lane - 48) : OOB;
    unsigned have, need = lane < 32 ? ((lane & 1) == (seg & 1) ? (seg >> 1) + 1 : 0) : (lane >= 48 && lane - 48 < nks) ? seg / 9 + 1 : 0;
    for (;;) { have = ld32(rs, pvo, 0); CFENCE(); if (__all(have >= need) || --budget <= 0) break; __builtin_amdgcn_s_sleep(4); }
    f32x4 acc[4][2];
    {
      u32x4 hf[14][2];
#pragma unroll
      for (int k = 0; k < 14; ++k)
#pragma unroll
        for (int tt = 0; tt < 2; ++tt) hf[k][tt] = ld128(rs, k < nks ? l16 + tt * 1024 : OOB, OFF_HH + (seg * 54 + wave * 14 + k) * 2048);
      static_for<14>([&](auto K) {
        static_for<4>([&](auto C) {
          constexpr int k = K, c = C, wi = ((k >> 1) * 2 + (c >> 1)) * 2 + (c & 1);
          mf_w<wi, k & 1, false, k == 0>(acc[c][0], w, hf[k][0]); mf_w<wi, k & 1, false, k == 0>(acc[c][1], w, hf[k][1]);
        });
      });
    }
    u32x4 bx[4][8];
#pragma unroll
    for (int c = 0; c < 4; ++c)
#pragma unroll
      for (int hh = 0; hh < 8; ++hh) bx[c][hh] = ld128(rs, l16 + (hh & 3) * 1024, b_addr(seg, cq * 4 + c, pol) + wave * 16384 + (hh & 4) * 1024);
    const size_t oo = (size_t)(seg * 32 + (tid >> 3)) * 1280 + cq * 128 + (tid & 7) * 16;
    const u32x4 xv = *(const u32x4*)(a.x + oo);
    acc_wait(acc);
    u_scatter(0, acc, wave, lane);
    BAR();
    f32x4 g0, g1;
    u_gather(0, g0, g1, tid);
#pragma unroll
    for (int r = 0; r < 4; ++r) { g0[r] = rbf(silu(rbf(g0[r]))); g1[r] = rbf(silu(rbf(g1[r]))); }
#pragma unroll
    for (int hh = 0; hh < 8; ++hh) asm volatile("" : "+a"(bx[0][hh]));
    {
      const bool act = lane < (seg == NSEG - 1 ? 16 : 8);
      const unsigned rvo = act ? ((seg + 1) * 128 + (lane >> 3) * 32 + wave * 8 + (lane & 7)) * 128 : OOB;
      sleep_until_turn(rs, sc, seg == NSEG - 1 ? 2 * seg + 1 : 2 * seg + 3, seg == NSEG - 1 ? UOFF_LAST : UOFF);
      for (;;) { const unsigned v = ld32(rs, rvo, OFF_REP); CFENCE(); if (__all(!act || v != 0) || --budget <= 0) break; if (seg == NSEG - 1) __builtin_amdgcn_s_sleep(2); else __builtin_amdgcn_s_sleep(16); }
    }
    EV(8);
    const unsigned so = OFF_PT + seg * 81920;
#pragma unroll
    for (int c = 0; c < 4; ++c) { acc[c][0] = (f32x4){0, 0, 0, 0}; acc[c][1] = (f32x4){0, 0, 0, 0}; }
    {
      u32x4 px[8][2];
#pragma unroll
      for (int hh = 0; hh < 8; ++hh)
#pragma unroll
        for (int tt = 0; tt < 2; ++tt) px[hh][tt] = ld128(rs, l16 + tt * 1024, so + (wave * 8 + hh) * 2048);
      acc_wait(acc);
#pragma unroll
      for (int hh = 0; hh < 4; ++hh)
#pragma unroll
        for (int c = 0; c < 4; ++c) { if (c == 0) { mf_va(acc[c][0], px[hh][0], bx[c][hh]); mf_va(acc[c][1], px[hh][1], bx[c][hh]); } else { mf_vv(acc[c][0], px[hh][0], bx[c][hh]); mf_vv(acc[c][1], px[hh][1], bx[c][hh]); } }
      CFENCE();
      u32x4 pm[2][2], z[4][2];
#pragma unroll
      for (int hf = 0; hf < 2; ++hf)
#pragma unroll
        for (int tt = 0; tt < 2; ++tt) pm[hf][tt] = ld128(rs, l16 + hf * 1024, so + 65536 + tt * 8192 + wave * 2048);
#pragma unroll
      for (int c = 0; c < 4; ++c)
#pragma unroll
        for (int hf = 0; hf < 2; ++hf) z[c][hf] = ld128(rs, l16 + hf * 1024, z_base(seg, pol) + (cq * 4 + c) * 16384 + wave * 2048);
      CFENCE();
#pragma unroll
      for (int hh = 4; hh < 8; ++hh)
#pragma unroll
        for (int c = 0; c < 4; ++c) { if (c == 0) { mf_va(acc[c][0], px[hh][0], bx[c][hh]); mf_va(acc[c][1], px[hh][1], bx[c][hh]); } else { mf_vv(acc[c][0], px[hh][0], bx[c][hh]); mf_vv(acc[c][1], px[hh][1], bx[c][hh]); } }
#pragma unroll
      for (int hf = 0; hf < 2; ++hf)
#pragma unroll
        for (int c = 0; c < 4; ++c) { mf_vv(acc[c][0], pm[hf][0], z[c][hf]); mf_vv(acc[c][1], pm[hf][1], z[c][hf]); }
    }
    acc_wait(acc);
    u_scatter(40960, acc, wave, lane);
    BAR();
    f32x4 s0, s1;
    u_gather(40960, s0, s1, tid);
    u32x4 o;
#pragma unroll
    for (int j = 0; j < 2; ++j) {
      o[j] = pk(__builtin_bit_cast(float, xv[j] << 16) + rbf(rbf(s0[2 * j]) * g0[2 * j]), __builtin_bit_cast(float, xv[j] & 0xFFFF0000u) + rbf(rbf(s0[2 * j + 1]) * g0[2 * j + 1]));
      o[2 + j] = pk(__builtin_bit_cast(float, xv[2 + j] << 16) + rbf(rbf(s1[2 * j]) * g1[2 * j]), __builtin_bit_cast(float, xv[2 + j] & 0xFFFF0000u) + rbf(rbf(s1[2 * j + 1]) * g1[2 * j + 1]));
    }
    *(u32x4*)(a.out + oo) = o;
    EV(9);
  }
}

#define WDELAY 122
#define WPACE 0
#define WAHEAD 3
DEV void role_w(rsrc_t rs, int xq, int k, int wave, int lane, unsigned pol) {
  const int cb0 = 10 * xq + 2 * k;
  unsigned sink = 0;
  int budget = SPIN_CAP;
  Sched sc; clk_init(sc);
  for (int seg = 1 + wave; seg < NSEG; seg += 4) {
    const int s = seg - WAHEAD;
    if (s >= 2) {
      while (sc.j0 == 0 && --budget > 0) { adopt_cur(sc, ld128(rs, 0, OFF_CTL + CTL_CUR), 2 * NSEG); CFENCE(); __builtin_amdgcn_s_sleep(8); }
      sleep_until_turn(rs, sc, 2 * s, WDELAY * 8 - 240);
    }
    while (__builtin_amdgcn_readfirstlane(ld32(rs, seg * 128, OFF_CTL + CTL_RL)) == 0 && --budget > 0) { CFENCE(); __builtin_amdgcn_s_sleep(8); }
    if (s >= 2) wait_until(tau_of(sc, 2 * s) + WDELAY * 8);
#define WLANES 64
    constexpr int NI = 64 / WLANES;
    unsigned v[12 * NI];
    const unsigned wvo = lane < WLANES ? lane * 128 : OOB;
#pragma unroll
    for (int j = 0; j < 12 * NI; ++j) {
      const int b = j / NI, u = min(k + 5 * (b - 8), 15);
      const unsigned so = b < 8 ? b_addr(seg, cb0 + (b >> 2), pol) + (b & 3) * 16384 : kq_addr(u & 1, seg, (u >> 1) * 4 + xq, pol);
      v[j] = ld32(rs, wvo, so + (j % NI) * WLANES * 128); CFENCE(); __builtin_amdgcn_s_sleep(WPACE);
    }
#pragma unroll
    for (int j = 0; j < 12 * NI; ++j) sink += v[j];
    CFENCE();
  }
  asm volatile("" :: "v"(sink));
}

#define LEAD1 64
#define LEAD2 64
#define NADAPT 8
#define TKOFF 200
#define TKSLEEP 2
#define GPOLL 2600
#define GPOLL0 2400
DEV void role_tk(rsrc_t rs, int lane) {
  Sched sc; clk_init(sc);
  Sched old = sc;
  int budget = SPIN_CAP, run = 0, m1 = 0, m2 = 0, relock = 0;
  const unsigned tref = now8();
  for (int j = 0; j < 2 * NSEG - 6; ++j) {
    const int s = j >> 1;
    const unsigned vo = rep_off(j, lane);
    u32x4 e;
    if (sc.j0 != 0 && j >= relock) wait_until((sc.j0 <= j + 1 ? tau_of(sc, j + 1) : tau_of(old, j + 1)) + TKOFF);
    for (;;) { e = ld128(rs, vo, OFF_REP); CFENCE(); if (__all(e.x != 0) || --budget <= 0) break; __builtin_amdgcn_s_sleep(TKSLEEP); }
    const bool zrow = !(j & 1) && lane >= 32;
    const unsigned M = tref + (unsigned)wmax((int)(e.y - tref) - (zrow ? VSLACK : 0));
    if (j + 1 >= relock) {
      const unsigned tn = sc.j0 <= j + 1 ? tau_of(sc, j + 1) : tau_of(old, j + 1);
      if (sc.j0 == 0 || (int)(M - tn) > LATE_THR) {
        sc.base += M + (sc.j0 == 0 ? GPOLL0 : GPOLL) - tau_of(sc, j + 3); sc.j0 = relock = j + 3;
        st128(rs, (u32x4){(unsigned)sc.j0, sc.base, (unsigned)sc.T, (unsigned)sc.D1}, lane < 8 ? lane * 16 : OOB, OFF_CTL + CTL_CUR);
        run = 0; m1 = 0; m2 = 0;
      }
    }
    clk_update(sc);
    const int du = (int)((float)(int)(e.z >> 2) * 2048.f * __builtin_amdgcn_rcpf((float)sc.R)) - (zrow ? VSLACK : 0);
  }
}

DEV float rowsum16(float v) {
  v += __builtin_bit_cast(float, __builtin_amdgcn_update_dpp(0, __builtin_bit_cast(int, v), 0xB1, 0xf, 0xf, false));
  v += __builtin_bit_cast(float, __builtin_amdgcn_update_dpp(0, __builtin_bit_cast(int, v), 0x4E, 0xf, 0xf, false));
  v += __builtin_bit_cast(float, __builtin_amdgcn_update_dpp(0, __builtin_bit_cast(int, v), 0x124, 0xf, 0xf, false));
  v += __builtin_bit_cast(float, __builtin_amdgcn_update_dpp(0, __builtin_bit_cast(int, v), 0x128, 0xf, 0xf, false));
  return v;
}
DEV void layernorm(const Args& a, rsrc_t rs, int seg, int wave, int lane) {
  const int sub = lane & 15;
  u32x4 v[2][5];
#pragma unroll
  for (int p = 0; p < 2; ++p)
#pragma unroll
    for (int i = 0; i < 5; ++i) v[p][i] = *(const u32x4*)(a.x + (size_t)(seg * 32 + wave * 8 + p * 4 + (lane >> 4)) * 1280 + (sub + 16 * i) * 16);
#pragma unroll
  for (int p = 0; p < 2; ++p) {
    float f[40], sum = 0.f;
#pragma unroll
    for (int i = 0; i < 5; ++i)
#pragma unroll
      for (int j = 0; j < 4; ++j) { f[i * 8 + 2 * j] = __builtin_bit_cast(float, v[p][i][j] << 16); f[i * 8 + 2 * j + 1] = __builtin_bit_cast(float, v[p][i][j] & 0xFFFF0000u); sum += f[i * 8 + 2 * j] + f[i * 8 + 2 * j + 1]; }
    const float mean = rowsum16(sum) * (1.f / 640.f);
    float sq = 0.f;
#pragma unroll
    for (int i = 0; i < 40; ++i) { const float d = f[i] - mean; sq = fmaf(d, d, sq); }
    const float mb = rbf(mean), vb = rbf(rowsum16(sq) * (1.f / 640.f));
    const float inv = 1.f / rbf(sqrtf(rbf(vb + 1e-5f)));
    const int row = wave * 8 + p * 4 + (lane >> 4);
#pragma unroll
    for (int i = 0; i < 5; ++i) {
      u32x4 o;
#pragma unroll
      for (int j = 0; j < 4; ++j) o[j] = pk(rbf(f[i * 8 + 2 * j] - mb) * inv, rbf(f[i * 8 + 2 * j + 1] - mb) * inv);
      const int c = sub + 16 * i;
      ldsw128(((c >> 2) * 2 + (row >> 4)) * 1024 + ((c & 3) * 16 + (row & 15)) * 16, o);
    }
  }
  BAR();
#pragma unroll
  for (int i = 0; i < 10; ++i) st128(rs, ldsr128((wave * 10 + i) * 1024 + lane * 16), lane * 16, OFF_XN + seg * 40960 + (wave * 10 + i) * 1024);
  VMWAIT(0);
  BAR();
  if (wave == 0) st32(rs, 1, lane == 0 ? seg * 128 : OOB, OFF_CTL + CTL_XP);
}

__global__ void __launch_bounds__(256, 1) k_main(Args a) {
  const int lane = threadIdx.x & 63, wave = __builtin_amdgcn_readfirstlane(threadIdx.x >> 6);
  const rsrc_t rs = __builtin_amdgcn_make_buffer_rsrc(a.ws, 0, WS_BYTES, 0x00020000);
  const u32x4 pr = ld128(rs, 0, OFF_CTL + CTL_POL);
  const unsigned xoff = __builtin_amdgcn_readfirstlane(ld32(rs, 16, OFF_CTL + CTL_POL));
  const int b = blockIdx.x, xq = (b + xoff) & 7, ix = b >> 3;
  EV(0);
  const unsigned pol = __builtin_amdgcn_readfirstlane((pr.y < pr.x ? 1u : 0u) | (pr.w < pr.z ? 2u : 0u)) ^ ((xoff & 4) ? 3u : 0u);
  if (xq < 4) {
    const int r = ix * 4 + xq;
    if (r < 32) role_p(a, rs, r, wave, lane, pol);
    else if (r < 64) role_v(a, rs, r - 32, wave, lane, pol);
    else {
      layernorm(a, rs, r - 64, wave, lane);
      EV(1);
      if (r < 104) role_c(a, rs, 10 * xq + ((r - 64) >> 2), wave, lane, pol);
      else if (r < 124) role_w(rs, xq, (r - 104) >> 2, wave, lane, pol);
      else if (r == 124 && wave == 0) role_tk(rs, lane);
      else if (r == 125 && wave == 0) role_agg(rs, lane);
    }
  } else {
    const int pid = (xq - 4) * 16 + (ix >> 1), par = ix & 1;
    const int wid = (xq - 4) * 32 + ix;
    if (pid & 1) role_x<K_VB>(a, rs, pid >> 1, par, 2, prog_off(1, pid >> 1, par, wave), wave, lane, pol);
    else role_x<K_QK>(a, rs, pid >> 1, par, 2, prog_off(0, pid >> 1, par, wave), wave, lane, pol);
    if (wid < 126) role_x<K_F1>(a, rs, (wid % 14) * 4 + wave, wid / 14, 9, hprog_off(wid / 14, (wid % 14) * 4 + wave), wave, lane, pol);
    role_u(a, rs, wid, wave, lane, pol);
  }
}

__global__ void __launch_bounds__(256) k_pro(Args a) {
  const int b = blockIdx.x, tid = threadIdx.x, lane = tid & 63, wave = __builtin_amdgcn_readfirstlane(tid >> 6);
  if (b < 32) {
    if ((b & 7) == 0 && wave == 0) {
      const int j = b >> 3;
      const rsrc_t rs = __builtin_amdgcn_make_buffer_rsrc(a.ws, 0, WS_BYTES, 0x00020000);
      const unsigned base = OFF_COMM + (j >> 1) * 2 * MB + 48 * 16384 + (j & 1) * 8192;
      st128(rs, (u32x4){1, 2, 3, 4}, lane * 16, base);
      VMWAIT(0);
      unsigned best = ~0u;
#pragma unroll
      for (int r = 0; r < 3; ++r) {
        unsigned long long c0, c1; unsigned v;
        asm volatile("s_memtime %0\n\ts_waitcnt lgkmcnt(0)" : "=s"(c0) :: "memory");
        v = ld32(rs, r * 256, base);
        asm volatile("s_waitcnt vmcnt(0)\n\ts_memtime %0\n\ts_waitcnt lgkmcnt(0)" : "=s"(c1) : "v"(v) : "memory");
        best = min(best, (unsigned)(c1 - c0));
      }
      if (lane == 0) { *(unsigned*)(a.ws + OFF_CTL + CTL_POL + 4 * j) = best; if (j == 0) *(unsigned*)(a.ws + OFF_CTL + CTL_POL + 16) = xcc_id(); }
    }
  } else if (b < 160) {
#pragma unroll
    for (int i = 0; i < 2; ++i) {
      const unsigned c = ((b - 32) * 2 + i) * 256 + tid;
      *(u32x4*)(a.ws + OFF_COMM + (c >> 14) * MB + (c & 16383) * 16) = (u32x4){SENT, SENT, SENT, SENT};
    }
  } else {
    const unsigned i = (b - 160) * 256 + tid;
    if (i < 1600) { if (i * 128 != CTL_POL) *(unsigned*)(a.ws + OFF_CTL + i * 128) = 0; }
    else if (i < 1600 + 65 * 128) *(unsigned*)(a.ws + OFF_REP + (i - 1600) * 128) = 0;
  }
}

extern "C" void kernel_launch(void* const* d_in, const int* in_sizes, int n_in, void* d_out, int out_size, void* d_ws, size_t ws_size, hipStream_t stream) {
  Args a;
  a.x = (const unsigned char*)d_in[0]; a.mem0 = (const unsigned char*)d_in[1]; a.wq = (const unsigned char*)d_in[2]; a.wk = (const unsigned char*)d_in[3];
  a.wv = (const unsigned char*)d_in[4]; a.wo = (const unsigned char*)d_in[5]; a.w1 = (const unsigned char*)d_in[8]; a.w3 = (const unsigned char*)d_in[9];
  a.w2 = (const unsigned char*)d_in[10]; a.out = (unsigned char*)d_out; a.ws = (unsigned char*)d_ws;
  (void)hipFuncSetAttribute((const void*)k_main, hipFuncAttributeMaxDynamicSharedMemorySize, 163840);
  k_pro<<<200, 256, 0, stream>>>(a);
  k_main<<<256, 256, 163840, stream>>>(a);
}
```

```cpp
#include <hip/hip_runtime.h>
#include <stdint.h>

typedef __bf16 bf16x8 __attribute__((ext_vector_type(8)));
typedef __bf16 bf16x16 __attribute__((ext_vector_type(16)));
typedef __bf16 bf16x2 __attribute__((ext_vector_type(2)));
typedef float f32x2 __attribute__((ext_vector_type(2)));
typedef float f32x4 __attribute__((ext_vector_type(4)));
typedef unsigned u32x2 __attribute__((ext_vector_type(2)));
typedef unsigned u32x4 __attribute__((ext_vector_type(4)));
typedef unsigned u32x8 __attribute__((ext_vector_type(8)));
typedef short s16x4 __attribute__((ext_vector_type(4)));
#define LAS __attribute__((address_space(3)))
#define DEV __device__ __forceinline__

constexpr int NSEG = 64;
constexpr unsigned MB = 1u << 20;
constexpr unsigned OFF_COMM = 0;
constexpr unsigned OFF_CTL = 4 * MB;
constexpr unsigned OFF_XN = 5 * MB;
constexpr unsigned OFF_PT = 14 * MB;
constexpr unsigned OFF_HH = 20 * MB;
constexpr unsigned OFF_BLK = 32 * MB;
constexpr unsigned OFF_DBG = 240 * MB;
constexpr unsigned WS_BYTES = 248 * MB;
constexpr unsigned OOB = 0xF0000000u;
constexpr unsigned C_MEM1 = 16384, C_PM = 16384 + 2048, C_PX = 2 * 16384, C_Z = 4 * 16384;
constexpr unsigned CTL_POL = 65536;
constexpr unsigned CTL_RL = 65536 + 4096;
constexpr unsigned CTL_HP = 131072;
constexpr unsigned SENT = 0xFFFFFFFFu;
constexpr int SPIN_CAP = 1 << 20;
constexpr int SC1 = 16;

typedef __amdgpu_buffer_rsrc_t rsrc_t;
DEV u32x4 ld128(rsrc_t r, unsigned vo, unsigned so) { return __builtin_amdgcn_raw_buffer_load_b128(r, vo, so, SC1); }
DEV u32x2 ld64(rsrc_t r, unsigned vo, unsigned so) { return __builtin_amdgcn_raw_buffer_load_b64(r, vo, so, SC1); }
DEV unsigned ld32(rsrc_t r, unsigned vo, unsigned so) { return __builtin_amdgcn_raw_buffer_load_b32(r, vo, so, SC1); }
DEV void st128(rsrc_t r, u32x4 v, unsigned vo, unsigned so) { __builtin_amdgcn_raw_buffer_store_b128(v, r, vo, so, SC1); }
DEV void st64(rsrc_t r, u32x2 v, unsigned vo, unsigned so) { __builtin_amdgcn_raw_buffer_store_b64(v, r, vo, so, SC1); }
DEV void st32(rsrc_t r, unsigned v, unsigned vo, unsigned so) { __builtin_amdgcn_raw_buffer_store_b32(v, r, vo, so, SC1); }
DEV unsigned pk(float a, float b) { bf16x2 v = {(__bf16)a, (__bf16)b}; return __builtin_bit_cast(unsigned, v); }
DEV unsigned pks(float a, float b) { return min(pk(a, b), 0xFFFFFFFEu); }
DEV unsigned mx4(u32x4 v) { return max(max(v.x, v.y), max(v.z, v.w)); }
DEV f32x4 mfma(u32x4 a, u32x4 b, f32x4 c) { return __builtin_amdgcn_mfma_f32_16x16x32_bf16(__builtin_bit_cast(bf16x8, a), __builtin_bit_cast(bf16x8, b), c, 0, 0, 0); }
DEV f32x4 smf(u32x4 a, u32x8 b, f32x4 c, int idx) { return __builtin_amdgcn_smfmac_f32_16x16x64_bf16(__builtin_bit_cast(bf16x8, a), __builtin_bit_cast(bf16x16, b), c, idx, 0, 0); }
DEV u32x4 lo4(u32x8 v) { return __builtin_shufflevector(v, v, 0, 1, 2, 3); }
DEV u32x4 hi4(u32x8 v) { return __builtin_shufflevector(v, v, 4, 5, 6, 7); }
DEV u32x8 cat8(u32x4 a, u32x4 b) { return __builtin_shufflevector(a, b, 0, 1, 2, 3, 4, 5, 6, 7); }
DEV u32x4 cat4(u32x2 a, u32x2 b) { return __builtin_shufflevector(a, b, 0, 1, 2, 3); }
DEV u32x2 trread(unsigned lds_addr) { return __builtin_bit_cast(u32x2, __builtin_amdgcn_ds_read_tr16_b64_v4i16((LAS s16x4*)(uintptr_t)lds_addr)); }
DEV u32x4 ldsr128(unsigned a) { return *(LAS u32x4*)(uintptr_t)a; }
DEV void ldsw128(unsigned a, u32x4 v) { *(LAS u32x4*)(uintptr_t)a = v; }
DEV void ldsw64(unsigned a, u32x2 v) { *(LAS u32x2*)(uintptr_t)a = v; }
#define BAR() asm volatile("s_waitcnt lgkmcnt(0)\n\ts_barrier" ::: "memory")
#define VMWAIT(n) asm volatile("s_waitcnt vmcnt(" #n ")" ::: "memory")
#define LGWAIT() asm volatile("s_waitcnt lgkmcnt(0)" ::: "memory")
#define CFENCE() asm volatile("" ::: "memory")
template <int N> DEV void vmwait() { asm volatile("s_waitcnt vmcnt(%0)" :: "n"(N) : "memory"); }
DEV void glds(const void* sbase, unsigned voff, unsigned ldst) {
  unsigned keep;
  asm volatile("s_mov_b32 %0, m0\n\ts_mov_b32 m0, %3\n\ts_nop 0\n\tglobal_load_lds_dwordx4 %1, %2\n\ts_mov_b32 m0, %0" : "=&s"(keep) : "v"(voff), "s"(sbase), "s"(ldst) : "memory");
}
constexpr unsigned CTL_XP = 196608;
DEV void glds_sc1(const void* sbase, unsigned voff, unsigned ldst) {
  unsigned keep;
  asm volatile("s_mov_b32 %0, m0\n\ts_mov_b32 m0, %3\n\ts_nop 0\n\tglobal_load_lds_dwordx4 %1, %2 sc1\n\ts_mov_b32 m0, %0" : "=&s"(keep) : "v"(voff), "s"(sbase), "s"(ldst) : "memory");
}
DEV void set_m0(unsigned v) { asm volatile("s_mov_b32 m0, %0" :: "s"(v) : "memory"); }
template <int OFF> DEV void glds_off(const void* sbase, unsigned voff) { asm volatile("global_load_lds_dwordx4 %0, %1 offset:%2 sc1" :: "v"(voff), "s"(sbase), "n"(OFF) : "memory"); }
template <int V> struct IC { static constexpr int value = V; constexpr operator int() const { return V; } };
template <int N, int I = 0, class F> DEV void static_for(F&& f) { if constexpr (I < N) { f(IC<I>{}); static_for<N, I + 1>(f); } }
DEV float fold32(float a, float b) {
  auto r = __builtin_amdgcn_permlane32_swap(__builtin_bit_cast(unsigned, a), __builtin_bit_cast(unsigned, b), false, false);
  return __builtin_bit_cast(float, (unsigned)r[0]) + __builtin_bit_cast(float, (unsigned)r[1]);
}
DEV float xmax16(float v) { auto r = __builtin_amdgcn_permlane16_swap(__builtin_bit_cast(unsigned, v), __builtin_bit_cast(unsigned, v), false, false); return fmaxf(__builtin_bit_cast(float, (unsigned)r[0]), __builtin_bit_cast(float, (unsigned)r[1])); }
DEV float xmax32(float v) { auto r = __builtin_amdgcn_permlane32_swap(__builtin_bit_cast(unsigned, v), __builtin_bit_cast(unsigned, v), false, false); return fmaxf(__builtin_bit_cast(float, (unsigned)r[0]), __builtin_bit_cast(float, (unsigned)r[1])); }
DEV float xadd16(float v) { auto r = __builtin_amdgcn_permlane16_swap(__builtin_bit_cast(unsigned, v), __builtin_bit_cast(unsigned, v), false, false); return __builtin_bit_cast(float, (unsigned)r[0]) + __builtin_bit_cast(float, (unsigned)r[1]); }
DEV float xadd32(float v) { auto r = __builtin_amdgcn_permlane32_swap(__builtin_bit_cast(unsigned, v), __builtin_bit_cast(unsigned, v), false, false); return __builtin_bit_cast(float, (unsigned)r[0]) + __builtin_bit_cast(float, (unsigned)r[1]); }
DEV float bf2f(unsigned short u) { return __builtin_bit_cast(float, (unsigned)u << 16); }
DEV float rbf(float x) { return (float)(__bf16)x; }

DEV void smf_a(f32x4& c, u32x4 a, const u32x8& w, int idx) { asm volatile("v_smfmac_f32_16x16x64_bf16 %0, %1, %2, %3" : "+v"(c) : "v"(a), "a"(w), "v"(idx)); }
DEV void smf_v(f32x4& c, u32x4 a, const u32x8& w, int idx) { asm volatile("v_smfmac_f32_16x16x64_bf16 %0, %1, %2, %3" : "+v"(c) : "v"(a), "v"(w), "v"(idx)); }
template <int I> DEV void smf_w(f32x4& c, u32x4 a, const u32x8 (&w)[40], int idx) { if constexpr (I < 32) smf_a(c, a, w[I], idx); else smf_v(c, a, w[I], idx); }
DEV void mf_va(f32x4& c, u32x4 a, u32x4 b) { asm volatile("v_mfma_f32_16x16x32_bf16 %0, %1, %2, %0" : "+v"(c) : "v"(a), "a"(b)); }
DEV void mf_vv(f32x4& c, u32x4 a, u32x4 b) { asm volatile("v_mfma_f32_16x16x32_bf16 %0, %1, %2, %0" : "+v"(c) : "v"(a), "v"(b)); }
template <bool A> DEV void pin_w(u32x8& x, u32x8& y) {
  if constexpr (A) asm volatile("s_waitcnt lgkmcnt(0)" : "+a"(x), "+a"(y) :: "memory"); else asm volatile("s_waitcnt lgkmcnt(0)" : "+v"(x), "+v"(y) :: "memory");
}
template <bool A> DEV void pin_w4(u32x4& x, u32x4& y, u32x4& z, u32x4& u) {
  if constexpr (A) asm volatile("s_waitcnt lgkmcnt(0)" : "+a"(x), "+a"(y), "+a"(z), "+a"(u) :: "memory"); else asm volatile("s_waitcnt lgkmcnt(0)" : "+v"(x), "+v"(y), "+v"(z), "+v"(u) :: "memory");
}
DEV void pin2(f32x4& x, f32x4& y) { asm volatile("" : "+v"(x), "+v"(y)); }
DEV void pin2n(f32x4& x, f32x4& y) { asm volatile("s_nop 3" : "+v"(x), "+v"(y)); }
DEV unsigned pin1(unsigned x) { asm volatile("" :: "v"(x)); return x; }
#define NOP(n) asm volatile("s_nop " #n)
#define PIN(x) asm volatile("" : "+v"(x))
#define PINS(x) asm volatile("" : "+s"(x))
DEV unsigned xcc_id() { return (unsigned)__builtin_amdgcn_s_getreg(20 | (3 << 11)) & 0xfu; }
DEV unsigned rtc() { unsigned long long t; asm volatile("s_memrealtime %0\n\ts_waitcnt lgkmcnt(0)" : "=s"(t) :: "memory"); return (unsigned)t; }
constexpr unsigned CTL_CUR = 65536 + 12288 + 1024;
constexpr unsigned OFF_REP = 8 * MB;
constexpr int MAXWAIT = 400 * 8;
#define D1_INIT 860
#define D2_INIT 520
struct Sched { int j0, jbad; unsigned base; int T, D1; unsigned c0, v0; int R; unsigned ct, tt; };
DEV unsigned now8() { return rtc() << 3; }
DEV unsigned cycles() { unsigned long long t; asm volatile("s_memtime %0\n\ts_waitcnt lgkmcnt(0)" : "=s"(t) :: "memory"); return (unsigned)t; }
DEV void clk_init(Sched& c) { c.j0 = 0; c.jbad = 0; c.base = 0; c.T = D1_INIT + D2_INIT; c.D1 = D1_INIT; c.v0 = rtc(); c.c0 = cycles(); c.R = 24 * 256; c.ct = c.c0; c.tt = 0; }
DEV void clk_update(Sched& c) {
  const unsigned v = rtc() - c.v0, y = cycles() - c.c0;
  if (v > 300) c.R = __builtin_amdgcn_readfirstlane((int)((float)y * 256.f * __builtin_amdgcn_rcpf((float)v)));
}
DEV unsigned tau_of(const Sched& c, int j) { return c.base + (j >> 1) * c.T + ((j & 1) ? c.D1 : 0); }
DEV void adopt_cur(Sched& c, u32x4 v, int j) {
  const int j0 = __builtin_amdgcn_readfirstlane(v.x);
  if (j0 > c.j0 && j0 <= j) { c.j0 = j0; c.base = __builtin_amdgcn_readfirstlane(v.y); c.T = __builtin_amdgcn_readfirstlane(v.z); c.D1 = __builtin_amdgcn_readfirstlane(v.w); }
}
DEV bool wait_until(unsigned tau) {
  int d = (int)(tau - now8());
  if (d <= 0 || d > MAXWAIT) return false;
  while ((int)(tau - now8()) > 0) {}
  return true;
}
#define NOP16 "s_nop 0\n\ts_nop 0\n\ts_nop 0\n\ts_nop 0\n\ts_nop 0\n\ts_nop 0\n\ts_nop 0\n\ts_nop 0\n\ts_nop 0\n\ts_nop 0\n\ts_nop 0\n\ts_nop 0\n\ts_nop 0\n\ts_nop 0\n\ts_nop 0\n\ts_nop 0\n\t"
#define FK 96
DEV unsigned est_cycle(unsigned tau, int R, unsigned c0, int best) {
  unsigned long long v, c; unsigned t; int rem;
  asm volatile(
    "1:\n\t"
    "s_memrealtime %0\n\ts_memtime %1\n\ts_waitcnt lgkmcnt(0)\n\t"
    "s_lshl_b32 %2, s94, 3\n\t"
    "s_sub_i32 %2, %5, %2\n\t"
    "s_mul_i32 %2, %2, %6\n\t"
    "s_ashr_i32 %2, %2, 11\n\t"
    "s_sub_i32 %3, s96, %7\n\t"
    "s_add_i32 %2, %2, %3\n\t"
    "s_min_i32 %4, %4, %2\n\t"
    "s_sub_i32 %2, %2, 100\n\t"
    "s_max_i32 %4, %4, %2\n\t"
    "s_sub_i32 %3, %4, %3\n\t"
    "s_cmp_gt_i32 %3, 520\n\t"
    "s_cbranch_scc1 1b"
    : "=&{s[94:95]}"(v), "=&{s[96:97]}"(c), "=&s"(t), "=&s"(rem), "+s"(best)
    : "s"(tau), "s"(R), "s"(c0) : "memory", "scc");
  return c0 + best;
}
DEV void release(unsigned cs) {
  unsigned long long c, pc; int rem;
  asm volatile(
    "1:\n\t"
    "s_memtime %0\n\ts_waitcnt lgkmcnt(0)\n\t"
    "s_sub_i32 %2, %3, s96\n\t"
    "s_cmp_gt_i32 %2, %4\n\t"
    "s_cbranch_scc1 1b\n\t"
    "s_sub_i32 %2, %2, %5\n\t"
    "s_max_i32 %2, %2, 0\n\t"
    "s_and_b32 %2, %2, -4\n\t"
    "s_sub_i32 %2, 140, %2\n\t"
    "s_getpc_b64 %1\n\t"
    "s_add_u32 s98, s98, %2\n\t"
    "s_addc_u32 s99, s99, 0\n\t"
    "s_setpc_b64 %1\n\t"
    NOP16 NOP16 "s_barrier"
    : "=&{s[96:97]}"(c), "=&{s[98:99]}"(pc), "=&s"(rem)
    : "s"(cs), "n"(FK + 124), "n"(FK) : "memory", "scc");
}
#define DRIFT 10
#define LATE_THR 120
constexpr unsigned L_TM = 61440;
DEV void mark_overrun(Sched& c, int j, unsigned t) { if ((int)(t - tau_of(c, j + 1)) > LATE_THR + 40) c.jbad = j; }
template <class F>
DEV unsigned trigger(rsrc_t rs, Sched& c, int j, unsigned hold, F&& poll) {
  unsigned cs, timed = 0;
  for (;;) {
    const unsigned tau = tau_of(c, j), now = now8();
    const int d = (int)(tau - now);
    const bool ok = c.j0 > c.jbad && d > -30 * 8 && d <= MAXWAIT;
    if (ok && d <= 0) { cs = cycles(); break; }
    if (ok && d <= 50 * 8) {
      const unsigned dt = tau - c.tt;
      const int prior = dt < 4096 ? (int)((dt * (unsigned)c.R) >> 11) + DRIFT : 0x7fffffff;
      cs = est_cycle(__builtin_amdgcn_readfirstlane(tau), __builtin_amdgcn_readfirstlane(c.R), __builtin_amdgcn_readfirstlane(c.ct), __builtin_amdgcn_readfirstlane(prior));
      c.ct = cs; c.tt = tau; timed = 1;
      break;
    }
    if (!ok && (unsigned)(hold - now) > (unsigned)MAXWAIT) { poll(); cs = cycles(); break; }
    if (d > 100 * 8 || !ok) { adopt_cur(c, ld128(rs, 0, OFF_CTL + CTL_CUR), j); CFENCE(); }
  }
  ldsw64(L_TM + (j & 2) * 8, (u32x2){cs, timed | (unsigned)c.R << 2});
  return __builtin_amdgcn_readfirstlane(cs);
}
#define STAMP(k)
#define STV(k) stdummy
#define STAMP_DECL unsigned stdummy
#define STAMP_OUT(e, s)

#define BFAR 1
DEV unsigned blk_addr(int blk, unsigned pol, unsigned far) { return OFF_BLK + blk * 16384 + (((pol >> ((blk >> 7) & 1)) ^ far) & 1) * 8192; }
DEV unsigned z_base(int s, unsigned pol) { const int pg = s / 3; return blk_addr(10240 + pg * 128 + (s - 3 * pg) * 40, pol, 0); }
DEV unsigned kq_addr(int isq, int seg, int h, unsigned pol) { return blk_addr(isq * 2048 + seg * 32 + h, pol, 1 - BFAR); }
DEV unsigned b_addr(int seg, int cb, unsigned pol) { return blk_addr((seg * 40 + cb) * 4, pol, BFAR); }

#define EV(slot)
struct Args {
  const unsigned char* x; const unsigned char* mem0; const unsigned char* wq; const unsigned char* wk; const unsigned char* wv; const unsigned char* wo;
  const unsigned char* w1; const unsigned char* w3; const unsigned char* w2; unsigned char* out; unsigned char* ws;
};

enum { K_P, K_V, K_QK, K_VB, K_F1 };
constexpr unsigned L_XB0 = 120 * 1024, L_XB1 = 80 * 1024;
template <int KIND, class WT, class LATE>
DEV void load_weights(WT& w, const unsigned char* baseA, const unsigned char* baseB, int wave, int lane, int cmask, LATE&& late) {
  constexpr int NG = 20;
  constexpr bool WO = KIND == K_V || KIND == K_VB;
  constexpr unsigned SA = KIND == K_F1 ? 3456 : 8192, SB = WO ? 1280 : SA;
  constexpr int MA = (KIND == K_P || KIND == K_V) ? 2 : 0, MBB = KIND == K_P ? 2 : KIND == K_VB ? 1 : 0;
  const int gB = lane >> 4, q = (lane >> 2) & 3, pp = lane & 3;
  unsigned vo[2][4], rd[2][2];
#pragma unroll
  for (int m = 0; m < 2; ++m) {
    const int mode = m ? MBB : MA;
#pragma unroll
    for (int p = 0; p < 4; ++p) {
      const int key = mode == 0 ? (gB | (p >> 1) << 2) : mode == 1 ? (gB | (p & 1) << 2) : ((gB >> 1) | p << 1);
      vo[m][p] = (wave * 16 + p * 4 + gB) * (m ? SB : SA) + (((lane & 15) ^ (key << 1)) & cmask) * 16;
    }
    const int key = mode == 2 ? ((gB & 1) | (q >> 1) << 1 | (q & 1) << 2) : (q | (gB & 1) << 2);
    const int row = mode == 0 ? 8 * gB + q : mode == 1 ? 4 * gB + q : 16 * (gB >> 1) + 4 * (q >> 1) + 2 * (gB & 1) + 8 * (q & 1);
#pragma unroll
    for (int nt = 0; nt < 2; ++nt) rd[m][nt] = row * 256 + ((4 * wave + 2 * nt + (pp >> 1)) ^ (key << 1)) * 16 + (pp & 1) * 8;
  }
  auto issue = [&](int g) {
    const unsigned char* src = g < 10 ? baseA + (size_t)g * 64 * SA : WO ? baseB + (size_t)((g - 10) & 1) * 64 * SB + ((g - 10) >> 1) * 256 : baseB + (size_t)(g - 10) * 64 * SB;
#pragma unroll
    for (int p = 0; p < 4; ++p) glds(src, vo[g >= 10][p], (g % 7) * 16384 + wave * 4096 + p * 1024);
  };
#pragma unroll
  for (int g = 0; g < 7; ++g) issue(g);
  static_for<NG>([&](auto G) {
    constexpr int g = G, mode = g < 10 ? MA : MBB;
    if constexpr (g == 0) vmwait<24>(); else if constexpr (g < NG - 5) vmwait<20>(); else if constexpr (g == NG - 5) late();
    if constexpr (g <= NG - 5) BAR();
    if constexpr (g >= 1 && g + 6 < NG) issue(g + 6);
    constexpr unsigned phs = mode == 2 ? 256 : mode == 1 ? 16 * 256 : 4 * 256, sl = (g % 7) * 16384;
#pragma unroll
    for (int nt = 0; nt < 2; ++nt) {
      const unsigned rb = rd[g >= 10][nt] + sl;
      u32x2 a = trread(rb), b = trread(rb + phs), c = trread(rb + 8192), d = trread(rb + 8192 + phs);
      if constexpr (KIND == K_P || KIND == K_V) w[g * 2 + nt] = cat8(cat4(a, b), cat4(c, d));
      else { w[g * 4 + nt * 2] = cat4(a, b); w[g * 4 + nt * 2 + 1] = cat4(c, d); }
    }
    if constexpr (KIND == K_P || KIND == K_V) pin_w<g < 16>(w[g * 2], w[g * 2 + 1]);
    else pin_w4<g < 16>(w[g * 4], w[g * 4 + 1], w[g * 4 + 2], w[g * 4 + 3]);
  });
}
DEV void load_w2(u32x4 (&w)[56], const unsigned char* base, int wave, int lane) {
  const int gB = lane >> 4, q = (lane >> 2) & 3, pp = lane & 3;
  const unsigned ring = wave * 32768;
  const unsigned vo = (lane >> 3) * 1280 + ((lane & 7) ^ (((lane >> 4) & 3) << 1)) * 16;
  unsigned rd[4];
#pragma unroll
  for (int c = 0; c < 4; ++c) rd[c] = ring + (4 * gB + q) * 128 + ((2 * c + (pp >> 1)) ^ ((((gB & 1) << 1) | (q >> 1)) << 1)) * 16 + (pp & 1) * 8;
  auto issue = [&](int g) {
    const unsigned char* src = base + (size_t)min(wave * 7 + g, 26) * 64 * 1280;
#pragma unroll
    for (int p = 0; p < 8; ++p) glds(src + p * 8 * 1280, vo, ring + (g & 3) * 8192 + p * 1024);
  };
#pragma unroll
  for (int g = 0; g < 4; ++g) issue(g);
  static_for<7>([&](auto G) {
    constexpr int g = G;
    vmwait<8 * (6 - g < 3 ? 6 - g : 3)>();
#pragma unroll
    for (int c = 0; c < 4; ++c) {
      const unsigned rb = rd[c] + (g & 3) * 8192;
      w[g * 8 + c * 2] = cat4(trread(rb), trread(rb + 2048)); w[g * 8 + c * 2 + 1] = cat4(trread(rb + 4096), trread(rb + 6144));
    }
    pin_w4<true>(w[g * 8], w[g * 8 + 1], w[g * 8 + 2], w[g * 8 + 3]); pin_w4<true>(w[g * 8 + 4], w[g * 8 + 5], w[g * 8 + 6], w[g * 8 + 7]);
    if constexpr (g + 4 < 7) issue(g + 4);
  });
}

DEV unsigned prog_off(int type, int h, int par, int wave) { return OFF_CTL + (((type * 32 + h) * 2 + par) * 4 + wave) * 128; }
DEV unsigned hprog_off(int rep, int slice) { return OFF_CTL + CTL_HP + (rep * 56 + slice) * 128; }
DEV float silu(float x) { return x * __builtin_amdgcn_rcpf(1.f + __builtin_amdgcn_exp2f(-1.4426950408889634f * x)); }
DEV u32x2 pk4(f32x4 v) { return (u32x2){pk(v[0], v[1]), pk(v[2], v[3])}; }

template <int I, int HI, bool WA, bool FIRST, int N>
DEV void mf_w(f32x4& c, const u32x4 (&w)[N], u32x4 x) {
  const u32x4 f = w[2 * I + HI];
  if constexpr (FIRST) {
    if constexpr (I < 32) { if constexpr (WA) asm volatile("v_mfma_f32_16x16x32_bf16 %0, %1, %2, 0" : "=&v"(c) : "a"(f), "v"(x)); else asm volatile("v_mfma_f32_16x16x32_bf16 %0, %2, %1, 0" : "=&v"(c) : "a"(f), "v"(x)); }
    else { if constexpr (WA) asm volatile("v_mfma_f32_16x16x32_bf16 %0, %1, %2, 0" : "=&v"(c) : "v"(f), "v"(x)); else asm volatile("v_mfma_f32_16x16x32_bf16 %0, %2, %1, 0" : "=&v"(c) : "v"(f), "v"(x)); }
  } else {
    if constexpr (I < 32) { if constexpr (WA) asm volatile("v_mfma_f32_16x16x32_bf16 %0, %1, %2, %0" : "+v"(c) : "a"(f), "v"(x)); else asm volatile("v_mfma_f32_16x16x32_bf16 %0, %2, %1, %0" : "+v"(c) : "a"(f), "v"(x)); }
    else { if constexpr (WA) asm volatile("v_mfma_f32_16x16x32_bf16 %0, %1, %2, %0" : "+v"(c) : "v"(f), "v"(x)); else asm volatile("v_mfma_f32_16x16x32_bf16 %0, %2, %1, %0" : "+v"(c) : "v"(f), "v"(x)); }
  }
}

DEV void issue_x(const unsigned char* ws, int seg, unsigned buf, int wave, int lane) {
#pragma unroll
  for (int i = 0; i < 10; ++i) glds_sc1(ws + OFF_XN + (size_t)seg * 40960 + (wave * 10 + i) * 1024, lane * 16, buf + (wave * 10 + i) * 1024);
}
template <int NT, class MID>
DEV void xproj(f32x4 (&acc)[NT][2], const u32x4 (&w)[80], unsigned xa, MID&& mid) {
  u32x4 xr[3][2];
#pragma unroll
  for (int k = 0; k < 2; ++k) { xr[k][0] = ldsr128(xa + k * 2048); xr[k][1] = ldsr128(xa + k * 2048 + 1024); }
  static_for<20>([&](auto K) {
    constexpr int ks = K;
    static_for<2 * NT>([&](auto J) {
      constexpr int j = J, t = j >> 1, wi = ((t >> 1) * 10 + (ks >> 1)) * 2 + (t & 1);
      mf_w<wi, ks & 1, true, ks == 0>(acc[t][j & 1], w, xr[ks % 3][j & 1]);
      if constexpr (ks + 2 < 20 && j == 1) { CFENCE(); xr[(ks + 2) % 3][0] = ldsr128(xa + (ks + 2) * 2048); CFENCE(); }
      if constexpr (ks + 2 < 20 && j == NT / 2 + 1) { CFENCE(); xr[(ks + 2) % 3][1] = ldsr128(xa + (ks + 2) * 2048 + 1024); CFENCE(); }
      if constexpr (j == (NT == 4 ? 5 : 0)) mid(K);
    });
  });
}
template <int NT> DEV void acc_wait(f32x4 (&c)[NT][2]) {
  if constexpr (NT == 4) asm volatile("s_nop 7" : "+v"(c[0][0]), "+v"(c[0][1]), "+v"(c[1][0]), "+v"(c[1][1]), "+v"(c[2][0]), "+v"(c[2][1]), "+v"(c[3][0]), "+v"(c[3][1]));
  else asm volatile("s_nop 7" : "+v"(c[0][0]), "+v"(c[0][1]), "+v"(c[1][0]), "+v"(c[1][1]));
}

template <int KIND>
DEV void role_x(const Args& a, rsrc_t rs, int id, int seg0, int dseg, unsigned po, int wave, int lane, unsigned pol) {
  constexpr int NST = KIND == K_QK ? 4 : KIND == K_F1 ? 2 : 10;
  u32x4 w[80];
  auto late = [&]() {
    if constexpr (KIND != K_F1) {
      int budget = SPIN_CAP;
      for (;;) { const unsigned v = ld32(rs, lane * 128, OFF_CTL + CTL_XP); CFENCE(); if (__all(v == 1) || --budget <= 0) break; }
    }
    VMWAIT(0);
    issue_x(a.ws, seg0, L_XB0, wave, lane);
  };
  if constexpr (KIND == K_QK) load_weights<K_QK>(w, a.wq + id * 256, a.wk + id * 256, wave, lane, 15, late);
  else if constexpr (KIND == K_F1) load_weights<K_F1>(w, a.w1 + (id >> 2) * 256, a.w3 + (id >> 2) * 256, wave, lane, id >= 52 ? 7 : 15, late);
  else load_weights<K_VB>(w, a.wv + id * 256, a.wo + (size_t)id * 128 * 1280, wave, lane, 15, late);
  VMWAIT(0);
  EV(KIND == K_F1 ? 4 : 1);
  const unsigned l16 = lane * 16, fvo = lane == 0 ? po : OOB;
  const unsigned svo = (KIND == K_F1 && id > 53) ? OOB : l16, bvo = l16 + (id >> 3) * 16384 + (id & 7) * 1024;
  int it = 0;
  for (int seg = seg0; seg < NSEG; seg += dseg, ++it) {
    const unsigned buf = (it & 1) ? L_XB1 : L_XB0;
    vmwait<NST>();
    BAR();
    const bool more = seg + dseg < NSEG;
    const unsigned char* xsrc = a.ws + OFF_XN + (size_t)(more ? seg + dseg : seg) * 40960 + wave * 10240;
    const unsigned xdst = L_XB0 + L_XB1 - buf + wave * 10240;
    const unsigned char *xs0 = xsrc, *xs1 = xsrc + 4096, *xs2 = xsrc + 8192;
    PINS(xs0); PINS(xs1); PINS(xs2);
    auto mid = [&](auto K) {
      if constexpr (K % 5 == 0 && K <= 10) set_m0(xdst + (K / 5) * 4096);
      if constexpr (K % 5 != 0 && K <= 12) glds_off<(K % 5 - 1) * 1024>(K < 5 ? xs0 : K < 10 ? xs1 : xs2, l16);
      if constexpr (K == 16) { if (it > 1) { vmwait<10>(); st32(rs, it, fvo, 0); } }
    };
    if constexpr (KIND != K_VB) {
      f32x4 acc[4][2];
      xproj<4>(acc, w, buf + l16, mid);
      acc_wait(acc);
      if constexpr (KIND == K_QK) {
#pragma unroll
        for (int m = 0; m < 2; ++m)
#pragma unroll
          for (int tt = 0; tt < 2; ++tt) st128(rs, cat4(pk4(acc[2 * m][tt]), pk4(acc[2 * m + 1][tt])), l16, kq_addr(1 - m, seg, id, pol) + (tt * 4 + wave) * 1024);
      } else {
#pragma unroll
        for (int tt = 0; tt < 2; ++tt) {
          f32x4 hv[2];
#pragma unroll
          for (int t = 0; t < 2; ++t)
#pragma unroll
            for (int r = 0; r < 4; ++r) hv[t][r] = rbf(silu(rbf(acc[t][tt][r]))) * rbf(acc[2 + t][tt][r]);
          st128(rs, cat4(pk4(hv[0]), pk4(hv[1])), svo, OFF_HH + (seg * 54 + id) * 2048 + tt * 1024);
        }
      }
    } else {
      f32x4 acc[2][2];
      xproj<2>(acc, w, buf + l16, mid);
      acc_wait(acc);
#pragma unroll
      for (int tt = 0; tt < 2; ++tt) ldsw128((wave * 2 + tt) * 1024 + l16, cat4(pk4(acc[0][tt]), pk4(acc[1][tt])));
      BAR();
      u32x4 vf[4][2];
#pragma unroll
      for (int ks = 0; ks < 4; ++ks) { vf[ks][0] = ldsr128((ks * 2) * 1024 + l16); vf[ks][1] = ldsr128((ks * 2 + 1) * 1024 + l16); }
      f32x4 c[3][2];
      static_for<12>([&](auto CT) {
        constexpr int ct = CT;
        u32x4 o;
        static_for<4>([&](auto KS) {
          constexpr int ks = KS, wi = 20 + ((ct >> 1) * 2 + (ks >> 1)) * 2 + (ct & 1);
          if constexpr (ct < 10) { mf_w<wi, ks & 1, false, ks == 0>(c[ct % 3][0], w, vf[ks][0]); mf_w<wi, ks & 1, false, ks == 0>(c[ct % 3][1], w, vf[ks][1]); }
          if constexpr (ct >= 2) {
            constexpr int e = (ct - 2) % 3;
            if constexpr (ks == 0) { if constexpr (ct == 11) pin2n(c[e][0], c[e][1]); else pin2(c[e][0], c[e][1]); }
            o[ks] = pk(c[e][ks >> 1][2 * (ks & 1)], c[e][ks >> 1][2 * (ks & 1) + 1]); pin1(o[ks]);
          }
        });
        if constexpr (ct >= 2) { CFENCE(); st128(rs, o, bvo, b_addr(seg, ((ct - 2) >> 1) * 8 + wave * 2 + (ct & 1), pol)); CFENCE(); }
      });
    }
    if (it == 0) { vmwait<0>(); st32(rs, 1, fvo, 0); EV(KIND == K_F1 ? 5 : 2); }
  }
  VMWAIT(0);
  st32(rs, it, fvo, 0);
  EV(KIND == K_F1 ? 6 : 3);
  BAR();
}

constexpr int REPL = 4;
DEV unsigned slot_base(int s, unsigned pol) { return OFF_COMM + (s & 3) * MB + ((pol >> ((s >> 1) & 1)) & 1) * 8192; }
DEV int wmax(int v) {
  v = max(v, __builtin_amdgcn_update_dpp(v, v, 0xB1, 0xf, 0xf, false));
  v = max(v, __builtin_amdgcn_update_dpp(v, v, 0x4E, 0xf, 0xf, false));
  v = max(v, __builtin_amdgcn_update_dpp(v, v, 0x124, 0xf, 0xf, false));
  v = max(v, __builtin_amdgcn_update_dpp(v, v, 0x128, 0xf, 0xf, false));
  return max(max(__builtin_amdgcn_readlane(v, 0), __builtin_amdgcn_readlane(v, 16)), max(__builtin_amdgcn_readlane(v, 32), __builtin_amdgcn_readlane(v, 48)));
}
DEV int wmin(int v) { return -wmax(-v); }
DEV void report(rsrc_t rs, int e, int s, unsigned flags, int lane) {
  unsigned long long v, c;
  asm volatile("s_memrealtime %0\n\ts_memtime %1\n\ts_waitcnt lgkmcnt(0)" : "=s"(v), "=s"(c) :: "memory");
  const u32x2 tm = *(volatile LAS u32x2*)(uintptr_t)(L_TM + (s & 1) * 16);
  st128(rs, (u32x4){(unsigned)s + 1, (unsigned)v << 3, (((unsigned)c - tm.x) << 2) | flags | (tm.y & 1), tm.y >> 2}, lane < 8 ? lane * 16 : OOB, OFF_REP + (s * 128 + e) * 128);
}
DEV void sleep_until_turn(rsrc_t rs, Sched& c, int j, int off) {
  for (;;) {
    adopt_cur(c, ld128(rs, 0, OFF_CTL + CTL_CUR), j + 1); CFENCE();
    const int d = (int)(tau_of(c, j) + off - now8());
    if (c.j0 == 0 || d > 200 * MAXWAIT) return;
    if (d < 150 * 8) { wait_until(tau_of(c, j) + off); return; }
    if (d > 500 * 8) __builtin_amdgcn_s_sleep(127); else __builtin_amdgcn_s_sleep(40);
  }
}
DEV int probe_seg(int s, int lane) { return min(s + (3 << (lane & 3)), NSEG - 1); }
DEV unsigned probe_ld(rsrc_t rs, int s, int lane) { return ld32(rs, lane < 4 ? probe_seg(s, lane) * 128 : OOB, OFF_CTL + CTL_RL); }
DEV int probe_rdy(unsigned v, int s, int lane) { return wmax(v != 0 ? probe_seg(s, lane) + 1 : 0); }
DEV void role_agg(rsrc_t rs, int lane) {
  int done = 0, budget = SPIN_CAP;
  while (done < NSEG && --budget > 0) {
    int m = NSEG;
#pragma unroll
    for (int i = 0; i < 8; ++i) m = min(m, 2 * (int)ld32(rs, (i * 64 + lane) * 128, OFF_CTL) + ((lane >> 2) & 1));
    CFENCE();
    m = min(wmin(m), NSEG);
    if (m > done) { st32(rs, 1, (lane >= done && lane < m) ? lane * 128 : OOB, OFF_CTL + CTL_RL); done = m; }
    __builtin_amdgcn_s_sleep(16);
  }
}
DEV unsigned rep_off(int j, int lane) { return ((j >> 1) * 128 + ((j & 1) ? 64 + min(lane, 39) : lane)) * 128; }
#define PTSLEEP 2
#define HOLDP 720
#define HOLDC 1080
#define NOPM_(n) asm volatile("s_nop " #n ::: "memory")
#define NOPM(n) NOPM_(n)
#define VSLACK 48
#define PACE 11
#define NOPV(n, x) asm volatile("s_nop " #n : "+v"(x))
template <int N> DEV void zero_acc(f32x4 (&c)[N]) {
#pragma unroll
  for (int t = 0; t < N; ++t) { c[t] = (f32x4){0, 0, 0, 0}; PIN(c[t]); }
}
DEV unsigned memoff(int j) { return j < 8 ? j * 1024 : C_MEM1 + (j - 8) * 1024; }
DEV bool has_nan(f32x4 v) { return __any(__builtin_isunordered(v[0], v[1]) || __builtin_isunordered(v[2], v[3])); }
template <int N> DEV bool has_sent(const u32x4 (&a)[N]) {
  unsigned m = 0;
#pragma unroll
  for (int k = 0; k < N; ++k) m = max(m, mx4(a[k]));
  return __any(m == SENT);
}
DEV int mrow(int i, int half) { return 4 * (i >> 1) + 2 * half + (i & 1); }
DEV unsigned foldpk(f32x4 v) { return pk(v[0] + v[2], v[1] + v[3]); }
DEV void ldsw32(unsigned a, unsigned v) { *(LAS unsigned*)(uintptr_t)a = v; }
DEV void mem_wait(rsrc_t rs, unsigned sb, int lane, int& budget) {
  for (;;) {
    const unsigned u = ld32(rs, lane * 128, sb), v = ld32(rs, C_MEM1 + (lane & 15) * 128, sb);
    CFENCE();
    if (__all(u != SENT && v != SENT) || --budget <= 0) break;
  }
}
template <int NT, int NK>
DEV void project(rsrc_t rs, const unsigned (&vo)[10], unsigned sb, const u32x8 (&w)[40], int idx, u32x4 (&am)[10], f32x4 (&acc)[NT]) {
#pragma unroll
  for (int k = 0; k < 8; ++k) {
    am[k] = ld128(rs, vo[k], sb);
    if (k < 7) NOPM(PACE); else CFENCE();
  }
  static_for<NK>([&](auto K) {
    constexpr int ks = K;
    smf_w<ks * 2>(acc[0], am[ks], w, idx); smf_w<ks * 2 + 1>(acc[1], am[ks], w, idx);
    if constexpr (ks < 2) { CFENCE(); am[8 + ks] = ld128(rs, vo[8 + ks], sb); CFENCE(); }
    if constexpr (NT == 4) { smf_w<20 + ks * 2>(acc[2], am[ks], w, idx); smf_w<20 + ks * 2 + 1>(acc[3], am[ks], w, idx); }
  });
}
#define DPL " row_ror:8 row_mask:0xf bank_mask:0x3\n\t"
#define DPH " row_ror:8 row_mask:0xf bank_mask:0xc\n\t"
#define DPA " quad_perm:[2,3,0,1] row_mask:0xf bank_mask:0xf\n\t"
#define FADD2(d0, d1, c) asm volatile("v_add_f32 %0, %2, %4\n\tv_add_f32 %1, %3, %5" : "=&v"(d0), "=&v"(d1) : "v"(c[0]), "v"(c[1]), "v"(c[2]), "v"(c[3]))

constexpr unsigned P_RING = 16384;
#define ROWRED(op) "s_nop 1\n\t" op "_dpp %7, %6, %6" DPA op "_dpp %8, %6, %6" DPA "s_nop 1\n\tv_permlane16_swap_b32 %7, %8\n\t" op " %6, %7, %8\n\t" op " %7, %7, %8\n\t" \
                   "s_nop 1\n\tv_permlane32_swap_b32 %6, %7\n\t" op " %6, %6, %7\n\t"
#define SM_SELM "v_cndmask_b32 %4, %24, %13, %26\n\tv_cndmask_b32 %5, %25, %14, %26\n\t"
#define SM_SELX "v_cndmask_b32 %0, %20, %15, %26\n\tv_cndmask_b32 %1, %21, %16, %26\n\tv_cndmask_b32 %2, %22, %17, %26\n\tv_cndmask_b32 %3, %23, %18, %26\n\t"
#define SM_EXPM "v_fma_f32 %4, %4, %19, -%12\n\tv_fma_f32 %5, %5, %19, -%12\n\tv_exp_f32 %4, %4\n\tv_exp_f32 %5, %5\n\t"
#define SM_EXPX "v_fma_f32 %0, %0, %19, -%12\n\tv_fma_f32 %1, %1, %19, -%12\n\tv_exp_f32 %0, %0\n\tv_fma_f32 %2, %2, %19, -%12\n\tv_exp_f32 %1, %1\n\tv_fma_f32 %3, %3, %19, -%12\n\tv_exp_f32 %2, %2\n\t" \
                "v_add_f32 %6, %4, %5\n\tv_exp_f32 %3, %3\n\tv_add_f32 %7, %0, %1\n\tv_add_f32 %6, %6, %2\n\tv_add_f32 %7, %7, %3\n\tv_add_f32 %6, %6, %7\n\t"
#define SM_OUT "v_rcp_f32 %7, %6\n\tv_med3_f32 %8, %6, %27, %28\n\t" \
               "v_mul_f32 %0, %0, %7\n\tv_mul_f32 %1, %1, %7\n\tv_cmp_eq_f32 %11, %8, %6\n\tv_mul_f32 %2, %2, %7\n\tv_mul_f32 %3, %3, %7\n\t" \
               "v_cvt_pk_bf16_f32 v248, |%0|, |%1|\n\tv_cvt_pk_bf16_f32 v250, |%0|, |%1|\n\tv_cvt_pk_bf16_f32 v249, |%2|, |%3|\n\tv_cvt_pk_bf16_f32 v251, |%2|, |%3|\n\t" \
               "v_permlane32_swap_b32 v248, v250\n\tv_mul_f32 %4, %4, %7\n\tv_permlane32_swap_b32 v249, v251\n\tv_mul_f32 %5, %5, %7"
#define SM_OPS : "=&v"(x0), "=&v"(x1), "=&v"(x2), "=&v"(x3), "=&v"(y0), "=&v"(y1), "=&v"(sum), "=&v"(t1), "=&v"(t2), "=&{v[248:251]}"(ox), "=&v"(t3), "=&s"(okm), "+v"(mref) \
               : "v"(s0[2]), "v"(s0[3]), "v"(s2[0]), "v"(s2[1]), "v"(s2[2]), "v"(s2[3]), "s"(cs), "v"(s1[0]), "v"(s1[1]), "v"(s1[2]), "v"(s1[3]), "v"(s0[0]), "v"(s0[1]), \
                 "s"(0xCCCCCCCCCCCCCCCCull), "v"(lo), "v"(hi)
template <int CLS>
DEV void p_loop(const Args& a, rsrc_t rs, int h, int wave, int lane, unsigned pol, const u32x8 (&w)[40], Sched& sc) {
  const int n = lane & 15, g = lane >> 4;
  const int idx = (lane & 2) ? 0xEEEE : 0x4444;
  const float NEG = -3.0e38f;
  const unsigned l16 = lane * 16, cpy = (h & (REPL - 1)) * 65536;
  unsigned vo[10];
#pragma unroll
  for (int k = 0; k < 10; ++k) { vo[k] = l16 + memoff(k); PIN(vo[k]); }
  f32x4 i1, i2;
#pragma unroll
  for (int r = 0; r < 4; ++r) {
    const bool tri = (4 * g + r) <= n;
    i1[r] = (wave == 1 && !tri) ? NEG : 0.f;
    i2[r] = (wave == 1 || (wave == 2 && !tri)) ? NEG : 0.f;
  }
  unsigned kvo[2];
#pragma unroll
  for (int tl = 0; tl < 2; ++tl) {
    int tok = 16 * tl + n;
    if (CLS == 0) { const int gp = n >> 2, r = n & 3; tok = 16 * (r >> 1) + 8 * (gp & 1) + 4 * (gp >> 1) + 2 * tl + (r & 1); }
    kvo[tl] = P_RING + (tok >> 4) * 4096 + (g * 16 + (tok & 15)) * 16;
  }
  const unsigned qvo = P_RING + 8192 + (wave == 2 ? 4096 : 0) + l16;
  unsigned wra = (32 * wave + n) * 16 + g * 4, wrq = 2 * wra;
  unsigned tra = (4 * g + ((lane >> 2) & 3)) * 16 + (lane & 1) * 8, trq = (4 * g + ((lane >> 2) & 3)) * 32 + (lane & 3) * 8;
  PIN(wra); PIN(wrq); PIN(tra); PIN(trq);
  unsigned vox[2], vom[2];
#pragma unroll
  for (int c = 0; c < 2; ++c) {
    if (CLS == 0) {
      vox[c] = (2 * c + (lane >> 5)) * 65536 + C_PX + (h >> 4) * 16384 + (h & 15) * 512 + (lane & 31) * 16;
      vom[c] = lane < 32 ? (2 * c + (lane >> 4)) * 65536 + C_PM + h * 128 + (lane & 15) * 8 : OOB;
      PIN(vox[c]); PIN(vom[c]);
    } else { vox[c] = (h * 2 + (wave - 1)) * 1024 + l16; vom[c] = lane < 16 ? 65536 + (wave - 1) * 8192 + h * 256 + l16 : OOB; }
  }
  const unsigned pvo = prog_off(0, h, (lane >> 2) & 1, lane & 3);
  const float cs = 0.08838834764831845f * 1.4426950408889634f;
  float mref = 0.f, lo = 0x1p-64f, hi = 0x1p64f;
  PIN(lo); PIN(hi);
  int budget = SPIN_CAP, rdy = 0;
  auto fetch = [&](int seg) {
    while (rdy <= seg && --budget > 0) { rdy = wmin(2 * (int)ld32(rs, pvo, 0) + ((lane >> 2) & 1)); CFENCE(); }
    static_for<4>([&](auto J) {
      const unsigned char* src = a.ws + kq_addr(J >> 1, seg, h, pol) + (J & 1) * 4096;
      set_m0(P_RING + (seg & 1) * 16384 + J * 4096); NOP(0);
      glds_off<0>(src, l16); glds_off<1024>(src, l16); glds_off<2048>(src, l16); glds_off<3072>(src, l16);
    });
  };
  u32x4 kx[2][4], qf[4];
  auto pull = [&](int seg) {
    const unsigned rb = (seg & 1) * 16384;
#pragma unroll
    for (int ks = 0; ks < 4; ++ks) { kx[0][ks] = ldsr128(kvo[0] + rb + ks * 1024); kx[1][ks] = ldsr128(kvo[1] + rb + ks * 1024); if (CLS == 1) qf[ks] = ldsr128(qvo + rb + ks * 1024); }
  };
  if (CLS == 2) { fetch(0); VMWAIT(0); }
  BAR();
  EV(2);
  if (CLS == 2) fetch(1); else pull(0);
  unsigned hold = now8(), pend = 0;
  for (int s = 0; s < NSEG; ++s) {
    unsigned sb = slot_base(s, pol), sbr = sb + cpy;
    const float n0 = g >= 2 ? NEG : 0.f;
    f32x4 s0 = {n0, n0, n0, n0}, s1 = i1, s2 = i2;
    PINS(sb); PINS(sbr);
    if (CLS < 2) {
      PIN(s0); PIN(s1); PIN(s2);
#pragma unroll
      for (int ks = 0; ks < 4; ++ks) { PIN(kx[0][ks]); PIN(kx[1][ks]); }
    }
    if (CLS == 1) {
      NOP(1);
#pragma unroll
      for (int ks = 0; ks < 4; ++ks) { mf_vv(s1, kx[0][ks], qf[ks]); mf_vv(s2, kx[1][ks], qf[ks]); }
    }
    unsigned cst = 0;
    if (CLS == 2) cst = trigger(rs, sc, 2 * s, hold, [&]() { mem_wait(rs, sbr, lane, budget); });
    f32x4 acc[4];
    zero_acc(acc);
    STAMP_DECL;
    unsigned good = 2;
    if (CLS == 2) release(cst); else BAR();
    STAMP(0);
    for (;;) {
      u32x4 am[10];
      project<4, 8>(rs, vo, sbr, w, idx, am, acc);
      float a0, a1, b0, b1;
      smf_w<16>(acc[0], am[8], w, idx); smf_w<17>(acc[1], am[8], w, idx); smf_w<18>(acc[0], am[9], w, idx); smf_w<19>(acc[1], am[9], w, idx);
      smf_w<36>(acc[2], am[8], w, idx); smf_w<37>(acc[3], am[8], w, idx); smf_w<38>(acc[2], am[9], w, idx);
      FADD2(a0, a1, acc[0]);
      smf_w<39>(acc[3], am[9], w, idx);
      FADD2(b0, b1, acc[1]);
      STAMP(1);
      asm volatile("ds_write2_b32 %0, %1, %1 offset0:0 offset1:1" :: "v"(wrq), "v"(pk(a0, a1)) : "memory");
      asm volatile("ds_write2_b32 %0, %1, %1 offset0:128 offset1:129" :: "v"(wrq), "v"(pk(b0, b1)) : "memory");
      const bool bad = __any(__builtin_isunordered(a0, a1));
      PIN(acc[2]);
      const unsigned f2 = foldpk(acc[2]);
      pin1(f2);
      PIN(acc[3]);
      ldsw32(wra + 4096, f2); ldsw32(wra + 4096 + 256, foldpk(acc[3]));
      if (__builtin_expect(!bad, 1)) break;
      if ((!(good & 2) && !has_sent(am)) || --budget <= 0) break;
      good = 0;
      zero_acc(acc);
      mem_wait(rs, sbr, lane, budget);
    }
    BAR();
    STAMP(2);
    u32x4 km[4];
#define TRQ(ks) qf[ks] = cat4(trread(trq + (ks) * 1024), trread(trq + (ks) * 1024 + 512))
#define TRK(ks) km[ks] = cat4(trread(tra + 4096 + (ks) * 512), trread(tra + 4096 + (ks) * 512 + 256))
    if (CLS == 0) {
      TRQ(0); TRQ(1); TRK(0); TRK(1); CFENCE();
      mf_vv(s1, kx[0][0], qf[0]); CFENCE(); TRQ(2); CFENCE(); mf_vv(s2, kx[1][0], qf[0]); CFENCE(); TRK(2); CFENCE();
      mf_vv(s1, kx[0][1], qf[1]); CFENCE(); TRQ(3); CFENCE(); mf_vv(s2, kx[1][1], qf[1]); CFENCE(); TRK(3); CFENCE();
      mf_vv(s0, km[0], qf[0]); mf_vv(s0, km[1], qf[1]); mf_vv(s0, km[2], qf[2]); mf_vv(s0, km[3], qf[3]);
      mf_vv(s1, kx[0][2], qf[2]); mf_vv(s2, kx[1][2], qf[2]); mf_vv(s1, kx[0][3], qf[3]); mf_vv(s2, kx[1][3], qf[3]);
      STAMP(3);
      float x0, x1, x2, x3, y0, y1, sum, t1, t2, t3;
      u32x4 ox; u32x2 om; unsigned long long okm;
      asm volatile(SM_SELM SM_EXPM "s_nop 1\n\t" SM_SELX SM_EXPX ROWRED("v_add_f32") SM_OUT SM_OPS);
      if (__builtin_expect(okm != ~0ull, 0))
        asm volatile(SM_SELM SM_SELX "v_max3_f32 %6, %4, %5, %0\n\tv_max3_f32 %7, %1, %2, %3\n\tv_max_f32 %6, %6, %7\n\t" ROWRED("v_max_f32") "v_mul_f32 %12, %19, %6\n\t"
                     SM_EXPM SM_EXPX ROWRED("v_add_f32") SM_OUT SM_OPS);
      st128(rs, ox, vox[0], sb); st128(rs, ox, vox[1], sb);
      asm volatile("v_cvt_pk_bf16_f32 v252, |%1|, |%2|\n\tv_cvt_pk_bf16_f32 v253, |%1|, |%2|\n\ts_nop 1\n\tv_permlane16_swap_b32 v252, v253" : "=&{v[252:253]}"(om) : "v"(y0), "v"(y1) : "memory");
      st64(rs, om, vom[0], sb); st64(rs, om, vom[1], sb);
      CFENCE();
      STAMP(4);
      STAMP_OUT(h, s);
      report(rs, h, s, good, lane);
      mref += __builtin_amdgcn_logf(sum);
      if (s >= 1) {
        const unsigned rb = slot_base(s - 1, pol);
#pragma unroll
        for (int c = 0; c < 2; ++c) { st128(rs, (u32x4){SENT, SENT, SENT, SENT}, vox[c], rb); st64(rs, (u32x2){SENT, SENT}, vom[c], rb); }
      }
      if (s + 1 < NSEG) pull(s + 1);
    } else if (CLS == 1) {
      TRK(0); TRK(1); TRK(2); TRK(3);
#pragma unroll
      for (int ks = 0; ks < 4; ++ks) mf_vv(s0, km[ks], qf[ks]);
      asm volatile("s_nop 7" : "+v"(s0), "+v"(s1), "+v"(s2));
      float mxv = fmaxf(fmaxf(fmaxf(s1[0], s1[1]), fmaxf(s1[2], s1[3])), fmaxf(fmaxf(s2[0], s2[1]), fmaxf(s2[2], s2[3])));
      mxv = fmaxf(fmaxf(fmaxf(s0[0], s0[1]), fmaxf(s0[2], s0[3])), mxv);
      mxv = xmax32(xmax16(mxv));
      const float mb = -mxv * cs;
      float sum = 0.f;
#pragma unroll
      for (int r = 0; r < 4; ++r) {
        s0[r] = __builtin_amdgcn_exp2f(fmaf(s0[r], cs, mb)); s1[r] = __builtin_amdgcn_exp2f(fmaf(s1[r], cs, mb)); s2[r] = __builtin_amdgcn_exp2f(fmaf(s2[r], cs, mb));
        sum += s0[r] + s1[r] + s2[r];
      }
      sum = xadd32(xadd16(sum));
      const float inv = __builtin_amdgcn_rcpf(sum);
      s0 *= inv; s1 *= inv; s2 *= inv;
      const unsigned so = OFF_PT + s * 81920;
      const u32x2 pmv = pk4(s0);
      auto r0 = __builtin_amdgcn_permlane16_swap(pmv.x, pmv.x, false, false);
      auto r1 = __builtin_amdgcn_permlane16_swap(pmv.y, pmv.y, false, false);
      const u32x4 ptx = cat4(pk4(s1), pk4(s2));
      if (s + 1 < NSEG) { pull(s + 1); __builtin_amdgcn_s_sleep(PTSLEEP); }
      st128(rs, ptx, vox[0], so);
      st128(rs, (u32x4){(unsigned)r0[0], (unsigned)r1[0], (unsigned)r0[1], (unsigned)r1[1]}, vom[0], so);
      STAMP(4);
      STAMP_OUT(h, s);
    } else {
      const unsigned tend = now8() + 24 * 8;
      mark_overrun(sc, 2 * s, tend);
      const u32x4 cur = ld128(rs, 0, OFF_CTL + CTL_CUR);
      rdy = max(rdy, probe_rdy(pend, s, lane));
      adopt_cur(sc, cur, 2 * s + 2);
      if (s + 2 < NSEG) fetch(s + 2);
      pend = 0;
      if (rdy <= s + 5) pend = probe_ld(rs, s + 1, lane);
      STAMP_OUT(h, s);
      hold = tend + HOLDP; clk_update(sc);
    }
  }
  VMWAIT(0);
  BAR();
  if (CLS == 2) st128(rs, (u32x4){1, 1, 1, 1}, lane < 8 ? lane * 16 : OOB, OFF_REP + (NSEG * 128 + h) * 128);
  EV(3);
}
DEV void role_p(const Args& a, rsrc_t rs, int h, int wave, int lane, unsigned pol) {
  u32x8 w[40];
  Sched sc; clk_init(sc);
  load_weights<K_P>(w, a.wq + h * 256, a.wk + h * 256, wave, lane, 15, []() { VMWAIT(0); });
  BAR();
  EV(1);
  if (wave == 0) p_loop<0>(a, rs, h, wave, lane, pol, w, sc);
  else if (wave < 3) p_loop<1>(a, rs, h, wave, lane, pol, w, sc);
  else p_loop<2>(a, rs, h, wave, lane, pol, w, sc);
}

DEV void role_v(const Args& a, rsrc_t rs, int h, int wave, int lane, unsigned pol) {
  u32x8 w[40];
  Sched sc; clk_init(sc);
  load_weights<K_V>(w, a.wv + h * 256, a.wo + (size_t)h * 128 * 1280, wave, lane, 15, []() { VMWAIT(0); });
  BAR();
  EV(1);
  const int n = lane & 15, g = lane >> 4;
  const int idx = (lane & 2) ? 0xEEEE : 0x4444;
  unsigned wra = (16 * wave + (n >> 2) * 2 + (n & 1)) * 32 + g * 8 + ((n >> 1) & 1) * 4, tra = (8 * g + ((lane >> 2) & 3)) * 32 + (lane & 3) * 8;
  unsigned zv0 = g < 2 ? (wave * 2 + g) * 16384 + h * 256 + n * 16 : OOB;
  unsigned zv1 = (8 + (g >> 1) * 8 + wave * 2 + (g & 1)) * 16384 + h * 256 + n * 16, zv2 = zv1 + 16 * 16384;
  PIN(wra); PIN(tra); PIN(zv0); PIN(zv1); PIN(zv2);
  unsigned vo[10];
#pragma unroll
  for (int k = 0; k < 10; ++k) { vo[k] = lane * 16 + memoff(k); PIN(vo[k]); }
  int budget = SPIN_CAP;
  unsigned hold = now8();
  for (int s = 0; s < NSEG; ++s) {
    unsigned sb = slot_base(s, pol) + (h & (REPL - 1)) * 65536, zb = z_base(s, pol);
    PINS(sb); PINS(zb);
    f32x4 c[10], acc[2];
    zero_acc(c); zero_acc(acc);
    unsigned cst;
    if (wave == 3) cst = trigger(rs, sc, 2 * s, hold, [&]() { mem_wait(rs, sb, lane, budget); }); else cst = cycles();
    STAMP_DECL;
    unsigned good = 2;
    release(__builtin_amdgcn_readfirstlane(cst));
    STAMP(0);
    for (;;) {
      u32x4 am[10];
      project<2, 10>(rs, vo, sb, w, idx, am, acc);
      STAMP(1);
      asm volatile("s_nop 7" : "+v"(acc[0]), "+v"(acc[1]));
      ldsw32(wra, foldpk(acc[0])); ldsw32(wra + 256, foldpk(acc[1]));
      if (__builtin_expect(!has_nan(acc[0]), 1)) break;
      if ((!(good & 2) && !has_sent(am)) || --budget <= 0) break;
      good = 0;
      zero_acc(acc);
      mem_wait(rs, sb, lane, budget);
    }
    BAR();
    STAMP(2);
    u32x4 va[2];
#pragma unroll
    for (int ks = 0; ks < 2; ++ks) va[ks] = cat4(trread(tra + ks * 1024), trread(tra + ks * 1024 + 128));
    CFENCE(); const u32x4 cur = ld128(rs, 0, OFF_CTL + CTL_CUR); CFENCE();
    u32x4 O0, O1, O2;
#define VMM(t, k) smf_w<20 + ((t) >> 1) * 4 + 2 * (k) + ((t) & 1)>(c[t], va[k], w, idx)
#define VEA(t, T) FADD2(T##0, T##1, c[t])
#define VEC2(R, ra, rb, O) asm volatile("v_cvt_pk_bf16_f32 " ra ", %1, %2\n\tv_cvt_pk_bf16_f32 " rb ", %3, %4" : "+{" R "}"(O) : "v"(ta0), "v"(ta1), "v"(tb0), "v"(tb1))
#define VSW(txt, R, O) asm volatile(txt : "+{" R "}"(O))
    float ta0, ta1, tb0, tb1;
    asm volatile("" : "={v[232:235]}"(O0), "={v[236:239]}"(O1), "={v[240:243]}"(O2));
    VMM(0, 0); VMM(1, 0); VMM(0, 1); VMM(1, 1);
    VMM(2, 0); VMM(3, 0); VMM(2, 1); VMM(3, 1);
    VMM(4, 0); NOP(1); VMM(5, 0); VEA(0, ta); VMM(4, 1); VEA(1, tb); VMM(5, 1); VEC2("v[232:235]", "v232", "v233", O0);
    VMM(6, 0); VEA(2, ta); VMM(7, 0); VEA(3, tb); VMM(6, 1); VSW("v_permlane16_swap_b32 v232, v233", "v[232:235]", O0); VMM(7, 1); VEC2("v[236:239]", "v236", "v237", O1);
    VMM(8, 0); VSW("v_permlane32_swap_b32 v232, v234", "v[232:235]", O0); VMM(9, 0); VSW("v_permlane32_swap_b32 v233, v235", "v[232:235]", O0);
    VMM(8, 1); VEA(4, ta); VMM(9, 1); CFENCE(); st128(rs, O0, zv0, zb); CFENCE(); VEA(5, tb);
    VEC2("v[236:239]", "v238", "v239", O1);
    VEA(6, ta); VEA(7, tb);
    VSW("v_permlane32_swap_b32 v236, v238\n\tv_permlane32_swap_b32 v237, v239", "v[236:239]", O1);
    VEC2("v[240:243]", "v240", "v241", O2);
    VSW("v_permlane16_swap_b32 v236, v237\n\tv_permlane16_swap_b32 v238, v239", "v[236:239]", O1);
    CFENCE(); st128(rs, O1, zv1, zb); CFENCE();
    VEA(8, ta); VEA(9, tb); VEC2("v[240:243]", "v242", "v243", O2);
    VSW("s_nop 0\n\tv_permlane32_swap_b32 v240, v242\n\tv_permlane32_swap_b32 v241, v243\n\ts_nop 1\n\tv_permlane16_swap_b32 v240, v241\n\tv_permlane16_swap_b32 v242, v243", "v[240:243]", O2);
    CFENCE(); st128(rs, O2, zv2, zb);
    CFENCE();
    STAMP(4);
    STAMP_OUT(32 + h, s);
    if (wave == 0) report(rs, 32 + h, s, good, lane);
    if (wave == 3) { const unsigned tend = now8(); hold = tend + HOLDP; mark_overrun(sc, 2 * s, tend - VSLACK); clk_update(sc); adopt_cur(sc, cur, 2 * s + 2); }
  }
  VMWAIT(0);
  BAR();
  if (wave == 0) st128(rs, (u32x4){1, 1, 1, 1}, lane < 8 ? lane * 16 : OOB, OFF_REP + (NSEG * 128 + 32 + h) * 128);
}

#define CLOB_A "a0", "a1", "a2", "a3", "a4", "a5", "a6", "a7", "a8", "a9", "a10", "a11", "a12", "a13", "a14", "a15", "a16", "a17", "a18", "a19", "a20", "a21", "a22", "a23", "a24", "a25", "a26", "a27", "a28", "a29", "a30", "a31", "a32", "a33", "a34", "a35", "a36", "a37", "a38", "a39", "a40", "a41", "a42", "a43", "a44", "a45", "a46", "a47", "a48", "a49", "a50", "a51", "a52", "a53", "a54", "a55", "a56", "a57", "a58", "a59", "a60", "a61", "a62", "a63", "a64", "a65", "a66", "a67", "a68", "a69", "a70", "a71", "a72", "a73", "a74", "a75", "a76", "a77", "a78", "a79", "a80", "a81", "a82", "a83", "a84", "a85", "a86", "a87", "a88", "a89", "a90", "a91", "a92", "a93", "a94", "a95", "a96", "a97", "a98", "a99", "a100", "a101", "a102", "a103", "a104", "a105", "a106", "a107", "a108", "a109", "a110", "a111", "a112", "a113", "a114", "a115", "a116", "a117", "a118", "a119", "a120", "a121", "a122", "a123", "a124", "a125", "a126", "a127"
template <int J> DEV void c_ldb(unsigned l16, const unsigned char* src) {
  asm volatile("global_load_dwordx4 a[%c2:%c2+3], %0, %1 sc1\n\tglobal_load_dwordx4 a[%c2+4:%c2+7], %0, %1 offset:1024 sc1\n\t"
               "global_load_dwordx4 a[%c2+8:%c2+11], %0, %1 offset:2048 sc1\n\tglobal_load_dwordx4 a[%c2+12:%c2+15], %0, %1 offset:3072 sc1"
               :: "v"(l16), "s"(src + (J >> 1) * 16384 + (J & 1) * 4096), "n"(J * 16) : "memory", CLOB_A);
}
template <int J> DEV void c_smf(f32x4& acc, u32x4 p, int idx) { asm volatile("v_smfmac_f32_16x16x64_bf16 %0, %1, a[%c3:%c3+7], %2" : "+{v[248:251]}"(acc) : "v"(p), "v"(idx), "n"(J * 8)); }
#define ZFIRST 0
#define BDELAY 30
DEV void c_arm(rsrc_t rs, int s, int cb, int lane, unsigned pol) {
  const unsigned zn = z_base(s, pol) + cb * 16384;
#pragma unroll
  for (int j = 0; j < 8; ++j) st128(rs, (u32x4){SENT, SENT, SENT, SENT}, lane * 16 + (j & 3) * 1024, zn + (j >> 2) * 4096);
}
DEV void role_c(const Args& a, rsrc_t rs, int cb, int wave, int lane, unsigned pol) {
  Sched sc; clk_init(sc);
  c_arm(rs, wave, cb, lane, pol);
  VMWAIT(0);
  BAR();
  const unsigned pvo = prog_off(1, lane >> 1, lane & 1, (cb >> 1) & 3);
  if (wave == 3) {
    int rdy = 0;
    for (int s = 0; s < NSEG - 1; ++s) {
      BAR(); BAR();
      unsigned v = 0;
      if (rdy <= s + 4) v = probe_ld(rs, s, lane);
      BAR();
      rdy = max(rdy, probe_rdy(v, s, lane));
      ldsw32(L_TM + 32, rdy);
    }
    return;
  }
  if (wave == 2) {
    const unsigned rvo = (lane >> 4) < REPL ? (lane >> 4) * 65536 + (cb < 32 ? cb * 256 : C_MEM1 + (cb - 32) * 256) + (lane & 15) * 16 : OOB;
    for (int s = 0; s < NSEG - 1; ++s) {
      BAR(); BAR(); VMWAIT(0); BAR();
      st128(rs, (u32x4){SENT, SENT, SENT, SENT}, rvo, slot_base(s, pol));
      if (s + 4 < NSEG) c_arm(rs, s + 4, cb, lane, pol);
    }
    return;
  }
  int budget = SPIN_CAP;
  const unsigned cpy = ((cb / 10) & (REPL - 1)) * 65536;
  auto c_wait = [&](unsigned sb, unsigned zb) {
    for (;;) {
      const unsigned u = ld32(rs, C_PM + (lane & 31) * 128, sb), v = ld32(rs, cb * 16384 + lane * 128, zb);
      CFENCE();
      if (__all(u != SENT && v != SENT) || --budget <= 0) break;
    }
  };
  if (wave == 1) {
    unsigned hold = now8();
    for (int s = 0; s < NSEG - 1; ++s) {
      release(trigger(rs, sc, 2 * s + 1, hold, [&]() { c_wait(slot_base(s, pol) + cpy, z_base(s, pol)); }));
      clk_update(sc);
      BAR();
      hold = now8();
      mark_overrun(sc, 2 * s + 1, hold);
      hold += HOLDC;
      adopt_cur(sc, ld128(rs, 0, OFF_CTL + CTL_CUR), 2 * s + 3);
      if (sc.j0 > sc.jbad) wait_until(tau_of(sc, 2 * s + 2) + BDELAY * 8);
      BAR();
    }
    return;
  }
  const int n = lane & 15, g = lane >> 4;
  const int idx = (lane & 2) ? 0xEEEE : 0x4444;
  const unsigned l16 = lane * 16;
  unsigned pxvo[4], zvo[2], pmvo = C_PM + 2 * g * 128 + n * 8;
#pragma unroll
  for (int i = 0; i < 4; ++i) { pxvo[i] = C_PX + (i >> 1) * 16384 + (i & 1) * 4096 + l16; PIN(pxvo[i]); }
#pragma unroll
  for (int i = 0; i < 2; ++i) { zvo[i] = cb * 16384 + i * 4096 + l16; PIN(zvo[i]); }
  const unsigned mvo = (cb < 32 ? cb * 256 : C_MEM1 + (cb - 32) * 256) + mrow(2 * g + (n >> 3), (n & 7) >> 2) * 16 + (n & 3) * 4;
  unsigned oob = OOB; PIN(oob); PIN(pmvo);
  {
    const unsigned short* m0 = (const unsigned short*)a.mem0 + (2 * g + (n >> 3)) * 640 + cb * 16 + (n & 7);
    const unsigned v = min(m0[0] | (unsigned)m0[8] << 16, 0xFFFFFFFEu);
#pragma unroll
    for (int c = 0; c < REPL; ++c) st32(rs, v, mvo, slot_base(0, pol) + c * 65536);
  }
  int rdy = 0;
  auto fetch = [&](int seg) {
    while (rdy <= seg && --budget > 0) { rdy = wmin(2 * (int)ld32(rs, pvo, 0) + (lane & 1)); CFENCE(); }
    const unsigned char* src = a.ws + b_addr(seg, cb, pol);
    static_for<8>([&](auto J) { c_ldb<J>(l16, src); });
  };
  fetch(0);
  for (int s = 0; s < NSEG - 1; ++s) {
    unsigned sb = slot_base(s, pol) + cpy, zb = z_base(s, pol), sbn[REPL];
#pragma unroll
    for (int c = 0; c < REPL; ++c) { sbn[c] = slot_base(s + 1, pol) + c * 65536; PINS(sbn[c]); }
    PINS(sb); PINS(zb);
    STAMP_DECL;
    unsigned good = 2;
    BAR();
    STAMP(0);
    for (;;) {
      u32x4 z[8], pm[4], px[16];
#pragma unroll
      for (int j = 0; j < ZFIRST; ++j) { z[j] = ld128(rs, zvo[j >> 2] + (j & 3) * 1024, zb); CFENCE(); }
#pragma unroll
      for (int j = 0; j < 16; ++j) px[j] = ld128(rs, pxvo[j >> 2] + (j & 3) * 1024, sb);
      CFENCE();
#pragma unroll
      for (int j = 0; j < 4; ++j) pm[j] = cat4(ld64(rs, pmvo + j * 1024, sb), ld64(rs, pmvo + j * 1024 + 128, sb));
      CFENCE();
#pragma unroll
      for (int j = ZFIRST; j < 8; ++j) { z[j] = ld128(rs, zvo[j >> 2] + (j & 3) * 1024, zb); CFENCE(); }
      f32x4 acc = {0, 0, 0, 0};
      asm volatile("s_nop 1" : "+{v[248:251]}"(acc));
      static_for<16>([&](auto J) { c_smf<J>(acc, px[J], idx); });
#pragma unroll
      for (int j = 0; j < 4; ++j) asm volatile("v_smfmac_f32_16x16x64_bf16 %0, %1, %2, %3" : "+{v[248:251]}"(acc) : "v"(pm[j]), "v"(cat8(z[2 * j], z[2 * j + 1])), "v"(idx));
      STAMP(1);
      unsigned o, vo2; unsigned long long okm;
      asm volatile(
        "s_nop 7\n\t"
        "v_add_f32 v248, v248, v250\n\t"
        "v_add_f32 v250, v249, v251\n\t"
        "v_add_f32 v249, v249, v251\n\t"
        "v_mov_b32_dpp v250, v248" DPL
        "s_nop 0\n\t"
        "v_mov_b32_dpp v248, v249" DPH
        "v_cmp_o_f32 %3, v248, v250\n\t"
        "v_cvt_pk_bf16_f32 %1, v248, v250\n\t"
        "s_nop 0\n\t"
        "v_cndmask_b32 %2, %5, %4, %3"
        : "+{v[248:251]}"(acc), "=&v"(o), "=&v"(vo2), "=&s"(okm) : "v"(mvo), "v"(oob));
#pragma unroll
      for (int c = 0; c < REPL; ++c) st32(rs, o, vo2, sbn[c]);
      CFENCE();
      if (__builtin_expect(okm == ~0ull, 1)) break;
      if ((!(good & 2) && !(has_sent(z) || has_sent(pm) || has_sent(px))) || --budget <= 0) {
#pragma unroll
        for (int c = 0; c < REPL; ++c) st32(rs, min(o, 0xFFFFFFFEu), mvo, sbn[c]);
        break;
      }
      good = 0;
      c_wait(sb, zb);
    }
    STAMP(4);
    STAMP_OUT(64 + cb, s);
    report(rs, 64 + cb, s, good, lane);
    BAR();
    if (s == 0) EV(2); if (s == NSEG - 2) EV(3);
    if (s > 0) rdy = max(rdy, (int)__builtin_amdgcn_readfirstlane(*(volatile LAS unsigned*)(uintptr_t)(L_TM + 32)));
    BAR();
    if (s + 2 < NSEG) fetch(s + 1);
  }
}

DEV void u_scatter(unsigned img, const f32x4 (&acc)[4][2], int wave, int lane) {
  const int n = lane & 15, g = lane >> 4;
#pragma unroll
  for (int c = 0; c < 4; ++c)
#pragma unroll
    for (int tt = 0; tt < 2; ++tt)
#pragma unroll
      for (int r = 0; r < 4; ++r) *(LAS float*)(uintptr_t)(img + wave * 8704 + (tt * 16 + 4 * g + r) * 272 + c * 64 + n * 4) = acc[c][tt][r];
}
DEV void u_gather(unsigned img, f32x4& v0, f32x4& v1, int tid) {
  v0 = (f32x4){0, 0, 0, 0}; v1 = v0;
#pragma unroll
  for (int ww = 0; ww < 4; ++ww) { const unsigned p = img + ww * 8704 + (tid >> 3) * 272 + (tid & 7) * 32; v0 += *(LAS f32x4*)(uintptr_t)p; v1 += *(LAS f32x4*)(uintptr_t)(p + 16); }
}
#define UOFF 240
#define UOFF_LAST 240
#define UPACE 20
DEV void role_u(const Args& a, rsrc_t rs, int wid, int wave, int lane, unsigned pol) {
  if (wid >= 120) return;
  const int cq = wid % 10, tid = wave * 64 + lane;
  const unsigned l16 = lane * 16;
  u32x4 w[56];
  load_w2(w, a.w2 + cq * 128, wave, lane);
  BAR();
  EV(7);
  unsigned pvo = OOB;
  if (lane < 32) pvo = prog_off(1, wave * 8 + (lane >> 2), lane & 1, (cq * 2 + ((lane >> 1) & 1)) & 3);
  const int nks = wave < 3 ? 14 : 12;
  int budget = SPIN_CAP;
  Sched sc; clk_init(sc);
  const unsigned tu0 = rtc();
  for (int seg = wid / 10; seg < NSEG; seg += 12) {
    while ((int)(rtc() - tu0) < seg * UPACE) __builtin_amdgcn_s_sleep(8);
    const int rep = seg % 9;
    if (lane >= 48) pvo = lane - 48 < nks ? hprog_off(rep, wave * 14 + lane - 48) : OOB;
    unsigned have, need = lane < 32 ? ((lane & 1) == (seg & 1) ? (seg >> 1) + 1 : 0) : (lane >= 48 && lane - 48 < nks) ? seg / 9 + 1 : 0;
    for (;;) { have = ld32(rs, pvo, 0); CFENCE(); if (__all(have >= need) || --budget <= 0) break; __builtin_amdgcn_s_sleep(4); }
    f32x4 acc[4][2];
    {
      u32x4 hf[14][2];
#pragma unroll
      for (int k = 0; k < 14; ++k)
#pragma unroll
        for (int tt = 0; tt < 2; ++tt) hf[k][tt] = ld128(rs, k < nks ? l16 + tt * 1024 : OOB, OFF_HH + (seg * 54 + wave * 14 + k) * 2048);
      static_for<14>([&](auto K) {
        static_for<4>([&](auto C) {
          constexpr int k = K, c = C, wi = ((k >> 1) * 2 + (c >> 1)) * 2 + (c & 1);
          mf_w<wi, k & 1, false, k == 0>(acc[c][0], w, hf[k][0]); mf_w<wi, k & 1, false, k == 0>(acc[c][1], w, hf[k][1]);
        });
      });
    }
    u32x4 bx[4][8];
#pragma unroll
    for (int c = 0; c < 4; ++c)
#pragma unroll
      for (int hh = 0; hh < 8; ++hh) bx[c][hh] = ld128(rs, l16 + (hh & 3) * 1024, b_addr(seg, cq * 4 + c, pol) + wave * 16384 + (hh & 4) * 1024);
    const size_t oo = (size_t)(seg * 32 + (tid >> 3)) * 1280 + cq * 128 + (tid & 7) * 16;
    const u32x4 xv = *(const u32x4*)(a.x + oo);
    acc_wait(acc);
    u_scatter(0, acc, wave, lane);
    BAR();
    f32x4 g0, g1;
    u_gather(0, g0, g1, tid);
#pragma unroll
    for (int r = 0; r < 4; ++r) { g0[r] = rbf(silu(rbf(g0[r]))); g1[r] = rbf(silu(rbf(g1[r]))); }
#pragma unroll
    for (int hh = 0; hh < 8; ++hh) asm volatile("" : "+a"(bx[0][hh]));
    {
      const bool act = lane < (seg == NSEG - 1 ? 16 : 8);
      const unsigned rvo = act ? ((seg + 1) * 128 + (lane >> 3) * 32 + wave * 8 + (lane & 7)) * 128 : OOB;
      sleep_until_turn(rs, sc, seg == NSEG - 1 ? 2 * seg + 1 : 2 * seg + 3, seg == NSEG - 1 ? UOFF_LAST : UOFF);
      for (;;) { const unsigned v = ld32(rs, rvo, OFF_REP); CFENCE(); if (__all(!act || v != 0) || --budget <= 0) break; if (seg == NSEG - 1) __builtin_amdgcn_s_sleep(2); else __builtin_amdgcn_s_sleep(16); }
    }
    EV(8);
    const unsigned so = OFF_PT + seg * 81920;
#pragma unroll
    for (int c = 0; c < 4; ++c) { acc[c][0] = (f32x4){0, 0, 0, 0}; acc[c][1] = (f32x4){0, 0, 0, 0}; }
    {
      u32x4 px[8][2];
#pragma unroll
      for (int hh = 0; hh < 8; ++hh)
#pragma unroll
        for (int tt = 0; tt < 2; ++tt) px[hh][tt] = ld128(rs, l16 + tt * 1024, so + (wave * 8 + hh) * 2048);
      acc_wait(acc);
#pragma unroll
      for (int hh = 0; hh < 4; ++hh)
#pragma unroll
        for (int c = 0; c < 4; ++c) { if (c == 0) { mf_va(acc[c][0], px[hh][0], bx[c][hh]); mf_va(acc[c][1], px[hh][1], bx[c][hh]); } else { mf_vv(acc[c][0], px[hh][0], bx[c][hh]); mf_vv(acc[c][1], px[hh][1], bx[c][hh]); } }
      CFENCE();
      u32x4 pm[2][2], z[4][2];
#pragma unroll
      for (int hf = 0; hf < 2; ++hf)
#pragma unroll
        for (int tt = 0; tt < 2; ++tt) pm[hf][tt] = ld128(rs, l16 + hf * 1024, so + 65536 + tt * 8192 + wave * 2048);
#pragma unroll
      for (int c = 0; c < 4; ++c)
#pragma unroll
        for (int hf = 0; hf < 2; ++hf) z[c][hf] = ld128(rs, l16 + hf * 1024, z_base(seg, pol) + (cq * 4 + c) * 16384 + wave * 2048);
      CFENCE();
#pragma unroll
      for (int hh = 4; hh < 8; ++hh)
#pragma unroll
        for (int c = 0; c < 4; ++c) { if (c == 0) { mf_va(acc[c][0], px[hh][0], bx[c][hh]); mf_va(acc[c][1], px[hh][1], bx[c][hh]); } else { mf_vv(acc[c][0], px[hh][0], bx[c][hh]); mf_vv(acc[c][1], px[hh][1], bx[c][hh]); } }
#pragma unroll
      for (int hf = 0; hf < 2; ++hf)
#pragma unroll
        for (int c = 0; c < 4; ++c) { mf_vv(acc[c][0], pm[hf][0], z[c][hf]); mf_vv(acc[c][1], pm[hf][1], z[c][hf]); }
    }
    acc_wait(acc);
    u_scatter(40960, acc, wave, lane);
    BAR();
    f32x4 s0, s1;
    u_gather(40960, s0, s1, tid);
    u32x4 o;
#pragma unroll
    for (int j = 0; j < 2; ++j) {
      o[j] = pk(__builtin_bit_cast(float, xv[j] << 16) + rbf(rbf(s0[2 * j]) * g0[2 * j]), __builtin_bit_cast(float, xv[j] & 0xFFFF0000u) + rbf(rbf(s0[2 * j + 1]) * g0[2 * j + 1]));
      o[2 + j] = pk(__builtin_bit_cast(float, xv[2 + j] << 16) + rbf(rbf(s1[2 * j]) * g1[2 * j]), __builtin_bit_cast(float, xv[2 + j] & 0xFFFF0000u) + rbf(rbf(s1[2 * j + 1]) * g1[2 * j + 1]));
    }
    *(u32x4*)(a.out + oo) = o;
    EV(9);
  }
}

#define WDELAY 122
#define WPACE 0
#define WAHEAD 3
DEV void role_w(rsrc_t rs, int xq, int k, int wave, int lane, unsigned pol) {
  const int cb0 = 10 * xq + 2 * k;
  unsigned sink = 0;
  int budget = SPIN_CAP;
  Sched sc; clk_init(sc);
  for (int seg = 1 + wave; seg < NSEG; seg += 4) {
    const int s = seg - WAHEAD;
    if (s >= 2) {
      while (sc.j0 == 0 && --budget > 0) { adopt_cur(sc, ld128(rs, 0, OFF_CTL + CTL_CUR), 2 * NSEG); CFENCE(); __builtin_amdgcn_s_sleep(8); }
      sleep_until_turn(rs, sc, 2 * s, WDELAY * 8 - 240);
    }
    while (__builtin_amdgcn_readfirstlane(ld32(rs, seg * 128, OFF_CTL + CTL_RL)) == 0 && --budget > 0) { CFENCE(); __builtin_amdgcn_s_sleep(8); }
    if (s >= 2) wait_until(tau_of(sc, 2 * s) + WDELAY * 8);
#define WLANES 64
    constexpr int NI = 64 / WLANES;
    unsigned v[12 * NI];
    const unsigned wvo = lane < WLANES ? lane * 128 : OOB;
#pragma unroll
    for (int j = 0; j < 12 * NI; ++j) {
      const int b = j / NI, u = min(k + 5 * (b - 8), 15);
      const unsigned so = b < 8 ? b_addr(seg, cb0 + (b >> 2), pol) + (b & 3) * 16384 : kq_addr(u & 1, seg, (u >> 1) * 4 + xq, pol);
      v[j] = ld32(rs, wvo, so + (j % NI) * WLANES * 128); CFENCE(); __builtin_amdgcn_s_sleep(WPACE);
    }
#pragma unroll
    for (int j = 0; j < 12 * NI; ++j) sink += v[j];
    CFENCE();
  }
  asm volatile("" :: "v"(sink));
}

#define LEAD1 96
#define LEAD2 72
#define ATOL 8
#define NADAPT 8
#define TKOFF 200
#define TKSLEEP 2
#define GPOLL 2700
#define GPOLL0 2600
DEV void role_tk(rsrc_t rs, int lane) {
  Sched sc; clk_init(sc);
  Sched old = sc;
  int budget = SPIN_CAP, run = 0, m1 = 0, m2 = 0, relock = 0;
  const unsigned tref = now8();
  for (int j = 0; j < 2 * NSEG - 6; ++j) {
    const int s = j >> 1;
    const unsigned vo = rep_off(j, lane);
    u32x4 e;
    if (sc.j0 != 0 && j >= relock) wait_until((sc.j0 <= j + 1 ? tau_of(sc, j + 1) : tau_of(old, j + 1)) + TKOFF);
    for (;;) { e = ld128(rs, vo, OFF_REP); CFENCE(); if (__all(e.x != 0) || --budget <= 0) break; __builtin_amdgcn_s_sleep(TKSLEEP); }
    const bool zrow = !(j & 1) && lane >= 32;
    const unsigned M = tref + (unsigned)wmax((int)(e.y - tref) - (zrow ? VSLACK : 0));
    if (j + 1 >= relock) {
      const unsigned tn = sc.j0 <= j + 1 ? tau_of(sc, j + 1) : tau_of(old, j + 1);
      if (sc.j0 == 0 || (int)(M - tn) > LATE_THR) {
        sc.base += M + (sc.j0 == 0 ? GPOLL0 : GPOLL) - tau_of(sc, j + 3); sc.j0 = relock = j + 3;
        st128(rs, (u32x4){(unsigned)sc.j0, sc.base, (unsigned)sc.T, (unsigned)sc.D1}, lane < 8 ? lane * 16 : OOB, OFF_CTL + CTL_CUR);
        run = 0; m1 = 0; m2 = 0;
      }
    }
    const int du = (int)((float)(int)(e.z >> 2) * 2048.f * __builtin_amdgcn_rcpf((float)e.w)) - (zrow ? VSLACK : 0);
    if (sc.j0 <= j && __all((e.z & 3) == 3)) {
      const int d = wmax(du);
      if (j & 1) m2 += d; else m1 += d;
      if (++run == NADAPT) {
        const int n1 = m1 / (NADAPT / 2) + LEAD1, n2 = m2 / (NADAPT / 2) + LEAD2;
        if (abs(n1 - sc.D1) > ATOL || abs(n2 - (sc.T - sc.D1)) > ATOL) {
          old = sc;
          sc.base += (s + 3) * (sc.T - (n1 + n2)); sc.T = n1 + n2; sc.D1 = n1; sc.j0 = 2 * (s + 3);
          st128(rs, (u32x4){(unsigned)sc.j0, sc.base, (unsigned)sc.T, (unsigned)sc.D1}, lane < 8 ? lane * 16 : OOB, OFF_CTL + CTL_CUR);
        }
        run = 0; m1 = 0; m2 = 0;
      }
    } else { run = 0; m1 = 0; m2 = 0; }
  }
}

DEV float rowsum16(float v) {
  v += __builtin_bit_cast(float, __builtin_amdgcn_update_dpp(0, __builtin_bit_cast(int, v), 0xB1, 0xf, 0xf, false));
  v += __builtin_bit_cast(float, __builtin_amdgcn_update_dpp(0, __builtin_bit_cast(int, v), 0x4E, 0xf, 0xf, false));
  v += __builtin_bit_cast(float, __builtin_amdgcn_update_dpp(0, __builtin_bit_cast(int, v), 0x124, 0xf, 0xf, false));
  v += __builtin_bit_cast(float, __builtin_amdgcn_update_dpp(0, __builtin_bit_cast(int, v), 0x128, 0xf, 0xf, false));
  return v;
}
DEV void layernorm(const Args& a, rsrc_t rs, int seg, int wave, int lane) {
  const int sub = lane & 15;
  u32x4 v[2][5];
#pragma unroll
  for (int p = 0; p < 2; ++p)
#pragma unroll
    for (int i = 0; i < 5; ++i) v[p][i] = *(const u32x4*)(a.x + (size_t)(seg * 32 + wave * 8 + p * 4 + (lane >> 4)) * 1280 + (sub + 16 * i) * 16);
#pragma unroll
  for (int p = 0; p < 2; ++p) {
    float f[40], sum = 0.f;
#pragma unroll
    for (int i = 0; i < 5; ++i)
#pragma unroll
      for (int j = 0; j < 4; ++j) { f[i * 8 + 2 * j] = __builtin_bit_cast(float, v[p][i][j] << 16); f[i * 8 + 2 * j + 1] = __builtin_bit_cast(float, v[p][i][j] & 0xFFFF0000u); sum += f[i * 8 + 2 * j] + f[i * 8 + 2 * j + 1]; }
    const float mean = rowsum16(sum) * (1.f / 640.f);
    float sq = 0.f;
#pragma unroll
    for (int i = 0; i < 40; ++i) { const float d = f[i] - mean; sq = fmaf(d, d, sq); }
    const float mb = rbf(mean), vb = rbf(rowsum16(sq) * (1.f / 640.f));
    const float inv = 1.f / rbf(sqrtf(rbf(vb + 1e-5f)));
    const int row = wave * 8 + p * 4 + (lane >> 4);
#pragma unroll
    for (int i = 0; i < 5; ++i) {
      u32x4 o;
#pragma unroll
      for (int j = 0; j < 4; ++j) o[j] = pk(rbf(f[i * 8 + 2 * j] - mb) * inv, rbf(f[i * 8 + 2 * j + 1] - mb) * inv);
      const int c = sub + 16 * i;
      ldsw128(((c >> 2) * 2 + (row >> 4)) * 1024 + ((c & 3) * 16 + (row & 15)) * 16, o);
    }
  }
  BAR();
#pragma unroll
  for (int i = 0; i < 10; ++i) st128(rs, ldsr128((wave * 10 + i) * 1024 + lane * 16), lane * 16, OFF_XN + seg * 40960 + (wave * 10 + i) * 1024);
  VMWAIT(0);
  BAR();
  if (wave == 0) st32(rs, 1, lane == 0 ? seg * 128 : OOB, OFF_CTL + CTL_XP);
}

__global__ void __launch_bounds__(256, 1) k_main(Args a) {
  const int lane = threadIdx.x & 63, wave = __builtin_amdgcn_readfirstlane(threadIdx.x >> 6);
  const rsrc_t rs = __builtin_amdgcn_make_buffer_rsrc(a.ws, 0, WS_BYTES, 0x00020000);
  const u32x4 pr = ld128(rs, 0, OFF_CTL + CTL_POL);
  const unsigned xoff = __builtin_amdgcn_readfirstlane(ld32(rs, 16, OFF_CTL + CTL_POL));
  const int b = blockIdx.x, xq = (b + xoff) & 7, ix = b >> 3;
  EV(0);
  const unsigned pol = __builtin_amdgcn_readfirstlane((pr.y < pr.x ? 1u : 0u) | (pr.w < pr.z ? 2u : 0u)) ^ ((xoff & 4) ? 3u : 0u);
  if (xq < 4) {
    const int r = ix * 4 + xq;
    if (r < 32) role_p(a, rs, r, wave, lane, pol);
    else if (r < 64) role_v(a, rs, r - 32, wave, lane, pol);
    else {
      layernorm(a, rs, r - 64, wave, lane);
      EV(1);
      if (r < 104) role_c(a, rs, 10 * xq + ((r - 64) >> 2), wave, lane, pol);
      else if (r < 124) role_w(rs, xq, (r - 104) >> 2, wave, lane, pol);
      else if (r == 124 && wave == 0) role_tk(rs, lane);
      else if (r == 125 && wave == 0) role_agg(rs, lane);
    }
  } else {
    const int pid = (xq - 4) * 16 + (ix >> 1), par = ix & 1;
    const int wid = (xq - 4) * 32 + ix;
    if (pid & 1) role_x<K_VB>(a, rs, pid >> 1, par, 2, prog_off(1, pid >> 1, par, wave), wave, lane, pol);
    else role_x<K_QK>(a, rs, pid >> 1, par, 2, prog_off(0, pid >> 1, par, wave), wave, lane, pol);
    if (wid < 126) role_x<K_F1>(a, rs, (wid % 14) * 4 + wave, wid / 14, 9, hprog_off(wid / 14, (wid % 14) * 4 + wave), wave, lane, pol);
    role_u(a, rs, wid, wave, lane, pol);
  }
}

__global__ void __launch_bounds__(256) k_pro(Args a) {
  const int b = blockIdx.x, tid = threadIdx.x, lane = tid & 63, wave = __builtin_amdgcn_readfirstlane(tid >> 6);
  if (b < 32) {
    if ((b & 7) == 0 && wave == 0) {
      const int j = b >> 3;
      const rsrc_t rs = __builtin_amdgcn_make_buffer_rsrc(a.ws, 0, WS_BYTES, 0x00020000);
      const unsigned base = OFF_COMM + (j >> 1) * 2 * MB + 48 * 16384 + (j & 1) * 8192;
      st128(rs, (u32x4){1, 2, 3, 4}, lane * 16, base);
      VMWAIT(0);
      unsigned best = ~0u;
#pragma unroll
      for (int r = 0; r < 3; ++r) {
        unsigned long long c0, c1; unsigned v;
        asm volatile("s_memtime %0\n\ts_waitcnt lgkmcnt(0)" : "=s"(c0) :: "memory");
        v = ld32(rs, r * 256, base);
        asm volatile("s_waitcnt vmcnt(0)\n\ts_memtime %0\n\ts_waitcnt lgkmcnt(0)" : "=s"(c1) : "v"(v) : "memory");
        best = min(best, (unsigned)(c1 - c0));
      }
      if (lane == 0) { *(unsigned*)(a.ws + OFF_CTL + CTL_POL + 4 * j) = best; if (j == 0) *(unsigned*)(a.ws + OFF_CTL + CTL_POL + 16) = xcc_id(); }
    }
  } else if (b < 160) {
#pragma unroll
    for (int i = 0; i < 2; ++i) {
      const unsigned c = ((b - 32) * 2 + i) * 256 + tid;
      *(u32x4*)(a.ws + OFF_COMM + (c >> 14) * MB + (c & 16383) * 16) = (u32x4){SENT, SENT, SENT, SENT};
    }
  } else {
    const unsigned i = (b - 160) * 256 + tid;
    if (i < 1600) { if (i * 128 != CTL_POL) *(unsigned*)(a.ws + OFF_CTL + i * 128) = 0; }
    else if (i < 1600 + 65 * 128) *(unsigned*)(a.ws + OFF_REP + (i - 1600) * 128) = 0;
  }
}

extern "C" void kernel_launch(void* const* d_in, const int* in_sizes, int n_in, void* d_out, int out_size, void* d_ws, size_t ws_size, hipStream_t stream) {
  Args a;
  a.x = (const unsigned char*)d_in[0]; a.mem0 = (const unsigned char*)d_in[1]; a.wq = (const unsigned char*)d_in[2]; a.wk = (const unsigned char*)d_in[3];
  a.wv = (const unsigned char*)d_in[4]; a.wo = (const unsigned char*)d_in[5]; a.w1 = (const unsigned char*)d_in[8]; a.w3 = (const unsigned char*)d_in[9];
  a.w2 = (const unsigned char*)d_in[10]; a.out = (unsigned char*)d_out; a.ws = (unsigned char*)d_ws;
  (void)hipFuncSetAttribute((const void*)k_main, hipFuncAttributeMaxDynamicSharedMemorySize, 163840);
  k_pro<<<200, 256, 0, stream>>>(a);
  k_main<<<256, 256, 163840, stream>>>(a);
}
```

```cpp
#include <hip/hip_runtime.h>
#include <stdint.h>

typedef __bf16 bf16x8 __attribute__((ext_vector_type(8)));
typedef __bf16 bf16x16 __attribute__((ext_vector_type(16)));
typedef __bf16 bf16x2 __attribute__((ext_vector_type(2)));
typedef float f32x2 __attribute__((ext_vector_type(2)));
typedef float f32x4 __attribute__((ext_vector_type(4)));
typedef unsigned u32x2 __attribute__((ext_vector_type(2)));
typedef unsigned u32x4 __attribute__((ext_vector_type(4)));
typedef unsigned u32x8 __attribute__((ext_vector_type(8)));
typedef short s16x4 __attribute__((ext_vector_type(4)));
#define LAS __attribute__((address_space(3)))
#define DEV __device__ __forceinline__

constexpr int NSEG = 64;
constexpr unsigned MB = 1u << 20;
constexpr unsigned OFF_COMM = 0;
constexpr unsigned OFF_CTL = 4 * MB;
constexpr unsigned OFF_XN = 5 * MB;
constexpr unsigned OFF_PT = 14 * MB;
constexpr unsigned OFF_HH = 20 * MB;
constexpr unsigned OFF_BLK = 32 * MB;
constexpr unsigned OFF_DBG = 28 * MB;
constexpr unsigned WS_BYTES = 256 * MB;
constexpr unsigned OOB = 0xF0000000u;
constexpr unsigned C_MEM1 = 16384, C_PM = 16384 + 2048, C_PX = 2 * 16384, C_Z = 4 * 16384;
constexpr unsigned CTL_POL = 65536;
constexpr unsigned CTL_RL = 65536 + 4096;
constexpr unsigned CTL_HP = 131072;
constexpr unsigned SENT = 0xFFFFFFFFu;
constexpr int SPIN_CAP = 1 << 20;
constexpr int SC1 = 16;

typedef __amdgpu_buffer_rsrc_t rsrc_t;
DEV u32x4 ld128(rsrc_t r, unsigned vo, unsigned so) { return __builtin_amdgcn_raw_buffer_load_b128(r, vo, so, SC1); }
DEV u32x2 ld64(rsrc_t r, unsigned vo, unsigned so) { return __builtin_amdgcn_raw_buffer_load_b64(r, vo, so, SC1); }
DEV unsigned ld32(rsrc_t r, unsigned vo, unsigned so) { return __builtin_amdgcn_raw_buffer_load_b32(r, vo, so, SC1); }
DEV void st128(rsrc_t r, u32x4 v, unsigned vo, unsigned so) { __builtin_amdgcn_raw_buffer_store_b128(v, r, vo, so, SC1); }
#define BST_AUX 16
#define BLD_AUX 16
#define KST_AUX 16
template <int AUX> DEV void st128a(rsrc_t r, u32x4 v, unsigned vo, unsigned so) { __builtin_amdgcn_raw_buffer_store_b128(v, r, vo, so, AUX); }
template <int AUX> DEV u32x4 ld128a(rsrc_t r, unsigned vo, unsigned so) { return __builtin_amdgcn_raw_buffer_load_b128(r, vo, so, AUX); }
DEV void st64(rsrc_t r, u32x2 v, unsigned vo, unsigned so) { __builtin_amdgcn_raw_buffer_store_b64(v, r, vo, so, SC1); }
DEV void st32(rsrc_t r, unsigned v, unsigned vo, unsigned so) { __builtin_amdgcn_raw_buffer_store_b32(v, r, vo, so, SC1); }
DEV unsigned pk(float a, float b) { bf16x2 v = {(__bf16)a, (__bf16)b}; return __builtin_bit_cast(unsigned, v); }
DEV unsigned pks(float a, float b) { return min(pk(a, b), 0xFFFFFFFEu); }
DEV unsigned mx4(u32x4 v) { return max(max(v.x, v.y), max(v.z, v.w)); }
DEV f32x4 mfma(u32x4 a, u32x4 b, f32x4 c) { return __builtin_amdgcn_mfma_f32_16x16x32_bf16(__builtin_bit_cast(bf16x8, a), __builtin_bit_cast(bf16x8, b), c, 0, 0, 0); }
DEV f32x4 smf(u32x4 a, u32x8 b, f32x4 c, int idx) { return __builtin_amdgcn_smfmac_f32_16x16x64_bf16(__builtin_bit_cast(bf16x8, a), __builtin_bit_cast(bf16x16, b), c, idx, 0, 0); }
DEV u32x4 lo4(u32x8 v) { return __builtin_shufflevector(v, v, 0, 1, 2, 3); }
DEV u32x4 hi4(u32x8 v) { return __builtin_shufflevector(v, v, 4, 5, 6, 7); }
DEV u32x8 cat8(u32x4 a, u32x4 b) { return __builtin_shufflevector(a, b, 0, 1, 2, 3, 4, 5, 6, 7); }
DEV u32x4 cat4(u32x2 a, u32x2 b) { return __builtin_shufflevector(a, b, 0, 1, 2, 3); }
DEV u32x2 trread(unsigned lds_addr) { return __builtin_bit_cast(u32x2, __builtin_amdgcn_ds_read_tr16_b64_v4i16((LAS s16x4*)(uintptr_t)lds_addr)); }
DEV u32x4 ldsr128(unsigned a) { return *(LAS u32x4*)(uintptr_t)a; }
DEV void ldsw128(unsigned a, u32x4 v) { *(LAS u32x4*)(uintptr_t)a = v; }
DEV void ldsw64(unsigned a, u32x2 v) { *(LAS u32x2*)(uintptr_t)a = v; }
#define BAR() asm volatile("s_waitcnt lgkmcnt(0)\n\ts_barrier" ::: "memory")
#define VMWAIT(n) asm volatile("s_waitcnt vmcnt(" #n ")" ::: "memory")
#define LGWAIT() asm volatile("s_waitcnt lgkmcnt(0)" ::: "memory")
#define CFENCE() asm volatile("" ::: "memory")
template <int N> DEV void vmwait() { asm volatile("s_waitcnt vmcnt(%0)" :: "n"(N) : "memory"); }
DEV void glds(const void* sbase, unsigned voff, unsigned ldst) {
  unsigned keep;
  asm volatile("s_mov_b32 %0, m0\n\ts_mov_b32 m0, %3\n\ts_nop 0\n\tglobal_load_lds_dwordx4 %1, %2\n\ts_mov_b32 m0, %0" : "=&s"(keep) : "v"(voff), "s"(sbase), "s"(ldst) : "memory");
}
constexpr unsigned CTL_XP = 196608;
DEV void glds_sc1(const void* sbase, unsigned voff, unsigned ldst) {
  unsigned keep;
  asm volatile("s_mov_b32 %0, m0\n\ts_mov_b32 m0, %3\n\ts_nop 0\n\tglobal_load_lds_dwordx4 %1, %2 sc1\n\ts_mov_b32 m0, %0" : "=&s"(keep) : "v"(voff), "s"(sbase), "s"(ldst) : "memory");
}
DEV void set_m0(unsigned v) { asm volatile("s_mov_b32 m0, %0" :: "s"(v) : "memory"); }
template <int OFF> DEV void glds_off(const void* sbase, unsigned voff) { asm volatile("global_load_lds_dwordx4 %0, %1 offset:%2 sc1" :: "v"(voff), "s"(sbase), "n"(OFF) : "memory"); }
template <int V> struct IC { static constexpr int value = V; constexpr operator int() const { return V; } };
template <int N, int I = 0, class F> DEV void static_for(F&& f) { if constexpr (I < N) { f(IC<I>{}); static_for<N, I + 1>(f); } }
DEV float fold32(float a, float b) {
  auto r = __builtin_amdgcn_permlane32_swap(__builtin_bit_cast(unsigned, a), __builtin_bit_cast(unsigned, b), false, false);
  return __builtin_bit_cast(float, (unsigned)r[0]) + __builtin_bit_cast(float, (unsigned)r[1]);
}
DEV float xmax16(float v) { auto r = __builtin_amdgcn_permlane16_swap(__builtin_bit_cast(unsigned, v), __builtin_bit_cast(unsigned, v), false, false); return fmaxf(__builtin_bit_cast(float, (unsigned)r[0]), __builtin_bit_cast(float, (unsigned)r[1])); }
DEV float xmax32(float v) { auto r = __builtin_amdgcn_permlane32_swap(__builtin_bit_cast(unsigned, v), __builtin_bit_cast(unsigned, v), false, false); return fmaxf(__builtin_bit_cast(float, (unsigned)r[0]), __builtin_bit_cast(float, (unsigned)r[1])); }
DEV float xadd16(float v) { auto r = __builtin_amdgcn_permlane16_swap(__builtin_bit_cast(unsigned, v), __builtin_bit_cast(unsigned, v), false, false); return __builtin_bit_cast(float, (unsigned)r[0]) + __builtin_bit_cast(float, (unsigned)r[1]); }
DEV float xadd32(float v) { auto r = __builtin_amdgcn_permlane32_swap(__builtin_bit_cast(unsigned, v), __builtin_bit_cast(unsigned, v), false, false); return __builtin_bit_cast(float, (unsigned)r[0]) + __builtin_bit_cast(float, (unsigned)r[1]); }
DEV float bf2f(unsigned short u) { return __builtin_bit_cast(float, (unsigned)u << 16); }
DEV float rbf(float x) { return (float)(__bf16)x; }

DEV void smf_a(f32x4& c, u32x4 a, const u32x8& w, int idx) { asm volatile("v_smfmac_f32_16x16x64_bf16 %0, %1, %2, %3" : "+v"(c) : "v"(a), "a"(w), "v"(idx)); }
DEV void smf_v(f32x4& c, u32x4 a, const u32x8& w, int idx) { asm volatile("v_smfmac_f32_16x16x64_bf16 %0, %1, %2, %3" : "+v"(c) : "v"(a), "v"(w), "v"(idx)); }
template <int I> DEV void smf_w(f32x4& c, u32x4 a, const u32x8 (&w)[40], int idx) { if constexpr (I < 32) smf_a(c, a, w[I], idx); else smf_v(c, a, w[I], idx); }
DEV void mf_va(f32x4& c, u32x4 a, u32x4 b) { asm volatile("v_mfma_f32_16x16x32_bf16 %0, %1, %2, %0" : "+v"(c) : "v"(a), "a"(b)); }
DEV void mf_vv(f32x4& c, u32x4 a, u32x4 b) { asm volatile("v_mfma_f32_16x16x32_bf16 %0, %1, %2, %0" : "+v"(c) : "v"(a), "v"(b)); }
template <bool A> DEV void pin_w(u32x8& x, u32x8& y) {
  if constexpr (A) asm volatile("s_waitcnt lgkmcnt(0)" : "+a"(x), "+a"(y) :: "memory"); else asm volatile("s_waitcnt lgkmcnt(0)" : "+v"(x), "+v"(y) :: "memory");
}
template <bool A> DEV void pin_w4(u32x4& x, u32x4& y, u32x4& z, u32x4& u) {
  if constexpr (A) asm volatile("s_waitcnt lgkmcnt(0)" : "+a"(x), "+a"(y), "+a"(z), "+a"(u) :: "memory"); else asm volatile("s_waitcnt lgkmcnt(0)" : "+v"(x), "+v"(y), "+v"(z), "+v"(u) :: "memory");
}
DEV void pin2(f32x4& x, f32x4& y) { asm volatile("" : "+v"(x), "+v"(y)); }
DEV void pin2n(f32x4& x, f32x4& y) { asm volatile("s_nop 3" : "+v"(x), "+v"(y)); }
DEV unsigned pin1(unsigned x) { asm volatile("" :: "v"(x)); return x; }
#define NOP(n) asm volatile("s_nop " #n)
#define PIN(x) asm volatile("" : "+v"(x))
#define PINS(x) asm volatile("" : "+s"(x))
DEV unsigned xcc_id() { return (unsigned)__builtin_amdgcn_s_getreg(20 | (3 << 11)) & 0xfu; }
DEV unsigned rtc() { unsigned long long t; asm volatile("s_memrealtime %0\n\ts_waitcnt lgkmcnt(0)" : "=s"(t) :: "memory"); return (unsigned)t; }
constexpr unsigned CTL_CUR = 65536 + 12288 + 1024;
constexpr unsigned OFF_REP = 8 * MB;
constexpr int MAXWAIT = 400 * 8;
#define D1_INIT 840
#define D2_INIT 510
struct Sched { int j0, jbad, rx; unsigned base; int T, D1; unsigned c0, v0; int R; unsigned ct, tt; };
DEV unsigned now8() { return rtc() << 3; }
DEV unsigned cycles() { unsigned long long t; asm volatile("s_memtime %0\n\ts_waitcnt lgkmcnt(0)" : "=s"(t) :: "memory"); return (unsigned)t; }
DEV void clk_init(Sched& c) { c.j0 = 0; c.jbad = 0; c.rx = 0; c.base = 0; c.T = D1_INIT + D2_INIT; c.D1 = D1_INIT; c.v0 = rtc(); c.c0 = cycles(); c.R = 24 * 256; c.ct = c.c0; c.tt = 0; }
DEV void clk_update(Sched& c) {
  const unsigned v = rtc() - c.v0, y = cycles() - c.c0;
  if (v > 300) c.R = __builtin_amdgcn_readfirstlane((int)((float)y * 256.f * __builtin_amdgcn_rcpf((float)v)));
}
#define RELAX 120
#define NRELAX 3
DEV unsigned tau_of(const Sched& c, int j) { return c.base + (j >> 1) * c.T + ((j & 1) ? c.D1 : 0) + RELAX * min(max(j - c.rx, 0), NRELAX); }
DEV void adopt_cur(Sched& c, u32x4 v, int j) {
  const int j0 = __builtin_amdgcn_readfirstlane(v.x);
  if (j0 > c.j0 && j0 <= j) { c.j0 = j0; c.base = __builtin_amdgcn_readfirstlane(v.y); c.T = __builtin_amdgcn_readfirstlane(v.z); const int w = __builtin_amdgcn_readfirstlane(v.w); c.D1 = w & 0xffff; c.rx = w >> 16; }
}
DEV bool wait_until(unsigned tau) {
  int d = (int)(tau - now8());
  if (d <= 0 || d > MAXWAIT) return false;
  while ((int)(tau - now8()) > 0) {}
  return true;
}
#define NOP16 "s_nop 0\n\ts_nop 0\n\ts_nop 0\n\ts_nop 0\n\ts_nop 0\n\ts_nop 0\n\ts_nop 0\n\ts_nop 0\n\ts_nop 0\n\ts_nop 0\n\ts_nop 0\n\ts_nop 0\n\ts_nop 0\n\ts_nop 0\n\ts_nop 0\n\ts_nop 0\n\t"
#define FK 96
DEV unsigned est_cycle(unsigned tau, int R, unsigned c0, int best) {
  unsigned long long v, c; unsigned t; int rem;
  asm volatile(
    "1:\n\t"
    "s_memrealtime %0\n\ts_memtime %1\n\ts_waitcnt lgkmcnt(0)\n\t"
    "s_lshl_b32 %2, s94, 3\n\t"
    "s_sub_i32 %2, %5, %2\n\t"
    "s_mul_i32 %2, %2, %6\n\t"
    "s_ashr_i32 %2, %2, 11\n\t"
    "s_sub_i32 %3, s96, %7\n\t"
    "s_add_i32 %2, %2, %3\n\t"
    "s_min_i32 %4, %4, %2\n\t"
    "s_sub_i32 %2, %2, 100\n\t"
    "s_max_i32 %4, %4, %2\n\t"
    "s_sub_i32 %3, %4, %3\n\t"
    "s_cmp_gt_i32 %3, 520\n\t"
    "s_cbranch_scc1 1b"
    : "=&{s[94:95]}"(v), "=&{s[96:97]}"(c), "=&s"(t), "=&s"(rem), "+s"(best)
    : "s"(tau), "s"(R), "s"(c0) : "memory", "scc");
  return c0 + best;
}
DEV void release(unsigned cs) {
  unsigned long long c, pc; int rem;
  asm volatile(
    "1:\n\t"
    "s_memtime %0\n\ts_waitcnt lgkmcnt(0)\n\t"
    "s_sub_i32 %2, %3, s96\n\t"
    "s_cmp_gt_i32 %2, %4\n\t"
    "s_cbranch_scc1 1b\n\t"
    "s_sub_i32 %2, %2, %5\n\t"
    "s_max_i32 %2, %2, 0\n\t"
    "s_and_b32 %2, %2, -4\n\t"
    "s_sub_i32 %2, 140, %2\n\t"
    "s_getpc_b64 %1\n\t"
    "s_add_u32 s98, s98, %2\n\t"
    "s_addc_u32 s99, s99, 0\n\t"
    "s_setpc_b64 %1\n\t"
    NOP16 NOP16 "s_barrier"
    : "=&{s[96:97]}"(c), "=&{s[98:99]}"(pc), "=&s"(rem)
    : "s"(cs), "n"(FK + 124), "n"(FK) : "memory", "scc");
}
#define DRIFT 10
#define CSLEEP 3
#define LATE_THR 120
constexpr unsigned L_TM = 61440;
DEV void mark_overrun(Sched& c, int j, unsigned t) { if ((int)(t - tau_of(c, j + 1)) > LATE_THR + 40) c.jbad = c.j0; }
template <class F>
DEV unsigned trigger(rsrc_t rs, Sched& c, int j, unsigned hold, F&& poll) {
  unsigned cs, timed = 0;
  for (;;) {
    const unsigned tau = tau_of(c, j), now = now8();
    const int d = (int)(tau - now);
    const bool ok = c.j0 > c.jbad && d > -30 * 8 && d <= MAXWAIT;
    if (ok && d <= 0) { cs = cycles(); break; }
    if (ok && d <= 50 * 8) {
      const unsigned dt = tau - c.tt;
      const int prior = dt < 4096 ? (int)((dt * (unsigned)c.R) >> 11) + DRIFT : 0x7fffffff;
      cs = est_cycle(__builtin_amdgcn_readfirstlane(tau), __builtin_amdgcn_readfirstlane(c.R), __builtin_amdgcn_readfirstlane(c.ct), __builtin_amdgcn_readfirstlane(prior));
      c.ct = cs; c.tt = tau; timed = 1;
      break;
    }
    if (!ok && (unsigned)(hold - now) > (unsigned)MAXWAIT) { poll(); cs = cycles(); break; }
    if (d > 100 * 8 || !ok) { adopt_cur(c, ld128(rs, 0, OFF_CTL + CTL_CUR), j); CFENCE(); __builtin_amdgcn_s_sleep(CSLEEP); }
  }
  ldsw64(L_TM + (j & 2) * 8, (u32x2){cs, timed | (unsigned)c.R << 2});
  return __builtin_amdgcn_readfirstlane(cs);
}
#define STAMP(k)
#define STV(k) stdummy
#define STAMP_DECL unsigned stdummy
#define STAMP_OUT(e, s)

#define BMODE 0
#define KQMODE 0
constexpr unsigned BSTR = BMODE == 2 ? 8192 : 16384;
constexpr int ZBLK0 = BMODE == 0 ? 0 : BMODE == 1 ? 10240 : 5376;
DEV unsigned blk_addr(int blk, unsigned pol, unsigned far) { return OFF_BLK + blk * 16384 + (((pol >> ((blk >> 7) & 1)) ^ far) & 1) * 8192; }
DEV unsigned z_base(int s, unsigned pol) { const int pg = s / 3; return blk_addr(ZBLK0 + pg * 128 + (s - 3 * pg) * 40, pol, 0); }
DEV unsigned kq_addr(int isq, int seg, int h, unsigned pol) {
  if (KQMODE == 2) return OFF_BLK + (ZBLK0 + 3072) * 16384 + (isq * 2048 + seg * 32 + h) * 8192;
  return BMODE == 1 ? blk_addr(isq * 2048 + seg * 32 + h, pol, 1) : blk_addr(ZBLK0 + 3072 + isq * 2048 + seg * 32 + h, pol, 0);
}
DEV unsigned b_addr(int seg, int cb, unsigned pol) { return BMODE == 2 ? OFF_BLK + (seg * 40 + cb) * 32768 : blk_addr((seg * 40 + cb) * 4, pol, 1 - BMODE); }

#define EV(slot)
struct Args {
  const unsigned char* x; const unsigned char* mem0; const unsigned char* wq; const unsigned char* wk; const unsigned char* wv; const unsigned char* wo;
  const unsigned char* w1; const unsigned char* w3; const unsigned char* w2; unsigned char* out; unsigned char* ws;
};

enum { K_P, K_V, K_QK, K_VB, K_F1 };
constexpr unsigned L_XB0 = 120 * 1024, L_XB1 = 80 * 1024;
template <int KIND, class WT, class LATE>
DEV void load_weights(WT& w, const unsigned char* baseA, const unsigned char* baseB, int wave, int lane, int cmask, LATE&& late) {
  constexpr int NG = 20;
  constexpr bool WO = KIND == K_V || KIND == K_VB;
  constexpr unsigned SA = KIND == K_F1 ? 3456 : 8192, SB = WO ? 1280 : SA;
  constexpr int MA = (KIND == K_P || KIND == K_V) ? 2 : 0, MBB = KIND == K_P ? 2 : KIND == K_VB ? 1 : 0;
  const int gB = lane >> 4, q = (lane >> 2) & 3, pp = lane & 3;
  unsigned vo[2][4], rd[2][2];
#pragma unroll
  for (int m = 0; m < 2; ++m) {
    const int mode = m ? MBB : MA;
#pragma unroll
    for (int p = 0; p < 4; ++p) {
      const int key = mode == 0 ? (gB | (p >> 1) << 2) : mode == 1 ? (gB | (p & 1) << 2) : ((gB >> 1) | p << 1);
      vo[m][p] = (wave * 16 + p * 4 + gB) * (m ? SB : SA) + (((lane & 15) ^ (key << 1)) & cmask) * 16;
    }
    const int key = mode == 2 ? ((gB & 1) | (q >> 1) << 1 | (q & 1) << 2) : (q | (gB & 1) << 2);
    const int row = mode == 0 ? 8 * gB + q : mode == 1 ? 4 * gB + q : 16 * (gB >> 1) + 4 * (q >> 1) + 2 * (gB & 1) + 8 * (q & 1);
#pragma unroll
    for (int nt = 0; nt < 2; ++nt) rd[m][nt] = row * 256 + ((4 * wave + 2 * nt + (pp >> 1)) ^ (key << 1)) * 16 + (pp & 1) * 8;
  }
  auto issue = [&](int g) {
    const unsigned char* src = g < 10 ? baseA + (size_t)g * 64 * SA : WO ? baseB + (size_t)((g - 10) & 1) * 64 * SB + ((g - 10) >> 1) * 256 : baseB + (size_t)(g - 10) * 64 * SB;
#pragma unroll
    for (int p = 0; p < 4; ++p) glds(src, vo[g >= 10][p], (g % 7) * 16384 + wave * 4096 + p * 1024);
  };
#pragma unroll
  for (int g = 0; g < 7; ++g) issue(g);
  static_for<NG>([&](auto G) {
    constexpr int g = G, mode = g < 10 ? MA : MBB;
    if constexpr (g == 0) vmwait<24>(); else if constexpr (g < NG - 5) vmwait<20>(); else if constexpr (g == NG - 5) late();
    if constexpr (g <= NG - 5) BAR();
    if constexpr (g >= 1 && g + 6 < NG) issue(g + 6);
    constexpr unsigned phs = mode == 2 ? 256 : mode == 1 ? 16 * 256 : 4 * 256, sl = (g % 7) * 16384;
#pragma unroll
    for (int nt = 0; nt < 2; ++nt) {
      const unsigned rb = rd[g >= 10][nt] + sl;
      u32x2 a = trread(rb), b = trread(rb + phs), c = trread(rb + 8192), d = trread(rb + 8192 + phs);
      if constexpr (KIND == K_P || KIND == K_V) w[g * 2 + nt] = cat8(cat4(a, b), cat4(c, d));
      else { w[g * 4 + nt * 2] = cat4(a, b); w[g * 4 + nt * 2 + 1] = cat4(c, d); }
    }
    if constexpr (KIND == K_P || KIND == K_V) pin_w<g < 16>(w[g * 2], w[g * 2 + 1]);
    else pin_w4<g < 16>(w[g * 4], w[g * 4 + 1], w[g * 4 + 2], w[g * 4 + 3]);
  });
}
DEV void load_w2(u32x4 (&w)[56], const unsigned char* base, int wave, int lane) {
  const int gB = lane >> 4, q = (lane >> 2) & 3, pp = lane & 3;
  const unsigned ring = wave * 32768;
  const unsigned vo = (lane >> 3) * 1280 + ((lane & 7) ^ (((lane >> 4) & 3) << 1)) * 16;
  unsigned rd[4];
#pragma unroll
  for (int c = 0; c < 4; ++c) rd[c] = ring + (4 * gB + q) * 128 + ((2 * c + (pp >> 1)) ^ ((((gB & 1) << 1) | (q >> 1)) << 1)) * 16 + (pp & 1) * 8;
  auto issue = [&](int g) {
    const unsigned char* src = base + (size_t)min(wave * 7 + g, 26) * 64 * 1280;
#pragma unroll
    for (int p = 0; p < 8; ++p) glds(src + p * 8 * 1280, vo, ring + (g & 3) * 8192 + p * 1024);
  };
#pragma unroll
  for (int g = 0; g < 4; ++g) issue(g);
  static_for<7>([&](auto G) {
    constexpr int g = G;
    vmwait<8 * (6 - g < 3 ? 6 - g : 3)>();
#pragma unroll
    for (int c = 0; c < 4; ++c) {
      const unsigned rb = rd[c] + (g & 3) * 8192;
      w[g * 8 + c * 2] = cat4(trread(rb), trread(rb + 2048)); w[g * 8 + c * 2 + 1] = cat4(trread(rb + 4096), trread(rb + 6144));
    }
    pin_w4<true>(w[g * 8], w[g * 8 + 1], w[g * 8 + 2], w[g * 8 + 3]); pin_w4<true>(w[g * 8 + 4], w[g * 8 + 5], w[g * 8 + 6], w[g * 8 + 7]);
    if constexpr (g + 4 < 7) issue(g + 4);
  });
}

DEV unsigned prog_off(int type, int h, int par, int wave) { return OFF_CTL + (((type * 32 + h) * 2 + par) * 4 + wave) * 128; }
DEV unsigned hprog_off(int rep, int slice) { return OFF_CTL + CTL_HP + (rep * 56 + slice) * 128; }
DEV float silu(float x) { return x * __builtin_amdgcn_rcpf(1.f + __builtin_amdgcn_exp2f(-1.4426950408889634f * x)); }
DEV u32x2 pk4(f32x4 v) { return (u32x2){pk(v[0], v[1]), pk(v[2], v[3])}; }

template <int I, int HI, bool WA, bool FIRST, int N>
DEV void mf_w(f32x4& c, const u32x4 (&w)[N], u32x4 x) {
  const u32x4 f = w[2 * I + HI];
  if constexpr (FIRST) {
    if constexpr (I < 32) { if constexpr (WA) asm volatile("v_mfma_f32_16x16x32_bf16 %0, %1, %2, 0" : "=&v"(c) : "a"(f), "v"(x)); else asm volatile("v_mfma_f32_16x16x32_bf16 %0, %2, %1, 0" : "=&v"(c) : "a"(f), "v"(x)); }
    else { if constexpr (WA) asm volatile("v_mfma_f32_16x16x32_bf16 %0, %1, %2, 0" : "=&v"(c) : "v"(f), "v"(x)); else asm volatile("v_mfma_f32_16x16x32_bf16 %0, %2, %1, 0" : "=&v"(c) : "v"(f), "v"(x)); }
  } else {
    if constexpr (I < 32) { if constexpr (WA) asm volatile("v_mfma_f32_16x16x32_bf16 %0, %1, %2, %0" : "+v"(c) : "a"(f), "v"(x)); else asm volatile("v_mfma_f32_16x16x32_bf16 %0, %2, %1, %0" : "+v"(c) : "a"(f), "v"(x)); }
    else { if constexpr (WA) asm volatile("v_mfma_f32_16x16x32_bf16 %0, %1, %2, %0" : "+v"(c) : "v"(f), "v"(x)); else asm volatile("v_mfma_f32_16x16x32_bf16 %0, %2, %1, %0" : "+v"(c) : "v"(f), "v"(x)); }
  }
}

DEV void issue_x(const unsigned char* ws, int seg, unsigned buf, int wave, int lane) {
#pragma unroll
  for (int i = 0; i < 10; ++i) glds_sc1(ws + OFF_XN + (size_t)seg * 40960 + (wave * 10 + i) * 1024, lane * 16, buf + (wave * 10 + i) * 1024);
}
template <int NT, class MID>
DEV void xproj(f32x4 (&acc)[NT][2], const u32x4 (&w)[80], unsigned xa, MID&& mid) {
  u32x4 xr[3][2];
#pragma unroll
  for (int k = 0; k < 2; ++k) { xr[k][0] = ldsr128(xa + k * 2048); xr[k][1] = ldsr128(xa + k * 2048 + 1024); }
  static_for<20>([&](auto K) {
    constexpr int ks = K;
    static_for<2 * NT>([&](auto J) {
      constexpr int j = J, t = j >> 1, wi = ((t >> 1) * 10 + (ks >> 1)) * 2 + (t & 1);
      mf_w<wi, ks & 1, true, ks == 0>(acc[t][j & 1], w, xr[ks % 3][j & 1]);
      if constexpr (ks + 2 < 20 && j == 1) { CFENCE(); xr[(ks + 2) % 3][0] = ldsr128(xa + (ks + 2) * 2048); CFENCE(); }
      if constexpr (ks + 2 < 20 && j == NT / 2 + 1) { CFENCE(); xr[(ks + 2) % 3][1] = ldsr128(xa + (ks + 2) * 2048 + 1024); CFENCE(); }
      if constexpr (j == (NT == 4 ? 5 : 0)) mid(K);
    });
  });
}
template <int NT> DEV void acc_wait(f32x4 (&c)[NT][2]) {
  if constexpr (NT == 4) asm volatile("s_nop 7" : "+v"(c[0][0]), "+v"(c[0][1]), "+v"(c[1][0]), "+v"(c[1][1]), "+v"(c[2][0]), "+v"(c[2][1]), "+v"(c[3][0]), "+v"(c[3][1]));
  else asm volatile("s_nop 7" : "+v"(c[0][0]), "+v"(c[0][1]), "+v"(c[1][0]), "+v"(c[1][1]));
}

template <int KIND>
DEV void role_x(const Args& a, rsrc_t rs, int id, int seg0, int dseg, unsigned po, int wave, int lane, unsigned pol) {
  constexpr int NST = KIND == K_QK ? 4 : KIND == K_F1 ? 2 : 10;
  u32x4 w[80];
  auto late = [&]() {
    if constexpr (KIND != K_F1) {
      int budget = SPIN_CAP;
      for (;;) { const unsigned v = ld32(rs, lane * 128, OFF_CTL + CTL_XP); CFENCE(); if (__all(v == 1) || --budget <= 0) break; }
    }
    VMWAIT(0);
    issue_x(a.ws, seg0, L_XB0, wave, lane);
  };
  if constexpr (KIND == K_QK) load_weights<K_QK>(w, a.wq + id * 256, a.wk + id * 256, wave, lane, 15, late);
  else if constexpr (KIND == K_F1) load_weights<K_F1>(w, a.w1 + (id >> 2) * 256, a.w3 + (id >> 2) * 256, wave, lane, id >= 52 ? 7 : 15, late);
  else load_weights<K_VB>(w, a.wv + id * 256, a.wo + (size_t)id * 128 * 1280, wave, lane, 15, late);
  VMWAIT(0);
  EV(KIND == K_F1 ? 4 : 1);
  const unsigned l16 = lane * 16, fvo = lane < 32 ? po + lane * 4 : OOB;
  const unsigned svo = (KIND == K_F1 && id > 53) ? OOB : l16, bvo = l16 + (id >> 3) * BSTR + (id & 7) * 1024;
  int it = 0;
  for (int seg = seg0; seg < NSEG; seg += dseg, ++it) {
    const unsigned buf = (it & 1) ? L_XB1 : L_XB0;
    vmwait<NST>();
    BAR();
    const bool more = seg + dseg < NSEG;
    const unsigned char* xsrc = a.ws + OFF_XN + (size_t)(more ? seg + dseg : seg) * 40960 + wave * 10240;
    const unsigned xdst = L_XB0 + L_XB1 - buf + wave * 10240;
    const unsigned char *xs0 = xsrc, *xs1 = xsrc + 4096, *xs2 = xsrc + 8192;
    PINS(xs0); PINS(xs1); PINS(xs2);
    auto mid = [&](auto K) {
      if constexpr (K % 5 == 0 && K <= 10) set_m0(xdst + (K / 5) * 4096);
      if constexpr (K % 5 != 0 && K <= 12) glds_off<(K % 5 - 1) * 1024>(K < 5 ? xs0 : K < 10 ? xs1 : xs2, l16);
      if constexpr (K == 16) { if (it > 1) { vmwait<10>(); st32(rs, it, fvo, 0); } }
    };
    if constexpr (KIND != K_VB) {
      f32x4 acc[4][2];
      xproj<4>(acc, w, buf + l16, mid);
      acc_wait(acc);
      if constexpr (KIND == K_QK) {
#pragma unroll
        for (int m = 0; m < 2; ++m)
#pragma unroll
          for (int tt = 0; tt < 2; ++tt) st128a<KST_AUX>(rs, cat4(pk4(acc[2 * m][tt]), pk4(acc[2 * m + 1][tt])), l16, kq_addr(1 - m, seg, id, pol) + (tt * 4 + wave) * 1024);
      } else {
#pragma unroll
        for (int tt = 0; tt < 2; ++tt) {
          f32x4 hv[2];
#pragma unroll
          for (int t = 0; t < 2; ++t)
#pragma unroll
            for (int r = 0; r < 4; ++r) hv[t][r] = rbf(silu(rbf(acc[t][tt][r]))) * rbf(acc[2 + t][tt][r]);
          st128(rs, cat4(pk4(hv[0]), pk4(hv[1])), svo, OFF_HH + (seg * 54 + id) * 2048 + tt * 1024);
        }
      }
    } else {
      f32x4 acc[2][2];
      xproj<2>(acc, w, buf + l16, mid);
      acc_wait(acc);
#pragma unroll
      for (int tt = 0; tt < 2; ++tt) ldsw128((wave * 2 + tt) * 1024 + l16, cat4(pk4(acc[0][tt]), pk4(acc[1][tt])));
      BAR();
      u32x4 vf[4][2];
#pragma unroll
      for (int ks = 0; ks < 4; ++ks) { vf[ks][0] = ldsr128((ks * 2) * 1024 + l16); vf[ks][1] = ldsr128((ks * 2 + 1) * 1024 + l16); }
      f32x4 c[3][2];
      static_for<12>([&](auto CT) {
        constexpr int ct = CT;
        u32x4 o;
        static_for<4>([&](auto KS) {
          constexpr int ks = KS, wi = 20 + ((ct >> 1) * 2 + (ks >> 1)) * 2 + (ct & 1);
          if constexpr (ct < 10) { mf_w<wi, ks & 1, false, ks == 0>(c[ct % 3][0], w, vf[ks][0]); mf_w<wi, ks & 1, false, ks == 0>(c[ct % 3][1], w, vf[ks][1]); }
          if constexpr (ct >= 2) {
            constexpr int e = (ct - 2) % 3;
            if constexpr (ks == 0) { if constexpr (ct == 11) pin2n(c[e][0], c[e][1]); else pin2(c[e][0], c[e][1]); }
            o[ks] = pk(c[e][ks >> 1][2 * (ks & 1)], c[e][ks >> 1][2 * (ks & 1) + 1]); pin1(o[ks]);
          }
        });
        if constexpr (ct >= 2) { CFENCE(); st128a<BST_AUX>(rs, o, bvo, b_addr(seg, ((ct - 2) >> 1) * 8 + wave * 2 + (ct & 1), pol)); CFENCE(); }
      });
    }
    if (it == 0) { vmwait<0>(); st32(rs, 1, fvo, 0); EV(KIND == K_F1 ? 5 : 2); }
  }
  VMWAIT(0);
  st32(rs, it, fvo, 0);
  EV(KIND == K_F1 ? 6 : 3);
  BAR();
}

constexpr int REPL = 4;
DEV unsigned slot_base(int s, unsigned pol) { return OFF_COMM + (s & 3) * MB + ((pol >> ((s >> 1) & 1)) & 1) * 8192; }
DEV int wmax(int v) {
  v = max(v, __builtin_amdgcn_update_dpp(v, v, 0xB1, 0xf, 0xf, false));
  v = max(v, __builtin_amdgcn_update_dpp(v, v, 0x4E, 0xf, 0xf, false));
  v = max(v, __builtin_amdgcn_update_dpp(v, v, 0x124, 0xf, 0xf, false));
  v = max(v, __builtin_amdgcn_update_dpp(v, v, 0x128, 0xf, 0xf, false));
  return max(max(__builtin_amdgcn_readlane(v, 0), __builtin_amdgcn_readlane(v, 16)), max(__builtin_amdgcn_readlane(v, 32), __builtin_amdgcn_readlane(v, 48)));
}
DEV int wmin(int v) { return -wmax(-v); }
DEV void report(rsrc_t rs, int e, int s, unsigned flags, int lane) {
  unsigned long long v, c;
  asm volatile("s_memrealtime %0\n\ts_memtime %1\n\ts_waitcnt lgkmcnt(0)" : "=s"(v), "=s"(c) :: "memory");
  const u32x2 tm = *(volatile LAS u32x2*)(uintptr_t)(L_TM + (s & 1) * 16);
  st128(rs, (u32x4){(unsigned)s + 1, (unsigned)v << 3, (((unsigned)c - tm.x) << 2) | flags | (tm.y & 1), tm.y >> 2}, lane < 8 ? lane * 16 : OOB, OFF_REP + (s * 128 + e) * 128);
}
DEV void sleep_until_turn(rsrc_t rs, Sched& c, int j, int off) {
  for (;;) {
    adopt_cur(c, ld128(rs, 0, OFF_CTL + CTL_CUR), j + 1); CFENCE();
    const int d = (int)(tau_of(c, j) + off - now8());
    if (c.j0 == 0 || d > 200 * MAXWAIT) return;
    if (d < 150 * 8) { wait_until(tau_of(c, j) + off); return; }
    if (d > 500 * 8) __builtin_amdgcn_s_sleep(127); else __builtin_amdgcn_s_sleep(40);
  }
}
DEV int probe_seg(int s, int lane) { return min(s + (3 << (lane & 3)), NSEG - 1); }
DEV unsigned probe_ld(rsrc_t rs, int s, int lane) { return ld32(rs, lane < 4 ? probe_seg(s, lane) * 128 : OOB, OFF_CTL + CTL_RL); }
DEV int probe_rdy(unsigned v, int s, int lane) { return wmax(v != 0 ? probe_seg(s, lane) + 1 : 0); }
DEV void role_agg(rsrc_t rs, int lane) {
  int done = 0, budget = SPIN_CAP;
  while (done < NSEG && --budget > 0) {
    int m = NSEG;
#pragma unroll
    for (int i = 0; i < 8; ++i) m = min(m, 2 * (int)ld32(rs, (i * 64 + lane) * 128, OFF_CTL) + ((lane >> 2) & 1));
    CFENCE();
    m = min(wmin(m), NSEG);
    for (; done < m; done = min(done + 8, m)) st128(rs, (u32x4){1, 1, 1, 1}, done + (lane >> 3) < m ? done * 128 + lane * 16 : OOB, OFF_CTL + CTL_RL);
    __builtin_amdgcn_s_sleep(16);
  }
}
DEV unsigned rep_off(int j, int lane) { return ((j >> 1) * 128 + ((j & 1) ? 64 + min(lane, 39) : lane)) * 128; }
#define PTSLEEP 2
#define PSLEEP 2
#define HOLDP 720
#define HOLDC 1080
#define NOPM_(n) asm volatile("s_nop " #n ::: "memory")
#define NOPM(n) NOPM_(n)
#define VSLACK 48
#define PACE 11
#define NOPV(n, x) asm volatile("s_nop " #n : "+v"(x))
template <int N> DEV void zero_acc(f32x4 (&c)[N]) {
#pragma unroll
  for (int t = 0; t < N; ++t) { c[t] = (f32x4){0, 0, 0, 0}; PIN(c[t]); }
}
DEV unsigned memoff(int j) { return j < 8 ? j * 1024 : C_MEM1 + (j - 8) * 1024; }
DEV bool has_nan(f32x4 v) { return __any(__builtin_isunordered(v[0], v[1]) || __builtin_isunordered(v[2], v[3])); }
template <int N> DEV bool has_sent(const u32x4 (&a)[N]) {
  unsigned m = 0;
#pragma unroll
  for (int k = 0; k < N; ++k) m = max(m, mx4(a[k]));
  return __any(m == SENT);
}
DEV int mrow(int i, int half) { return 4 * (i >> 1) + 2 * half + (i & 1); }
DEV unsigned foldpk(f32x4 v) { return pk(v[0] + v[2], v[1] + v[3]); }
DEV void ldsw32(unsigned a, unsigned v) { *(LAS unsigned*)(uintptr_t)a = v; }
DEV void mem_wait(rsrc_t rs, unsigned sb, int lane, int& budget) {
  for (;;) {
    const unsigned u = ld32(rs, lane * 128, sb), v = ld32(rs, C_MEM1 + (lane & 15) * 128, sb);
    CFENCE();
    if (__all(u != SENT && v != SENT) || --budget <= 0) break;
    __builtin_amdgcn_s_sleep(PSLEEP);
  }
}
template <int NT, int NK>
DEV void project(rsrc_t rs, const unsigned (&vo)[10], unsigned sb, const u32x8 (&w)[40], int idx, u32x4 (&am)[10], f32x4 (&acc)[NT]) {
#pragma unroll
  for (int k = 0; k < 8; ++k) {
    am[k] = ld128(rs, vo[k], sb);
    if (k < 7) NOPM(PACE); else CFENCE();
  }
  static_for<NK>([&](auto K) {
    constexpr int ks = K;
    smf_w<ks * 2>(acc[0], am[ks], w, idx); smf_w<ks * 2 + 1>(acc[1], am[ks], w, idx);
    if constexpr (ks < 2) { CFENCE(); am[8 + ks] = ld128(rs, vo[8 + ks], sb); CFENCE(); }
    if constexpr (NT == 4) { smf_w<20 + ks * 2>(acc[2], am[ks], w, idx); smf_w<20 + ks * 2 + 1>(acc[3], am[ks], w, idx); }
  });
}
#define DPL " row_ror:8 row_mask:0xf bank_mask:0x3\n\t"
#define DPH " row_ror:8 row_mask:0xf bank_mask:0xc\n\t"
#define DPA " quad_perm:[2,3,0,1] row_mask:0xf bank_mask:0xf\n\t"
#define FADD2(d0, d1, c) asm volatile("v_add_f32 %0, %2, %4\n\tv_add_f32 %1, %3, %5" : "=&v"(d0), "=&v"(d1) : "v"(c[0]), "v"(c[1]), "v"(c[2]), "v"(c[3]))

constexpr unsigned P_RING = 16384;
#define ROWRED(op) "s_nop 1\n\t" op "_dpp %7, %6, %6" DPA op "_dpp %8, %6, %6" DPA "s_nop 1\n\tv_permlane16_swap_b32 %7, %8\n\t" op " %6, %7, %8\n\t" op " %7, %7, %8\n\t" \
                   "s_nop 1\n\tv_permlane32_swap_b32 %6, %7\n\t" op " %6, %6, %7\n\t"
#define SM_SELM "v_cndmask_b32 %4, %24, %13, %26\n\tv_cndmask_b32 %5, %25, %14, %26\n\t"
#define SM_SELX "v_cndmask_b32 %0, %20, %15, %26\n\tv_cndmask_b32 %1, %21, %16, %26\n\tv_cndmask_b32 %2, %22, %17, %26\n\tv_cndmask_b32 %3, %23, %18, %26\n\t"
#define SM_EXPM "v_fma_f32 %4, %4, %19, -%12\n\tv_fma_f32 %5, %5, %19, -%12\n\tv_exp_f32 %4, %4\n\tv_exp_f32 %5, %5\n\t"
#define SM_EXPX "v_fma_f32 %0, %0, %19, -%12\n\tv_fma_f32 %1, %1, %19, -%12\n\tv_exp_f32 %0, %0\n\tv_fma_f32 %2, %2, %19, -%12\n\tv_exp_f32 %1, %1\n\tv_fma_f32 %3, %3, %19, -%12\n\tv_exp_f32 %2, %2\n\t" \
                "v_add_f32 %6, %4, %5\n\tv_exp_f32 %3, %3\n\tv_add_f32 %7, %0, %1\n\tv_add_f32 %6, %6, %2\n\tv_add_f32 %7, %7, %3\n\tv_add_f32 %6, %6, %7\n\t"
#define SM_OUT "v_rcp_f32 %7, %6\n\tv_med3_f32 %8, %6, %27, %28\n\t" \
               "v_mul_f32 %0, %0, %7\n\tv_mul_f32 %1, %1, %7\n\tv_cmp_eq_f32 %11, %8, %6\n\tv_mul_f32 %2, %2, %7\n\tv_mul_f32 %3, %3, %7\n\t" \
               "v_cvt_pk_bf16_f32 v248, |%0|, |%1|\n\tv_cvt_pk_bf16_f32 v250, |%0|, |%1|\n\tv_cvt_pk_bf16_f32 v249, |%2|, |%3|\n\tv_cvt_pk_bf16_f32 v251, |%2|, |%3|\n\t" \
               "v_permlane32_swap_b32 v248, v250\n\tv_mul_f32 %4, %4, %7\n\tv_permlane32_swap_b32 v249, v251\n\tv_mul_f32 %5, %5, %7"
#define SM_OPS : "=&v"(x0), "=&v"(x1), "=&v"(x2), "=&v"(x3), "=&v"(y0), "=&v"(y1), "=&v"(sum), "=&v"(t1), "=&v"(t2), "=&{v[248:251]}"(ox), "=&v"(t3), "=&s"(okm), "+v"(mref) \
               : "v"(s0[2]), "v"(s0[3]), "v"(s2[0]), "v"(s2[1]), "v"(s2[2]), "v"(s2[3]), "s"(cs), "v"(s1[0]), "v"(s1[1]), "v"(s1[2]), "v"(s1[3]), "v"(s0[0]), "v"(s0[1]), \
                 "s"(0xCCCCCCCCCCCCCCCCull), "v"(lo), "v"(hi)
template <int CLS>
DEV void p_loop(const Args& a, rsrc_t rs, int h, int wave, int lane, unsigned pol, const u32x8 (&w)[40], Sched& sc) {
  const int n = lane & 15, g = lane >> 4;
  const int idx = (lane & 2) ? 0xEEEE : 0x4444;
  const float NEG = -3.0e38f;
  const unsigned l16 = lane * 16, cpy = (h & (REPL - 1)) * 65536;
  unsigned vo[10];
#pragma unroll
  for (int k = 0; k < 10; ++k) { vo[k] = l16 + memoff(k); PIN(vo[k]); }
  f32x4 i1, i2;
#pragma unroll
  for (int r = 0; r < 4; ++r) {
    const bool tri = (4 * g + r) <= n;
    i1[r] = (wave == 1 && !tri) ? NEG : 0.f;
    i2[r] = (wave == 1 || (wave == 2 && !tri)) ? NEG : 0.f;
  }
  unsigned kvo[2];
#pragma unroll
  for (int tl = 0; tl < 2; ++tl) {
    int tok = 16 * tl + n;
    if (CLS == 0) { const int gp = n >> 2, r = n & 3; tok = 16 * (r >> 1) + 8 * (gp & 1) + 4 * (gp >> 1) + 2 * tl + (r & 1); }
    kvo[tl] = P_RING + (tok >> 4) * 4096 + (g * 16 + (tok & 15)) * 16;
  }
  const unsigned qvo = P_RING + 8192 + (wave == 2 ? 4096 : 0) + l16;
  unsigned wra = (32 * wave + n) * 16 + g * 4, wrq = 2 * wra;
  unsigned tra = (4 * g + ((lane >> 2) & 3)) * 16 + (lane & 1) * 8, trq = (4 * g + ((lane >> 2) & 3)) * 32 + (lane & 3) * 8;
  PIN(wra); PIN(wrq); PIN(tra); PIN(trq);
  unsigned vox[2], vom[2];
#pragma unroll
  for (int c = 0; c < 2; ++c) {
    if (CLS == 0) {
      vox[c] = (2 * c + (lane >> 5)) * 65536 + C_PX + (h >> 4) * 16384 + (h & 15) * 512 + (lane & 31) * 16;
      vom[c] = lane < 32 ? (2 * c + (lane >> 4)) * 65536 + C_PM + h * 128 + (lane & 15) * 8 : OOB;
      PIN(vox[c]); PIN(vom[c]);
    } else { vox[c] = (h * 2 + (wave - 1)) * 1024 + l16; vom[c] = lane < 16 ? 65536 + (wave - 1) * 8192 + h * 256 + l16 : OOB; }
  }
  const unsigned pvo = prog_off(0, h, (lane >> 2) & 1, lane & 3);
  const float cs = 0.08838834764831845f * 1.4426950408889634f;
  float mref = 0.f, lo = 0x1p-64f, hi = 0x1p64f;
  PIN(lo); PIN(hi);
  int budget = SPIN_CAP, rdy = 0;
  auto fetch = [&](int seg) {
    while (rdy <= seg && --budget > 0) { rdy = wmin(2 * (int)ld32(rs, pvo, 0) + ((lane >> 2) & 1)); CFENCE(); }
    static_for<4>([&](auto J) {
      const unsigned char* src = a.ws + kq_addr(J >> 1, seg, h, pol) + (J & 1) * 4096;
      set_m0(P_RING + (seg & 1) * 16384 + J * 4096); NOP(0);
      glds_off<0>(src, l16); glds_off<1024>(src, l16); glds_off<2048>(src, l16); glds_off<3072>(src, l16);
    });
  };
  u32x4 kx[2][4], qf[4];
  auto pull = [&](int seg) {
    const unsigned rb = (seg & 1) * 16384;
#pragma unroll
    for (int ks = 0; ks < 4; ++ks) { kx[0][ks] = ldsr128(kvo[0] + rb + ks * 1024); kx[1][ks] = ldsr128(kvo[1] + rb + ks * 1024); if (CLS == 1) qf[ks] = ldsr128(qvo + rb + ks * 1024); }
  };
  if (CLS == 2) { fetch(0); VMWAIT(0); }
  BAR();
  EV(2);
  if (CLS == 2) fetch(1); else pull(0);
  unsigned hold = now8(), pend = 0;
  for (int s = 0; s < NSEG; ++s) {
    unsigned sb = slot_base(s, pol), sbr = sb + cpy;
    const float n0 = g >= 2 ? NEG : 0.f;
    f32x4 s0 = {n0, n0, n0, n0}, s1 = i1, s2 = i2;
    PINS(sb); PINS(sbr);
    if (CLS < 2) {
      PIN(s0); PIN(s1); PIN(s2);
#pragma unroll
      for (int ks = 0; ks < 4; ++ks) { PIN(kx[0][ks]); PIN(kx[1][ks]); }
    }
    if (CLS == 1) {
      NOP(1);
#pragma unroll
      for (int ks = 0; ks < 4; ++ks) { mf_vv(s1, kx[0][ks], qf[ks]); mf_vv(s2, kx[1][ks], qf[ks]); }
    }
    unsigned cst = 0;
    if (CLS == 2) cst = trigger(rs, sc, 2 * s, hold, [&]() { mem_wait(rs, sbr, lane, budget); });
    f32x4 acc[4];
    zero_acc(acc);
    STAMP_DECL;
    unsigned good = 2;
    if (CLS == 2) release(cst); else BAR();
    STAMP(0);
    for (;;) {
      u32x4 am[10];
      project<4, 8>(rs, vo, sbr, w, idx, am, acc);
      float a0, a1, b0, b1;
      smf_w<16>(acc[0], am[8], w, idx); smf_w<17>(acc[1], am[8], w, idx); smf_w<18>(acc[0], am[9], w, idx); smf_w<19>(acc[1], am[9], w, idx);
      smf_w<36>(acc[2], am[8], w, idx); smf_w<37>(acc[3], am[8], w, idx); smf_w<38>(acc[2], am[9], w, idx);
      FADD2(a0, a1, acc[0]);
      smf_w<39>(acc[3], am[9], w, idx);
      FADD2(b0, b1, acc[1]);
      STAMP(1);
      asm volatile("ds_write2_b32 %0, %1, %1 offset0:0 offset1:1" :: "v"(wrq), "v"(pk(a0, a1)) : "memory");
      asm volatile("ds_write2_b32 %0, %1, %1 offset0:128 offset1:129" :: "v"(wrq), "v"(pk(b0, b1)) : "memory");
      const bool bad = __any(__builtin_isunordered(a0, a1));
      PIN(acc[2]);
      const unsigned f2 = foldpk(acc[2]);
      pin1(f2);
      PIN(acc[3]);
      ldsw32(wra + 4096, f2); ldsw32(wra + 4096 + 256, foldpk(acc[3]));
      if (__builtin_expect(!bad, 1)) break;
      if ((!(good & 2) && !has_sent(am)) || --budget <= 0) break;
      good = 0;
      zero_acc(acc);
      mem_wait(rs, sbr, lane, budget);
    }
    BAR();
    STAMP(2);
    u32x4 km[4];
#define TRQ(ks) qf[ks] = cat4(trread(trq + (ks) * 1024), trread(trq + (ks) * 1024 + 512))
#define TRK(ks) km[ks] = cat4(trread(tra + 4096 + (ks) * 512), trread(tra + 4096 + (ks) * 512 + 256))
    if (CLS == 0) {
      TRQ(0); TRQ(1); TRK(0); TRK(1); CFENCE();
      mf_vv(s1, kx[0][0], qf[0]); CFENCE(); TRQ(2); CFENCE(); mf_vv(s2, kx[1][0], qf[0]); CFENCE(); TRK(2); CFENCE();
      mf_vv(s1, kx[0][1], qf[1]); CFENCE(); TRQ(3); CFENCE(); mf_vv(s2, kx[1][1], qf[1]); CFENCE(); TRK(3); CFENCE();
      mf_vv(s0, km[0], qf[0]); mf_vv(s0, km[1], qf[1]); mf_vv(s0, km[2], qf[2]); mf_vv(s0, km[3], qf[3]);
      mf_vv(s1, kx[0][2], qf[2]); mf_vv(s2, kx[1][2], qf[2]); mf_vv(s1, kx[0][3], qf[3]); mf_vv(s2, kx[1][3], qf[3]);
      STAMP(3);
      float x0, x1, x2, x3, y0, y1, sum, t1, t2, t3;
      u32x4 ox; u32x2 om; unsigned long long okm;
      asm volatile(SM_SELM SM_EXPM "s_nop 1\n\t" SM_SELX SM_EXPX ROWRED("v_add_f32") SM_OUT SM_OPS);
      if (__builtin_expect(okm != ~0ull, 0))
        asm volatile(SM_SELM SM_SELX "v_max3_f32 %6, %4, %5, %0\n\tv_max3_f32 %7, %1, %2, %3\n\tv_max_f32 %6, %6, %7\n\t" ROWRED("v_max_f32") "v_mul_f32 %12, %19, %6\n\t"
                     SM_EXPM SM_EXPX ROWRED("v_add_f32") SM_OUT SM_OPS);
      st128(rs, ox, vox[0], sb); st128(rs, ox, vox[1], sb);
      asm volatile("v_cvt_pk_bf16_f32 v252, |%1|, |%2|\n\tv_cvt_pk_bf16_f32 v253, |%1|, |%2|\n\ts_nop 1\n\tv_permlane16_swap_b32 v252, v253" : "=&{v[252:253]}"(om) : "v"(y0), "v"(y1) : "memory");
      st64(rs, om, vom[0], sb); st64(rs, om, vom[1], sb);
      CFENCE();
      STAMP(4);
      STAMP_OUT(h, s);
      report(rs, h, s, good, lane);
      mref += __builtin_amdgcn_logf(sum);
      if (s >= 1) {
        const unsigned rb = slot_base(s - 1, pol);
#pragma unroll
        for (int c = 0; c < 2; ++c) { st128(rs, (u32x4){SENT, SENT, SENT, SENT}, vox[c], rb); st64(rs, (u32x2){SENT, SENT}, vom[c], rb); }
      }
      if (s + 1 < NSEG) pull(s + 1);
    } else if (CLS == 1) {
      TRK(0); TRK(1); TRK(2); TRK(3);
#pragma unroll
      for (int ks = 0; ks < 4; ++ks) mf_vv(s0, km[ks], qf[ks]);
      asm volatile("s_nop 7" : "+v"(s0), "+v"(s1), "+v"(s2));
      float mxv = fmaxf(fmaxf(fmaxf(s1[0], s1[1]), fmaxf(s1[2], s1[3])), fmaxf(fmaxf(s2[0], s2[1]), fmaxf(s2[2], s2[3])));
      mxv = fmaxf(fmaxf(fmaxf(s0[0], s0[1]), fmaxf(s0[2], s0[3])), mxv);
      mxv = xmax32(xmax16(mxv));
      const float mb = -mxv * cs;
      float sum = 0.f;
#pragma unroll
      for (int r = 0; r < 4; ++r) {
        s0[r] = __builtin_amdgcn_exp2f(fmaf(s0[r], cs, mb)); s1[r] = __builtin_amdgcn_exp2f(fmaf(s1[r], cs, mb)); s2[r] = __builtin_amdgcn_exp2f(fmaf(s2[r], cs, mb));
        sum += s0[r] + s1[r] + s2[r];
      }
      sum = xadd32(xadd16(sum));
      const float inv = __builtin_amdgcn_rcpf(sum);
      s0 *= inv; s1 *= inv; s2 *= inv;
      const unsigned so = OFF_PT + s * 81920;
      const u32x2 pmv = pk4(s0);
      auto r0 = __builtin_amdgcn_permlane16_swap(pmv.x, pmv.x, false, false);
      auto r1 = __builtin_amdgcn_permlane16_swap(pmv.y, pmv.y, false, false);
      const u32x4 ptx = cat4(pk4(s1), pk4(s2));
      if (s + 1 < NSEG) { pull(s + 1); __builtin_amdgcn_s_sleep(PTSLEEP); }
      st128(rs, ptx, vox[0], so);
      st128(rs, (u32x4){(unsigned)r0[0], (unsigned)r1[0], (unsigned)r0[1], (unsigned)r1[1]}, vom[0], so);
      STAMP(4);
      STAMP_OUT(h, s);
    } else {
      const unsigned tend = now8() + 24 * 8;
      const u32x4 cur = ld128(rs, 0, OFF_CTL + CTL_CUR);
      rdy = max(rdy, probe_rdy(pend, s, lane));
      adopt_cur(sc, cur, 2 * s + 2);
      mark_overrun(sc, 2 * s, tend);
      if (s + 2 < NSEG) fetch(s + 2);
      pend = 0;
      if (rdy <= s + 5) pend = probe_ld(rs, s + 1, lane);
      STAMP_OUT(h, s);
      hold = tend + HOLDP; clk_update(sc);
    }
  }
  VMWAIT(0);
  BAR();
  if (CLS == 2) st128(rs, (u32x4){1, 1, 1, 1}, lane < 8 ? lane * 16 : OOB, OFF_REP + (NSEG * 128 + h) * 128);
  EV(3);
}
DEV void role_p(const Args& a, rsrc_t rs, int h, int wave, int lane, unsigned pol) {
  u32x8 w[40];
  Sched sc; clk_init(sc);
  load_weights<K_P>(w, a.wq + h * 256, a.wk + h * 256, wave, lane, 15, []() { VMWAIT(0); });
  BAR();
  EV(1);
  if (wave == 0) p_loop<0>(a, rs, h, wave, lane, pol, w, sc);
  else if (wave < 3) p_loop<1>(a, rs, h, wave, lane, pol, w, sc);
  else p_loop<2>(a, rs, h, wave, lane, pol, w, sc);
}

DEV void role_v(const Args& a, rsrc_t rs, int h, int wave, int lane, unsigned pol) {
  u32x8 w[40];
  Sched sc; clk_init(sc);
  load_weights<K_V>(w, a.wv + h * 256, a.wo + (size_t)h * 128 * 1280, wave, lane, 15, []() { VMWAIT(0); });
  BAR();
  EV(1);
  const int n = lane & 15, g = lane >> 4;
  const int idx = (lane & 2) ? 0xEEEE : 0x4444;
  unsigned wra = (16 * wave + (n >> 2) * 2 + (n & 1)) * 32 + g * 8 + ((n >> 1) & 1) * 4, tra = (8 * g + ((lane >> 2) & 3)) * 32 + (lane & 3) * 8;
  unsigned zv0 = g < 2 ? (wave * 2 + g) * 16384 + h * 256 + n * 16 : OOB;
  unsigned zv1 = (8 + (g >> 1) * 8 + wave * 2 + (g & 1)) * 16384 + h * 256 + n * 16, zv2 = zv1 + 16 * 16384;
  PIN(wra); PIN(tra); PIN(zv0); PIN(zv1); PIN(zv2);
  unsigned vo[10];
#pragma unroll
  for (int k = 0; k < 10; ++k) { vo[k] = lane * 16 + memoff(k); PIN(vo[k]); }
  int budget = SPIN_CAP;
  unsigned hold = now8();
  for (int s = 0; s < NSEG; ++s) {
    unsigned sb = slot_base(s, pol) + (h & (REPL - 1)) * 65536, zb = z_base(s, pol);
    PINS(sb); PINS(zb);
    f32x4 c[10], acc[2];
    zero_acc(c); zero_acc(acc);
    unsigned cst;
    if (wave == 3) cst = trigger(rs, sc, 2 * s, hold, [&]() { mem_wait(rs, sb, lane, budget); }); else cst = cycles();
    STAMP_DECL;
    unsigned good = 2;
    release(__builtin_amdgcn_readfirstlane(cst));
    STAMP(0);
    for (;;) {
      u32x4 am[10];
      project<2, 10>(rs, vo, sb, w, idx, am, acc);
      STAMP(1);
      asm volatile("s_nop 7" : "+v"(acc[0]), "+v"(acc[1]));
      ldsw32(wra, foldpk(acc[0])); ldsw32(wra + 256, foldpk(acc[1]));
      if (__builtin_expect(!has_nan(acc[0]), 1)) break;
      if ((!(good & 2) && !has_sent(am)) || --budget <= 0) break;
      good = 0;
      zero_acc(acc);
      mem_wait(rs, sb, lane, budget);
    }
    BAR();
    STAMP(2);
    u32x4 va[2];
#pragma unroll
    for (int ks = 0; ks < 2; ++ks) va[ks] = cat4(trread(tra + ks * 1024), trread(tra + ks * 1024 + 128));
    CFENCE(); const u32x4 cur = ld128(rs, 0, OFF_CTL + CTL_CUR); CFENCE();
    u32x4 O0, O1, O2;
#define VMM(t, k) smf_w<20 + ((t) >> 1) * 4 + 2 * (k) + ((t) & 1)>(c[t], va[k], w, idx)
#define VEA(t, T) FADD2(T##0, T##1, c[t])
#define VEC2(R, ra, rb, O) asm volatile("v_cvt_pk_bf16_f32 " ra ", %1, %2\n\tv_cvt_pk_bf16_f32 " rb ", %3, %4" : "+{" R "}"(O) : "v"(ta0), "v"(ta1), "v"(tb0), "v"(tb1))
#define VSW(txt, R, O) asm volatile(txt : "+{" R "}"(O))
    float ta0, ta1, tb0, tb1;
    asm volatile("" : "={v[232:235]}"(O0), "={v[236:239]}"(O1), "={v[240:243]}"(O2));
    VMM(0, 0); VMM(1, 0); VMM(0, 1); VMM(1, 1);
    VMM(2, 0); VMM(3, 0); VMM(2, 1); VMM(3, 1);
    VMM(4, 0); NOP(1); VMM(5, 0); VEA(0, ta); VMM(4, 1); VEA(1, tb); VMM(5, 1); VEC2("v[232:235]", "v232", "v233", O0);
    VMM(6, 0); VEA(2, ta); VMM(7, 0); VEA(3, tb); VMM(6, 1); VSW("v_permlane16_swap_b32 v232, v233", "v[232:235]", O0); VMM(7, 1); VEC2("v[236:239]", "v236", "v237", O1);
    VMM(8, 0); VSW("v_permlane32_swap_b32 v232, v234", "v[232:235]", O0); VMM(9, 0); VSW("v_permlane32_swap_b32 v233, v235", "v[232:235]", O0);
    VMM(8, 1); VEA(4, ta); VMM(9, 1); CFENCE(); st128(rs, O0, zv0, zb); CFENCE(); VEA(5, tb);
    VEC2("v[236:239]", "v238", "v239", O1);
    VEA(6, ta); VEA(7, tb);
    VSW("v_permlane32_swap_b32 v236, v238\n\tv_permlane32_swap_b32 v237, v239", "v[236:239]", O1);
    VEC2("v[240:243]", "v240", "v241", O2);
    VSW("v_permlane16_swap_b32 v236, v237\n\tv_permlane16_swap_b32 v238, v239", "v[236:239]", O1);
    CFENCE(); st128(rs, O1, zv1, zb); CFENCE();
    VEA(8, ta); VEA(9, tb); VEC2("v[240:243]", "v242", "v243", O2);
    VSW("s_nop 0\n\tv_permlane32_swap_b32 v240, v242\n\tv_permlane32_swap_b32 v241, v243\n\ts_nop 1\n\tv_permlane16_swap_b32 v240, v241\n\tv_permlane16_swap_b32 v242, v243", "v[240:243]", O2);
    CFENCE(); st128(rs, O2, zv2, zb);
    CFENCE();
    STAMP(4);
    STAMP_OUT(32 + h, s);
    if (wave == 0) report(rs, 32 + h, s, good, lane);
    if (wave == 3) { const unsigned tend = now8(); hold = tend + HOLDP; adopt_cur(sc, cur, 2 * s + 2); mark_overrun(sc, 2 * s, tend - VSLACK); clk_update(sc); }
  }
  VMWAIT(0);
  BAR();
  if (wave == 0) st128(rs, (u32x4){1, 1, 1, 1}, lane < 8 ? lane * 16 : OOB, OFF_REP + (NSEG * 128 + 32 + h) * 128);
}

#define CLOB_A "a0", "a1", "a2", "a3", "a4", "a5", "a6", "a7", "a8", "a9", "a10", "a11", "a12", "a13", "a14", "a15", "a16", "a17", "a18", "a19", "a20", "a21", "a22", "a23", "a24", "a25", "a26", "a27", "a28", "a29", "a30", "a31", "a32", "a33", "a34", "a35", "a36", "a37", "a38", "a39", "a40", "a41", "a42", "a43", "a44", "a45", "a46", "a47", "a48", "a49", "a50", "a51", "a52", "a53", "a54", "a55", "a56", "a57", "a58", "a59", "a60", "a61", "a62", "a63", "a64", "a65", "a66", "a67", "a68", "a69", "a70", "a71", "a72", "a73", "a74", "a75", "a76", "a77", "a78", "a79", "a80", "a81", "a82", "a83", "a84", "a85", "a86", "a87", "a88", "a89", "a90", "a91", "a92", "a93", "a94", "a95", "a96", "a97", "a98", "a99", "a100", "a101", "a102", "a103", "a104", "a105", "a106", "a107", "a108", "a109", "a110", "a111", "a112", "a113", "a114", "a115", "a116", "a117", "a118", "a119", "a120", "a121", "a122", "a123", "a124", "a125", "a126", "a127"
template <int J> DEV void c_ldb(unsigned l16, const unsigned char* src) {
  asm volatile("global_load_dwordx4 a[%c2:%c2+3], %0, %1 sc1\n\tglobal_load_dwordx4 a[%c2+4:%c2+7], %0, %1 offset:1024 sc1\n\t"
               "global_load_dwordx4 a[%c2+8:%c2+11], %0, %1 offset:2048 sc1\n\tglobal_load_dwordx4 a[%c2+12:%c2+15], %0, %1 offset:3072 sc1"
               :: "v"(l16), "s"(src + (J >> 1) * BSTR + (J & 1) * 4096), "n"(J * 16) : "memory", CLOB_A);
}
template <int J> DEV void c_smf(f32x4& acc, u32x4 p, int idx) { asm volatile("v_smfmac_f32_16x16x64_bf16 %0, %1, a[%c3:%c3+7], %2" : "+{v[248:251]}"(acc) : "v"(p), "v"(idx), "n"(J * 8)); }
#define ZFIRST 0
#define ASLEEP -1
#define BDELAY 30
DEV void c_arm(rsrc_t rs, int s, int cb, int lane, unsigned pol) {
  const unsigned zn = z_base(s, pol) + cb * 16384;
#pragma unroll
  for (int j = 0; j < 8; ++j) st128(rs, (u32x4){SENT, SENT, SENT, SENT}, lane * 16 + (j & 3) * 1024, zn + (j >> 2) * 4096);
}
DEV void role_c(const Args& a, rsrc_t rs, int cb, int wave, int lane, unsigned pol) {
  Sched sc; clk_init(sc);
  c_arm(rs, wave, cb, lane, pol);
  VMWAIT(0);
  BAR();
  const unsigned pvo = prog_off(1, lane >> 1, lane & 1, (cb >> 1) & 3);
  if (wave == 3) {
    int rdy = 0;
    for (int s = 0; s < NSEG - 1; ++s) {
      BAR(); BAR();
      unsigned v = 0;
      if (rdy <= s + 4) v = probe_ld(rs, s, lane);
      BAR();
      rdy = max(rdy, probe_rdy(v, s, lane));
      ldsw32(L_TM + 32, rdy);
    }
    return;
  }
  if (wave == 2) {
    const unsigned rvo = (lane >> 4) < REPL ? (lane >> 4) * 65536 + (cb < 32 ? cb * 256 : C_MEM1 + (cb - 32) * 256) + (lane & 15) * 16 : OOB;
    for (int s = 0; s < NSEG - 1; ++s) {
      BAR(); VMWAIT(0); BAR();
      if (ASLEEP >= 0) { __builtin_amdgcn_s_sleep(ASLEEP >= 0 ? ASLEEP : 0); if (s + 4 < NSEG) c_arm(rs, s + 4, cb, lane, pol); }
      BAR();
      st128(rs, (u32x4){SENT, SENT, SENT, SENT}, rvo, slot_base(s, pol));
      if (ASLEEP < 0 && s + 4 < NSEG) c_arm(rs, s + 4, cb, lane, pol);
    }
    return;
  }
  int budget = SPIN_CAP;
  const unsigned cpy = ((cb / 10) & (REPL - 1)) * 65536;
  auto c_wait = [&](unsigned sb, unsigned zb) {
    for (;;) {
      const unsigned u = ld32(rs, C_PM + (lane & 31) * 128, sb), v = ld32(rs, cb * 16384 + lane * 128, zb);
      CFENCE();
      if (__all(u != SENT && v != SENT) || --budget <= 0) break;
      __builtin_amdgcn_s_sleep(PSLEEP);
    }
  };
  if (wave == 1) {
    unsigned hold = now8();
    for (int s = 0; s < NSEG - 1; ++s) {
      release(trigger(rs, sc, 2 * s + 1, hold, [&]() { c_wait(slot_base(s, pol) + cpy, z_base(s, pol)); }));
      clk_update(sc);
      BAR();
      hold = now8();
      adopt_cur(sc, ld128(rs, 0, OFF_CTL + CTL_CUR), 2 * s + 3);
      mark_overrun(sc, 2 * s + 1, hold);
      hold += HOLDC;
      if (sc.j0 > sc.jbad) wait_until(tau_of(sc, 2 * s + 2) + BDELAY * 8);
      BAR();
    }
    return;
  }
  const int n = lane & 15, g = lane >> 4;
  const int idx = (lane & 2) ? 0xEEEE : 0x4444;
  const unsigned l16 = lane * 16;
  unsigned pxvo[4], zvo[2], pmvo = C_PM + 2 * g * 128 + n * 8;
#pragma unroll
  for (int i = 0; i < 4; ++i) { pxvo[i] = C_PX + (i >> 1) * 16384 + (i & 1) * 4096 + l16; PIN(pxvo[i]); }
#pragma unroll
  for (int i = 0; i < 2; ++i) { zvo[i] = cb * 16384 + i * 4096 + l16; PIN(zvo[i]); }
  const unsigned mvo = (cb < 32 ? cb * 256 : C_MEM1 + (cb - 32) * 256) + mrow(2 * g + (n >> 3), (n & 7) >> 2) * 16 + (n & 3) * 4;
  unsigned oob = OOB; PIN(oob); PIN(pmvo);
  {
    const unsigned short* m0 = (const unsigned short*)a.mem0 + (2 * g + (n >> 3)) * 640 + cb * 16 + (n & 7);
    const unsigned v = min(m0[0] | (unsigned)m0[8] << 16, 0xFFFFFFFEu);
#pragma unroll
    for (int c = 0; c < REPL; ++c) st32(rs, v, mvo, slot_base(0, pol) + c * 65536);
  }
  int rdy = 0;
  auto fetch = [&](int seg) {
    while (rdy <= seg && --budget > 0) { rdy = wmin(2 * (int)ld32(rs, pvo, 0) + (lane & 1)); CFENCE(); }
    const unsigned char* src = a.ws + b_addr(seg, cb, pol);
    static_for<8>([&](auto J) { c_ldb<J>(l16, src); });
  };
  fetch(0);
  for (int s = 0; s < NSEG - 1; ++s) {
    unsigned sb = slot_base(s, pol) + cpy, zb = z_base(s, pol), sbn[REPL];
#pragma unroll
    for (int c = 0; c < REPL; ++c) { sbn[c] = slot_base(s + 1, pol) + c * 65536; PINS(sbn[c]); }
    PINS(sb); PINS(zb);
    STAMP_DECL;
    unsigned good = 2;
    BAR();
    STAMP(0);
    for (;;) {
      u32x4 z[8], pm[4], px[16];
#pragma unroll
      for (int j = 0; j < ZFIRST; ++j) { z[j] = ld128(rs, zvo[j >> 2] + (j & 3) * 1024, zb); CFENCE(); }
#pragma unroll
      for (int j = 0; j < 16; ++j) px[j] = ld128(rs, pxvo[j >> 2] + (j & 3) * 1024, sb);
      CFENCE();
#pragma unroll
      for (int j = 0; j < 4; ++j) pm[j] = cat4(ld64(rs, pmvo + j * 1024, sb), ld64(rs, pmvo + j * 1024 + 128, sb));
      CFENCE();
#pragma unroll
      for (int j = ZFIRST; j < 8; ++j) { z[j] = ld128(rs, zvo[j >> 2] + (j & 3) * 1024, zb); CFENCE(); }
      f32x4 acc = {0, 0, 0, 0};
      asm volatile("s_nop 1" : "+{v[248:251]}"(acc));
      static_for<16>([&](auto J) { c_smf<J>(acc, px[J], idx); });
#pragma unroll
      for (int j = 0; j < 4; ++j) asm volatile("v_smfmac_f32_16x16x64_bf16 %0, %1, %2, %3" : "+{v[248:251]}"(acc) : "v"(pm[j]), "v"(cat8(z[2 * j], z[2 * j + 1])), "v"(idx));
      STAMP(1);
      unsigned o, vo2; unsigned long long okm;
      asm volatile(
        "s_nop 7\n\t"
        "v_add_f32 v248, v248, v250\n\t"
        "v_add_f32 v250, v249, v251\n\t"
        "v_add_f32 v249, v249, v251\n\t"
        "v_mov_b32_dpp v250, v248" DPL
        "s_nop 0\n\t"
        "v_mov_b32_dpp v248, v249" DPH
        "v_cmp_o_f32 %3, v248, v250\n\t"
        "v_cvt_pk_bf16_f32 %1, v248, v250\n\t"
        "s_nop 0\n\t"
        "v_cndmask_b32 %2, %5, %4, %3"
        : "+{v[248:251]}"(acc), "=&v"(o), "=&v"(vo2), "=&s"(okm) : "v"(mvo), "v"(oob));
#pragma unroll
      for (int c = 0; c < REPL; ++c) st32(rs, o, vo2, sbn[c]);
      CFENCE();
      if (__builtin_expect(okm == ~0ull, 1)) break;
      if ((!(good & 2) && !(has_sent(z) || has_sent(pm) || has_sent(px))) || --budget <= 0) {
#pragma unroll
        for (int c = 0; c < REPL; ++c) st32(rs, min(o, 0xFFFFFFFEu), mvo, sbn[c]);
        break;
      }
      good = 0;
      c_wait(sb, zb);
    }
    STAMP(4);
    STAMP_OUT(64 + cb, s);
    report(rs, 64 + cb, s, good, lane);
    BAR();
    if (s == 0) EV(2); if (s == NSEG - 2) EV(3);
    if (s > 0) rdy = max(rdy, (int)__builtin_amdgcn_readfirstlane(*(volatile LAS unsigned*)(uintptr_t)(L_TM + 32)));
    BAR();
    if (s + 2 < NSEG) fetch(s + 1);
  }
}

DEV void u_scatter(unsigned img, const f32x4 (&acc)[4][2], int wave, int lane) {
  const int n = lane & 15, g = lane >> 4;
#pragma unroll
  for (int c = 0; c < 4; ++c)
#pragma unroll
    for (int tt = 0; tt < 2; ++tt)
#pragma unroll
      for (int r = 0; r < 4; ++r) *(LAS float*)(uintptr_t)(img + wave * 8704 + (tt * 16 + 4 * g + r) * 272 + c * 64 + n * 4) = acc[c][tt][r];
}
DEV void u_gather(unsigned img, f32x4& v0, f32x4& v1, int tid) {
  v0 = (f32x4){0, 0, 0, 0}; v1 = v0;
#pragma unroll
  for (int ww = 0; ww < 4; ++ww) { const unsigned p = img + ww * 8704 + (tid >> 3) * 272 + (tid & 7) * 32; v0 += *(LAS f32x4*)(uintptr_t)p; v1 += *(LAS f32x4*)(uintptr_t)(p + 16); }
}
#define UOFF 240
#define UOFF_LAST 240
#define USLEEP 0
#define UPACE 20
DEV void role_u(const Args& a, rsrc_t rs, int wid, int wave, int lane, unsigned pol) {
  if (wid >= 120) return;
  const int cq = wid % 10, tid = wave * 64 + lane;
  const unsigned l16 = lane * 16;
  u32x4 w[56];
  load_w2(w, a.w2 + cq * 128, wave, lane);
  BAR();
  EV(7);
  unsigned pvo = OOB;
  if (lane < 32) pvo = prog_off(1, wave * 8 + (lane >> 2), lane & 1, (cq * 2 + ((lane >> 1) & 1)) & 3);
  const int nks = wave < 3 ? 14 : 12;
  int budget = SPIN_CAP;
  Sched sc; clk_init(sc);
  const unsigned tu0 = rtc();
  for (int seg = wid / 10; seg < NSEG; seg += 12) {
    while ((int)(rtc() - tu0) < seg * UPACE) __builtin_amdgcn_s_sleep(8);
    const int rep = seg % 9;
    if (lane >= 48) pvo = lane - 48 < nks ? hprog_off(rep, wave * 14 + lane - 48) : OOB;
    unsigned have, need = lane < 32 ? ((lane & 1) == (seg & 1) ? (seg >> 1) + 1 : 0) : (lane >= 48 && lane - 48 < nks) ? seg / 9 + 1 : 0;
    for (;;) { have = ld32(rs, pvo, 0); CFENCE(); if (__all(have >= need) || --budget <= 0) break; __builtin_amdgcn_s_sleep(4); }
    f32x4 acc[4][2];
    {
      u32x4 hf[14][2];
#pragma unroll
      for (int k = 0; k < 14; ++k)
#pragma unroll
        for (int tt = 0; tt < 2; ++tt) hf[k][tt] = ld128(rs, k < nks ? l16 + tt * 1024 : OOB, OFF_HH + (seg * 54 + wave * 14 + k) * 2048);
      static_for<14>([&](auto K) {
        static_for<4>([&](auto C) {
          constexpr int k = K, c = C, wi = ((k >> 1) * 2 + (c >> 1)) * 2 + (c & 1);
          mf_w<wi, k & 1, false, k == 0>(acc[c][0], w, hf[k][0]); mf_w<wi, k & 1, false, k == 0>(acc[c][1], w, hf[k][1]);
        });
      });
    }
    u32x4 bx[4][8];
#pragma unroll
    for (int c = 0; c < 4; ++c)
#pragma unroll
      for (int hh = 0; hh < 8; ++hh) { bx[c][hh] = ld128a<BLD_AUX>(rs, l16 + (hh & 3) * 1024, b_addr(seg, cq * 4 + c, pol) + wave * BSTR + (hh & 4) * 1024); if (USLEEP) { CFENCE(); __builtin_amdgcn_s_sleep(USLEEP); } }
    const size_t oo = (size_t)(seg * 32 + (tid >> 3)) * 1280 + cq * 128 + (tid & 7) * 16;
    const u32x4 xv = *(const u32x4*)(a.x + oo);
    acc_wait(acc);
    u_scatter(0, acc, wave, lane);
    BAR();
    f32x4 g0, g1;
    u_gather(0, g0, g1, tid);
#pragma unroll
    for (int r = 0; r < 4; ++r) { g0[r] = rbf(silu(rbf(g0[r]))); g1[r] = rbf(silu(rbf(g1[r]))); }
#pragma unroll
    for (int hh = 0; hh < 8; ++hh) asm volatile("" : "+a"(bx[0][hh]));
    {
      const bool act = lane < (seg == NSEG - 1 ? 16 : 8);
      const unsigned rvo = act ? ((seg + 1) * 128 + (lane >> 3) * 32 + wave * 8 + (lane & 7)) * 128 : OOB;
      sleep_until_turn(rs, sc, seg == NSEG - 1 ? 2 * seg + 1 : 2 * seg + 3, seg == NSEG - 1 ? UOFF_LAST : UOFF);
      for (;;) { const unsigned v = ld32(rs, rvo, OFF_REP); CFENCE(); if (__all(!act || v != 0) || --budget <= 0) break; if (seg == NSEG - 1) __builtin_amdgcn_s_sleep(2); else __builtin_amdgcn_s_sleep(16); }
    }
    EV(8);
    const unsigned so = OFF_PT + seg * 81920;
#pragma unroll
    for (int c = 0; c < 4; ++c) { acc[c][0] = (f32x4){0, 0, 0, 0}; acc[c][1] = (f32x4){0, 0, 0, 0}; }
    {
      u32x4 px[8][2];
#pragma unroll
      for (int hh = 0; hh < 8; ++hh)
#pragma unroll
        for (int tt = 0; tt < 2; ++tt) px[hh][tt] = ld128(rs, l16 + tt * 1024, so + (wave * 8 + hh) * 2048);
      acc_wait(acc);
#pragma unroll
      for (int hh = 0; hh < 4; ++hh)
#pragma unroll
        for (int c = 0; c < 4; ++c) { if (c == 0) { mf_va(acc[c][0], px[hh][0], bx[c][hh]); mf_va(acc[c][1], px[hh][1], bx[c][hh]); } else { mf_vv(acc[c][0], px[hh][0], bx[c][hh]); mf_vv(acc[c][1], px[hh][1], bx[c][hh]); } }
      CFENCE();
      u32x4 pm[2][2], z[4][2];
#pragma unroll
      for (int hf = 0; hf < 2; ++hf)
#pragma unroll
        for (int tt = 0; tt < 2; ++tt) pm[hf][tt] = ld128(rs, l16 + hf * 1024, so + 65536 + tt * 8192 + wave * 2048);
#pragma unroll
      for (int c = 0; c < 4; ++c)
#pragma unroll
        for (int hf = 0; hf < 2; ++hf) z[c][hf] = ld128(rs, l16 + hf * 1024, z_base(seg, pol) + (cq * 4 + c) * 16384 + wave * 2048);
      CFENCE();
#pragma unroll
      for (int hh = 4; hh < 8; ++hh)
#pragma unroll
        for (int c = 0; c < 4; ++c) { if (c == 0) { mf_va(acc[c][0], px[hh][0], bx[c][hh]); mf_va(acc[c][1], px[hh][1], bx[c][hh]); } else { mf_vv(acc[c][0], px[hh][0], bx[c][hh]); mf_vv(acc[c][1], px[hh][1], bx[c][hh]); } }
#pragma unroll
      for (int hf = 0; hf < 2; ++hf)
#pragma unroll
        for (int c = 0; c < 4; ++c) { mf_vv(acc[c][0], pm[hf][0], z[c][hf]); mf_vv(acc[c][1], pm[hf][1], z[c][hf]); }
    }
    acc_wait(acc);
    u_scatter(40960, acc, wave, lane);
    BAR();
    f32x4 s0, s1;
    u_gather(40960, s0, s1, tid);
    u32x4 o;
#pragma unroll
    for (int j = 0; j < 2; ++j) {
      o[j] = pk(__builtin_bit_cast(float, xv[j] << 16) + rbf(rbf(s0[2 * j]) * g0[2 * j]), __builtin_bit_cast(float, xv[j] & 0xFFFF0000u) + rbf(rbf(s0[2 * j + 1]) * g0[2 * j + 1]));
      o[2 + j] = pk(__builtin_bit_cast(float, xv[2 + j] << 16) + rbf(rbf(s1[2 * j]) * g1[2 * j]), __builtin_bit_cast(float, xv[2 + j] & 0xFFFF0000u) + rbf(rbf(s1[2 * j + 1]) * g1[2 * j + 1]));
    }
    *(u32x4*)(a.out + oo) = o;
    EV(9);
  }
}

#define WDELAY 18
#define WPACE 0
#define WBLAG 1
#define WAUX 16
#define WAHEAD 3
DEV void role_w(rsrc_t rs, int xq, int k, int wave, int lane, unsigned pol) {
  const int cb0 = 10 * xq + 2 * k;
  unsigned sink = 0;
  int budget = SPIN_CAP;
  Sched sc; clk_init(sc);
  for (int seg = 1 + wave; seg < NSEG; seg += 4) {
    const int s = seg - WAHEAD;
    if (s >= 2) {
      while (sc.j0 == 0 && --budget > 0) { adopt_cur(sc, ld128(rs, 0, OFF_CTL + CTL_CUR), 2 * NSEG); CFENCE(); __builtin_amdgcn_s_sleep(8); }
      sleep_until_turn(rs, sc, 2 * s + 1, WDELAY * 8 - 240);
    }
    while (__builtin_amdgcn_readfirstlane(ld32(rs, seg * 128, OFF_CTL + CTL_RL)) == 0 && --budget > 0) { CFENCE(); __builtin_amdgcn_s_sleep(8); }
    if (s >= 2) wait_until(tau_of(sc, 2 * s + 1) + WDELAY * 8);
#define WLANES 64
    constexpr int NI = 64 / WLANES;
    unsigned v[12 * NI];
    const unsigned wvo = lane < WLANES ? lane * 128 : OOB;
#pragma unroll
    for (int j = 0; j < 12 * NI; ++j) {
      const int b = j / NI, u = min(k + 5 * (b - 8), 15);
      const unsigned so = b < 8 ? b_addr(max(seg - WBLAG, 0), cb0 + (b >> 2), pol) + (b & 3) * BSTR : kq_addr(u & 1, seg, (u >> 1) * 4 + xq, pol);
      v[j] = __builtin_amdgcn_raw_buffer_load_b32(rs, wvo, so + (j % NI) * WLANES * 128, WAUX); CFENCE(); __builtin_amdgcn_s_sleep(WPACE);
    }
#pragma unroll
    for (int j = 0; j < 12 * NI; ++j) sink += v[j];
    CFENCE();
  }
  asm volatile("" :: "v"(sink));
}

#define LEAD1 84
#define LEAD2 64
#define GEXTRA 300
#define ATOL 8
#define NADAPT 8
#define TKOFF 200
#define TKSLEEP 2
#define GPOLL 2700
#define GPOLL0 2600
DEV void role_tk(rsrc_t rs, int lane) {
  Sched sc; clk_init(sc);
  Sched old = sc;
  int budget = SPIN_CAP, run = 0, m1 = 0, m2 = 0, relock = 0;
  unsigned Mp1 = 0, Mp2 = 0;
  const unsigned tref = now8();
  for (int j = 0; j < 2 * NSEG - 6; ++j) {
    const int s = j >> 1;
    const unsigned vo = rep_off(j, lane);
    u32x4 e;
    if (sc.j0 != 0 && j >= relock) wait_until((sc.j0 <= j + 1 ? tau_of(sc, j + 1) : tau_of(old, j + 1)) + TKOFF);
    for (;;) { e = ld128(rs, vo, OFF_REP); CFENCE(); if (__all(e.x != 0) || --budget <= 0) break; __builtin_amdgcn_s_sleep(TKSLEEP); }
    const bool zrow = !(j & 1) && lane >= 32;
    const unsigned M = tref + (unsigned)wmax((int)(e.y - tref) - (zrow ? VSLACK : 0));
    if (j + 1 >= relock) {
      const unsigned tn = sc.j0 <= j + 1 ? tau_of(sc, j + 1) : tau_of(old, j + 1);
      if (sc.j0 == 0 || (int)(M - tn) > LATE_THR) {
        int gap = sc.j0 == 0 ? GPOLL0 : GPOLL;
        if (j + 1 <= relock + 1 && j >= 2) gap = max(gap, min((int)(M - Mp2) + GEXTRA, 3600));
        sc.rx = j + 3;
        sc.base += M + gap - tau_of(sc, j + 3); sc.j0 = relock = j + 3;
        st128(rs, (u32x4){(unsigned)sc.j0, sc.base, (unsigned)sc.T, (unsigned)sc.D1 | (unsigned)sc.rx << 16}, lane < 8 ? lane * 16 : OOB, OFF_CTL + CTL_CUR);
        run = 0; m1 = 0; m2 = 0;
      }
    }
    Mp2 = Mp1; Mp1 = M;
    const int du = (int)((float)(int)(e.z >> 2) * 2048.f * __builtin_amdgcn_rcpf((float)e.w)) - (zrow ? VSLACK : 0);
    if (sc.j0 <= j && __all((e.z & 3) == 3)) {
      const int d = wmax(du);
      if (j & 1) m2 += d; else m1 += d;
      if (++run == NADAPT) {
        const int n1 = m1 / (NADAPT / 2) + LEAD1, n2 = m2 / (NADAPT / 2) + LEAD2;
        if (abs(n1 - sc.D1) > ATOL || abs(n2 - (sc.T - sc.D1)) > ATOL) {
          old = sc;
          sc.base += (s + 3) * (sc.T - (n1 + n2)); sc.T = n1 + n2; sc.D1 = n1; sc.j0 = 2 * (s + 3);
          st128(rs, (u32x4){(unsigned)sc.j0, sc.base, (unsigned)sc.T, (unsigned)sc.D1 | (unsigned)sc.rx << 16}, lane < 8 ? lane * 16 : OOB, OFF_CTL + CTL_CUR);
        }
        run = 0; m1 = 0; m2 = 0;
      }
    } else { run = 0; m1 = 0; m2 = 0; }
  }
}

DEV float rowsum16(float v) {
  v += __builtin_bit_cast(float, __builtin_amdgcn_update_dpp(0, __builtin_bit_cast(int, v), 0xB1, 0xf, 0xf, false));
  v += __builtin_bit_cast(float, __builtin_amdgcn_update_dpp(0, __builtin_bit_cast(int, v), 0x4E, 0xf, 0xf, false));
  v += __builtin_bit_cast(float, __builtin_amdgcn_update_dpp(0, __builtin_bit_cast(int, v), 0x124, 0xf, 0xf, false));
  v += __builtin_bit_cast(float, __builtin_amdgcn_update_dpp(0, __builtin_bit_cast(int, v), 0x128, 0xf, 0xf, false));
  return v;
}
DEV void layernorm(const Args& a, rsrc_t rs, int seg, int wave, int lane) {
  const int sub = lane & 15;
  u32x4 v[2][5];
#pragma unroll
  for (int p = 0; p < 2; ++p)
#pragma unroll
    for (int i = 0; i < 5; ++i) v[p][i] = *(const u32x4*)(a.x + (size_t)(seg * 32 + wave * 8 + p * 4 + (lane >> 4)) * 1280 + (sub + 16 * i) * 16);
#pragma unroll
  for (int p = 0; p < 2; ++p) {
    float f[40], sum = 0.f;
#pragma unroll
    for (int i = 0; i < 5; ++i)
#pragma unroll
      for (int j = 0; j < 4; ++j) { f[i * 8 + 2 * j] = __builtin_bit_cast(float, v[p][i][j] << 16); f[i * 8 + 2 * j + 1] = __builtin_bit_cast(float, v[p][i][j] & 0xFFFF0000u); sum += f[i * 8 + 2 * j] + f[i * 8 + 2 * j + 1]; }
    const float mean = rowsum16(sum) * (1.f / 640.f);
    float sq = 0.f;
#pragma unroll
    for (int i = 0; i < 40; ++i) { const float d = f[i] - mean; sq = fmaf(d, d, sq); }
    const float mb = rbf(mean), vb = rbf(rowsum16(sq) * (1.f / 640.f));
    const float inv = 1.f / rbf(sqrtf(rbf(vb + 1e-5f)));
    const int row = wave * 8 + p * 4 + (lane >> 4);
#pragma unroll
    for (int i = 0; i < 5; ++i) {
      u32x4 o;
#pragma unroll
      for (int j = 0; j < 4; ++j) o[j] = pk(rbf(f[i * 8 + 2 * j] - mb) * inv, rbf(f[i * 8 + 2 * j + 1] - mb) * inv);
      const int c = sub + 16 * i;
      ldsw128(((c >> 2) * 2 + (row >> 4)) * 1024 + ((c & 3) * 16 + (row & 15)) * 16, o);
    }
  }
  BAR();
#pragma unroll
  for (int i = 0; i < 10; ++i) st128(rs, ldsr128((wave * 10 + i) * 1024 + lane * 16), lane * 16, OFF_XN + seg * 40960 + (wave * 10 + i) * 1024);
  VMWAIT(0);
  BAR();
  if (wave == 0) st32(rs, 1, lane < 32 ? seg * 128 + lane * 4 : OOB, OFF_CTL + CTL_XP);
}

__global__ void __launch_bounds__(256, 1) k_main(Args a) {
  const int lane = threadIdx.x & 63, wave = __builtin_amdgcn_readfirstlane(threadIdx.x >> 6);
  const rsrc_t rs = __builtin_amdgcn_make_buffer_rsrc(a.ws, 0, WS_BYTES, 0x00020000);
  const u32x4 pr = ld128(rs, 0, OFF_CTL + CTL_POL);
  const unsigned xoff = __builtin_amdgcn_readfirstlane(ld32(rs, 16, OFF_CTL + CTL_POL));
  const int b = blockIdx.x, xq = (b + xoff) & 7, ix = b >> 3;
  EV(0);
  const unsigned pol = __builtin_amdgcn_readfirstlane((pr.y < pr.x ? 1u : 0u) | (pr.w < pr.z ? 2u : 0u)) ^ ((xoff & 4) ? 3u : 0u);
  if (xq < 4) {
    const int r = ix * 4 + xq;
    if (r < 32) role_p(a, rs, r, wave, lane, pol);
    else if (r < 64) role_v(a, rs, r - 32, wave, lane, pol);
    else {
      layernorm(a, rs, r - 64, wave, lane);
      EV(1);
      if (r < 104) role_c(a, rs, 10 * xq + ((r - 64) >> 2), wave, lane, pol);
      else if (r < 124) role_w(rs, xq, (r - 104) >> 2, wave, lane, pol);
      else if (r == 124 && wave == 0) role_tk(rs, lane);
      else if (r == 125 && wave == 0) role_agg(rs, lane);
    }
  } else {
    const int pid = (xq - 4) * 16 + (ix >> 1), par = ix & 1;
    const int wid = (xq - 4) * 32 + ix;
    if (pid & 1) role_x<K_VB>(a, rs, pid >> 1, par, 2, prog_off(1, pid >> 1, par, wave), wave, lane, pol);
    else role_x<K_QK>(a, rs, pid >> 1, par, 2, prog_off(0, pid >> 1, par, wave), wave, lane, pol);
    if (wid < 126) role_x<K_F1>(a, rs, (wid % 14) * 4 + wave, wid / 14, 9, hprog_off(wid / 14, (wid % 14) * 4 + wave), wave, lane, pol);
    role_u(a, rs, wid, wave, lane, pol);
  }
}

__global__ void __launch_bounds__(256) k_pro(Args a) {
  const int b = blockIdx.x, tid = threadIdx.x, lane = tid & 63, wave = __builtin_amdgcn_readfirstlane(tid >> 6);
  if (b < 32) {
    if ((b & 7) == 0 && wave == 0) {
      const int j = b >> 3;
      const rsrc_t rs = __builtin_amdgcn_make_buffer_rsrc(a.ws, 0, WS_BYTES, 0x00020000);
      const unsigned base = OFF_COMM + (j >> 1) * 2 * MB + 48 * 16384 + (j & 1) * 8192;
      st128(rs, (u32x4){1, 2, 3, 4}, lane * 16, base);
      VMWAIT(0);
      unsigned best = ~0u;
#pragma unroll
      for (int r = 0; r < 3; ++r) {
        unsigned long long c0, c1; unsigned v;
        asm volatile("s_memtime %0\n\ts_waitcnt lgkmcnt(0)" : "=s"(c0) :: "memory");
        v = ld32(rs, r * 256, base);
        asm volatile("s_waitcnt vmcnt(0)\n\ts_memtime %0\n\ts_waitcnt lgkmcnt(0)" : "=s"(c1) : "v"(v) : "memory");
        best = min(best, (unsigned)(c1 - c0));
      }
      if (lane == 0) { *(unsigned*)(a.ws + OFF_CTL + CTL_POL + 4 * j) = best; if (j == 0) *(unsigned*)(a.ws + OFF_CTL + CTL_POL + 16) = xcc_id(); }
    }
  } else if (b < 160) {
#pragma unroll
    for (int i = 0; i < 2; ++i) {
      const unsigned c = ((b - 32) * 2 + i) * 256 + tid;
      *(u32x4*)(a.ws + OFF_COMM + (c >> 14) * MB + (c & 16383) * 16) = (u32x4){SENT, SENT, SENT, SENT};
    }
  } else {
#pragma unroll
    for (int k = 0; k < 4; ++k) {
      const unsigned i = ((b - 160) * 4 + k) * 256 + tid;
      if (i < 12800) { if ((i >> 3) * 128 != CTL_POL) *(u32x4*)(a.ws + OFF_CTL + i * 16) = (u32x4){0, 0, 0, 0}; }
      else if (i < 12800 + 65 * 1024) *(u32x4*)(a.ws + OFF_REP + (i - 12800) * 16) = (u32x4){0, 0, 0, 0};
    }
  }
}

extern "C" void kernel_launch(void* const* d_in, const int* in_sizes, int n_in, void* d_out, int out_size, void* d_ws, size_t ws_size, hipStream_t stream) {
  Args a;
  a.x = (const unsigned char*)d_in[0]; a.mem0 = (const unsigned char*)d_in[1]; a.wq = (const unsigned char*)d_in[2]; a.wk = (const unsigned char*)d_in[3];
  a.wv = (const unsigned char*)d_in[4]; a.wo = (const unsigned char*)d_in[5]; a.w1 = (const unsigned char*)d_in[8]; a.w3 = (const unsigned char*)d_in[9];
  a.w2 = (const unsigned char*)d_in[10]; a.out = (unsigned char*)d_out; a.ws = (unsigned char*)d_ws;
  (void)hipFuncSetAttribute((const void*)k_main, hipFuncAttributeMaxDynamicSharedMemorySize, 163840);
  k_pro<<<240, 256, 0, stream>>>(a);
  k_main<<<256, 256, 163840, stream>>>(a);
}
```

```cpp
#include <hip/hip_runtime.h>
#include <stdint.h>

typedef __bf16 bf16x8 __attribute__((ext_vector_type(8)));
typedef __bf16 bf16x16 __attribute__((ext_vector_type(16)));
typedef __bf16 bf16x2 __attribute__((ext_vector_type(2)));
typedef float f32x2 __attribute__((ext_vector_type(2)));
typedef float f32x4 __attribute__((ext_vector_type(4)));
typedef unsigned u32x2 __attribute__((ext_vector_type(2)));
typedef unsigned u32x4 __attribute__((ext_vector_type(4)));
typedef unsigned u32x8 __attribute__((ext_vector_type(8)));
typedef short s16x4 __attribute__((ext_vector_type(4)));
#define LAS __attribute__((address_space(3)))
#define DEV __device__ __forceinline__

constexpr int NSEG = 64;
constexpr unsigned MB = 1u << 20;
constexpr unsigned OFF_COMM = 0;
constexpr unsigned OFF_CTL = 4 * MB;
constexpr unsigned OFF_XN = 5 * MB;
constexpr unsigned OFF_PT = 14 * MB;
constexpr unsigned OFF_HH = 20 * MB;
constexpr unsigned OFF_BLK = 32 * MB;
constexpr unsigned OFF_DBG = 28 * MB;
constexpr unsigned WS_BYTES = 256 * MB;
constexpr unsigned OOB = 0xF0000000u;
constexpr unsigned C_MEM1 = 16384, C_PM = 16384 + 2048, C_PX = 2 * 16384, C_Z = 4 * 16384;
constexpr unsigned CTL_POL = 65536;
constexpr unsigned CTL_RL = 65536 + 4096;
constexpr unsigned CTL_HP = 131072;
constexpr unsigned SENT = 0xFFFFFFFFu;
constexpr int SPIN_CAP = 1 << 20;
constexpr int SC1 = 16;

typedef __amdgpu_buffer_rsrc_t rsrc_t;
DEV u32x4 ld128(rsrc_t r, unsigned vo, unsigned so) { return __builtin_amdgcn_raw_buffer_load_b128(r, vo, so, SC1); }
DEV u32x2 ld64(rsrc_t r, unsigned vo, unsigned so) { return __builtin_amdgcn_raw_buffer_load_b64(r, vo, so, SC1); }
DEV unsigned ld32(rsrc_t r, unsigned vo, unsigned so) { return __builtin_amdgcn_raw_buffer_load_b32(r, vo, so, SC1); }
DEV void st128(rsrc_t r, u32x4 v, unsigned vo, unsigned so) { __builtin_amdgcn_raw_buffer_store_b128(v, r, vo, so, SC1); }
#define BST_AUX 16
#define BLD_AUX 16
#define KST_AUX 16
template <int AUX> DEV void st128a(rsrc_t r, u32x4 v, unsigned vo, unsigned so) { __builtin_amdgcn_raw_buffer_store_b128(v, r, vo, so, AUX); }
template <int AUX> DEV u32x4 ld128a(rsrc_t r, unsigned vo, unsigned so) { return __builtin_amdgcn_raw_buffer_load_b128(r, vo, so, AUX); }
DEV void st64(rsrc_t r, u32x2 v, unsigned vo, unsigned so) { __builtin_amdgcn_raw_buffer_store_b64(v, r, vo, so, SC1); }
DEV void st32(rsrc_t r, unsigned v, unsigned vo, unsigned so) { __builtin_amdgcn_raw_buffer_store_b32(v, r, vo, so, SC1); }
DEV unsigned pk(float a, float b) { bf16x2 v = {(__bf16)a, (__bf16)b}; return __builtin_bit_cast(unsigned, v); }
DEV unsigned pks(float a, float b) { return min(pk(a, b), 0xFFFFFFFEu); }
DEV unsigned mx4(u32x4 v) { return max(max(v.x, v.y), max(v.z, v.w)); }
DEV f32x4 mfma(u32x4 a, u32x4 b, f32x4 c) { return __builtin_amdgcn_mfma_f32_16x16x32_bf16(__builtin_bit_cast(bf16x8, a), __builtin_bit_cast(bf16x8, b), c, 0, 0, 0); }
DEV f32x4 smf(u32x4 a, u32x8 b, f32x4 c, int idx) { return __builtin_amdgcn_smfmac_f32_16x16x64_bf16(__builtin_bit_cast(bf16x8, a), __builtin_bit_cast(bf16x16, b), c, idx, 0, 0); }
DEV u32x4 lo4(u32x8 v) { return __builtin_shufflevector(v, v, 0, 1, 2, 3); }
DEV u32x4 hi4(u32x8 v) { return __builtin_shufflevector(v, v, 4, 5, 6, 7); }
DEV u32x8 cat8(u32x4 a, u32x4 b) { return __builtin_shufflevector(a, b, 0, 1, 2, 3, 4, 5, 6, 7); }
DEV u32x4 cat4(u32x2 a, u32x2 b) { return __builtin_shufflevector(a, b, 0, 1, 2, 3); }
DEV u32x2 trread(unsigned lds_addr) { return __builtin_bit_cast(u32x2, __builtin_amdgcn_ds_read_tr16_b64_v4i16((LAS s16x4*)(uintptr_t)lds_addr)); }
DEV u32x4 ldsr128(unsigned a) { return *(LAS u32x4*)(uintptr_t)a; }
DEV void ldsw128(unsigned a, u32x4 v) { *(LAS u32x4*)(uintptr_t)a = v; }
DEV void ldsw64(unsigned a, u32x2 v) { *(LAS u32x2*)(uintptr_t)a = v; }
#define BAR() asm volatile("s_waitcnt lgkmcnt(0)\n\ts_barrier" ::: "memory")
#define VMWAIT(n) asm volatile("s_waitcnt vmcnt(" #n ")" ::: "memory")
#define LGWAIT() asm volatile("s_waitcnt lgkmcnt(0)" ::: "memory")
#define CFENCE() asm volatile("" ::: "memory")
template <int N> DEV void vmwait() { asm volatile("s_waitcnt vmcnt(%0)" :: "n"(N) : "memory"); }
DEV void glds(const void* sbase, unsigned voff, unsigned ldst) {
  unsigned keep;
  asm volatile("s_mov_b32 %0, m0\n\ts_mov_b32 m0, %3\n\ts_nop 0\n\tglobal_load_lds_dwordx4 %1, %2\n\ts_mov_b32 m0, %0" : "=&s"(keep) : "v"(voff), "s"(sbase), "s"(ldst) : "memory");
}
constexpr unsigned CTL_XP = 196608;
DEV void glds_sc1(const void* sbase, unsigned voff, unsigned ldst) {
  unsigned keep;
  asm volatile("s_mov_b32 %0, m0\n\ts_mov_b32 m0, %3\n\ts_nop 0\n\tglobal_load_lds_dwordx4 %1, %2 sc1\n\ts_mov_b32 m0, %0" : "=&s"(keep) : "v"(voff), "s"(sbase), "s"(ldst) : "memory");
}
DEV void set_m0(unsigned v) { asm volatile("s_mov_b32 m0, %0" :: "s"(v) : "memory"); }
template <int OFF> DEV void glds_off(const void* sbase, unsigned voff) { asm volatile("global_load_lds_dwordx4 %0, %1 offset:%2 sc1" :: "v"(voff), "s"(sbase), "n"(OFF) : "memory"); }
template <int V> struct IC { static constexpr int value = V; constexpr operator int() const { return V; } };
template <int N, int I = 0, class F> DEV void static_for(F&& f) { if constexpr (I < N) { f(IC<I>{}); static_for<N, I + 1>(f); } }
DEV float fold32(float a, float b) {
  auto r = __builtin_amdgcn_permlane32_swap(__builtin_bit_cast(unsigned, a), __builtin_bit_cast(unsigned, b), false, false);
  return __builtin_bit_cast(float, (unsigned)r[0]) + __builtin_bit_cast(float, (unsigned)r[1]);
}
DEV float xmax16(float v) { auto r = __builtin_amdgcn_permlane16_swap(__builtin_bit_cast(unsigned, v), __builtin_bit_cast(unsigned, v), false, false); return fmaxf(__builtin_bit_cast(float, (unsigned)r[0]), __builtin_bit_cast(float, (unsigned)r[1])); }
DEV float xmax32(float v) { auto r = __builtin_amdgcn_permlane32_swap(__builtin_bit_cast(unsigned, v), __builtin_bit_cast(unsigned, v), false, false); return fmaxf(__builtin_bit_cast(float, (unsigned)r[0]), __builtin_bit_cast(float, (unsigned)r[1])); }
DEV float xadd16(float v) { auto r = __builtin_amdgcn_permlane16_swap(__builtin_bit_cast(unsigned, v), __builtin_bit_cast(unsigned, v), false, false); return __builtin_bit_cast(float, (unsigned)r[0]) + __builtin_bit_cast(float, (unsigned)r[1]); }
DEV float xadd32(float v) { auto r = __builtin_amdgcn_permlane32_swap(__builtin_bit_cast(unsigned, v), __builtin_bit_cast(unsigned, v), false, false); return __builtin_bit_cast(float, (unsigned)r[0]) + __builtin_bit_cast(float, (unsigned)r[1]); }
DEV float bf2f(unsigned short u) { return __builtin_bit_cast(float, (unsigned)u << 16); }
DEV float rbf(float x) { return (float)(__bf16)x; }

DEV void smf_a(f32x4& c, u32x4 a, const u32x8& w, int idx) { asm volatile("v_smfmac_f32_16x16x64_bf16 %0, %1, %2, %3" : "+v"(c) : "v"(a), "a"(w), "v"(idx)); }
DEV void smf_v(f32x4& c, u32x4 a, const u32x8& w, int idx) { asm volatile("v_smfmac_f32_16x16x64_bf16 %0, %1, %2, %3" : "+v"(c) : "v"(a), "v"(w), "v"(idx)); }
template <int I> DEV void smf_w(f32x4& c, u32x4 a, const u32x8 (&w)[40], int idx) { if constexpr (I < 32) smf_a(c, a, w[I], idx); else smf_v(c, a, w[I], idx); }
DEV void mf_va(f32x4& c, u32x4 a, u32x4 b) { asm volatile("v_mfma_f32_16x16x32_bf16 %0, %1, %2, %0" : "+v"(c) : "v"(a), "a"(b)); }
DEV void mf_vv(f32x4& c, u32x4 a, u32x4 b) { asm volatile("v_mfma_f32_16x16x32_bf16 %0, %1, %2, %0" : "+v"(c) : "v"(a), "v"(b)); }
template <bool A> DEV void pin_w(u32x8& x, u32x8& y) {
  if constexpr (A) asm volatile("s_waitcnt lgkmcnt(0)" : "+a"(x), "+a"(y) :: "memory"); else asm volatile("s_waitcnt lgkmcnt(0)" : "+v"(x), "+v"(y) :: "memory");
}
template <bool A> DEV void pin_w4(u32x4& x, u32x4& y, u32x4& z, u32x4& u) {
  if constexpr (A) asm volatile("s_waitcnt lgkmcnt(0)" : "+a"(x), "+a"(y), "+a"(z), "+a"(u) :: "memory"); else asm volatile("s_waitcnt lgkmcnt(0)" : "+v"(x), "+v"(y), "+v"(z), "+v"(u) :: "memory");
}
DEV void pin2(f32x4& x, f32x4& y) { asm volatile("" : "+v"(x), "+v"(y)); }
DEV void pin2n(f32x4& x, f32x4& y) { asm volatile("s_nop 3" : "+v"(x), "+v"(y)); }
DEV unsigned pin1(unsigned x) { asm volatile("" :: "v"(x)); return x; }
#define NOP(n) asm volatile("s_nop " #n)
#define PIN(x) asm volatile("" : "+v"(x))
#define PINS(x) asm volatile("" : "+s"(x))
DEV unsigned xcc_id() { return (unsigned)__builtin_amdgcn_s_getreg(20 | (3 << 11)) & 0xfu; }
DEV unsigned rtc() { unsigned long long t; asm volatile("s_memrealtime %0\n\ts_waitcnt lgkmcnt(0)" : "=s"(t) :: "memory"); return (unsigned)t; }
constexpr unsigned CTL_CUR = 65536 + 12288 + 1024;
constexpr unsigned OFF_REP = 8 * MB;
constexpr int MAXWAIT = 400 * 8;
#define D1_INIT 840
#define D2_INIT 510
struct Sched { int j0, jbad, rx; unsigned base; int T, D1; unsigned c0, v0; int R; unsigned ct, tt; };
DEV unsigned now8() { return rtc() << 3; }
DEV unsigned cycles() { unsigned long long t; asm volatile("s_memtime %0\n\ts_waitcnt lgkmcnt(0)" : "=s"(t) :: "memory"); return (unsigned)t; }
DEV void clk_init(Sched& c) { c.j0 = 0; c.jbad = 0; c.rx = 0; c.base = 0; c.T = D1_INIT + D2_INIT; c.D1 = D1_INIT; c.v0 = rtc(); c.c0 = cycles(); c.R = 24 * 256; c.ct = c.c0; c.tt = 0; }
DEV void clk_update(Sched& c) {
  const unsigned v = rtc() - c.v0, y = cycles() - c.c0;
  if (v > 300) c.R = __builtin_amdgcn_readfirstlane((int)((float)y * 256.f * __builtin_amdgcn_rcpf((float)v)));
}
#define RELAX 120
#define NRELAX 2
DEV unsigned tau_of(const Sched& c, int j) { return c.base + (j >> 1) * c.T + ((j & 1) ? c.D1 : 0) + RELAX * min(max(j - c.rx, 0), NRELAX); }
DEV void adopt_cur(Sched& c, u32x4 v, int j) {
  const int j0 = __builtin_amdgcn_readfirstlane(v.x);
  if (j0 > c.j0 && j0 <= j) { c.j0 = j0; c.base = __builtin_amdgcn_readfirstlane(v.y); c.T = __builtin_amdgcn_readfirstlane(v.z); const int w = __builtin_amdgcn_readfirstlane(v.w); c.D1 = w & 0xffff; c.rx = w >> 16; }
}
DEV bool wait_until(unsigned tau) {
  int d = (int)(tau - now8());
  if (d <= 0 || d > MAXWAIT) return false;
  while ((int)(tau - now8()) > 0) {}
  return true;
}
#define NOP16 "s_nop 0\n\ts_nop 0\n\ts_nop 0\n\ts_nop 0\n\ts_nop 0\n\ts_nop 0\n\ts_nop 0\n\ts_nop 0\n\ts_nop 0\n\ts_nop 0\n\ts_nop 0\n\ts_nop 0\n\ts_nop 0\n\ts_nop 0\n\ts_nop 0\n\ts_nop 0\n\t"
#define FK 96
DEV unsigned est_cycle(unsigned tau, int R, unsigned c0, int best) {
  unsigned long long v, c; unsigned t; int rem;
  asm volatile(
    "1:\n\t"
    "s_memrealtime %0\n\ts_memtime %1\n\ts_waitcnt lgkmcnt(0)\n\t"
    "s_lshl_b32 %2, s94, 3\n\t"
    "s_sub_i32 %2, %5, %2\n\t"
    "s_mul_i32 %2, %2, %6\n\t"
    "s_ashr_i32 %2, %2, 11\n\t"
    "s_sub_i32 %3, s96, %7\n\t"
    "s_add_i32 %2, %2, %3\n\t"
    "s_min_i32 %4, %4, %2\n\t"
    "s_sub_i32 %2, %2, 100\n\t"
    "s_max_i32 %4, %4, %2\n\t"
    "s_sub_i32 %3, %4, %3\n\t"
    "s_cmp_gt_i32 %3, 520\n\t"
    "s_cbranch_scc1 1b"
    : "=&{s[94:95]}"(v), "=&{s[96:97]}"(c), "=&s"(t), "=&s"(rem), "+s"(best)
    : "s"(tau), "s"(R), "s"(c0) : "memory", "scc");
  return c0 + best;
}
DEV void release(unsigned cs) {
  unsigned long long c, pc; int rem;
  asm volatile(
    "1:\n\t"
    "s_memtime %0\n\ts_waitcnt lgkmcnt(0)\n\t"
    "s_sub_i32 %2, %3, s96\n\t"
    "s_cmp_gt_i32 %2, %4\n\t"
    "s_cbranch_scc1 1b\n\t"
    "s_sub_i32 %2, %2, %5\n\t"
    "s_max_i32 %2, %2, 0\n\t"
    "s_and_b32 %2, %2, -4\n\t"
    "s_sub_i32 %2, 140, %2\n\t"
    "s_getpc_b64 %1\n\t"
    "s_add_u32 s98, s98, %2\n\t"
    "s_addc_u32 s99, s99, 0\n\t"
    "s_setpc_b64 %1\n\t"
    NOP16 NOP16 "s_barrier"
    : "=&{s[96:97]}"(c), "=&{s[98:99]}"(pc), "=&s"(rem)
    : "s"(cs), "n"(FK + 124), "n"(FK) : "memory", "scc");
}
#define DRIFT 10
#define CSLEEP 3
#define LATE_THR 120
constexpr unsigned L_TM = 61440;
DEV void mark_overrun(Sched& c, int j, unsigned t) { if ((int)(t - tau_of(c, j + 1)) > LATE_THR + 40) c.jbad = c.j0; }
template <class F>
DEV unsigned trigger(rsrc_t rs, Sched& c, int j, unsigned hold, F&& poll) {
  unsigned cs, timed = 0;
  for (;;) {
    const unsigned tau = tau_of(c, j), now = now8();
    const int d = (int)(tau - now);
    const bool ok = c.j0 > c.jbad && d > -30 * 8 && d <= MAXWAIT;
    if (ok && d <= 0) { cs = cycles(); break; }
    if (ok && d <= 50 * 8) {
      const unsigned dt = tau - c.tt;
      const int prior = dt < 4096 ? (int)((dt * (unsigned)c.R) >> 11) + DRIFT : 0x7fffffff;
      cs = est_cycle(__builtin_amdgcn_readfirstlane(tau), __builtin_amdgcn_readfirstlane(c.R), __builtin_amdgcn_readfirstlane(c.ct), __builtin_amdgcn_readfirstlane(prior));
      c.ct = cs; c.tt = tau; timed = 1;
      break;
    }
    if (!ok && (unsigned)(hold - now) > (unsigned)MAXWAIT) { poll(); cs = cycles(); break; }
    if (d > 100 * 8 || !ok) { adopt_cur(c, ld128(rs, 0, OFF_CTL + CTL_CUR), j); CFENCE(); __builtin_amdgcn_s_sleep(CSLEEP); }
  }
  ldsw64(L_TM + (j & 2) * 8, (u32x2){cs, timed | (unsigned)c.R << 2});
  return __builtin_amdgcn_readfirstlane(cs);
}
#define STAMP(k)
#define STV(k) stdummy
#define STAMP_DECL unsigned stdummy
#define STAMP_OUT(e, s)

#define BMODE 0
#define KQMODE 0
constexpr unsigned BSTR = BMODE == 2 ? 8192 : 16384;
constexpr int ZBLK0 = BMODE == 0 ? 0 : BMODE == 1 ? 10240 : 5376;
DEV unsigned blk_addr(int blk, unsigned pol, unsigned far) { return OFF_BLK + blk * 16384 + (((pol >> ((blk >> 7) & 1)) ^ far) & 1) * 8192; }
DEV unsigned z_base(int s, unsigned pol) { const int pg = s / 3; return blk_addr(ZBLK0 + pg * 128 + (s - 3 * pg) * 40, pol, 0); }
DEV unsigned kq_addr(int isq, int seg, int h, unsigned pol) {
  if (KQMODE == 2) return OFF_BLK + (ZBLK0 + 3072) * 16384 + (isq * 2048 + seg * 32 + h) * 8192;
  return BMODE == 1 ? blk_addr(isq * 2048 + seg * 32 + h, pol, 1) : blk_addr(ZBLK0 + 3072 + isq * 2048 + seg * 32 + h, pol, 0);
}
DEV unsigned b_addr(int seg, int cb, unsigned pol) { return BMODE == 2 ? OFF_BLK + (seg * 40 + cb) * 32768 : blk_addr((seg * 40 + cb) * 4, pol, 1 - BMODE); }

#define EV(slot)
struct Args {
  const unsigned char* x; const unsigned char* mem0; const unsigned char* wq; const unsigned char* wk; const unsigned char* wv; const unsigned char* wo;
  const unsigned char* w1; const unsigned char* w3; const unsigned char* w2; unsigned char* out; unsigned char* ws;
};

enum { K_P, K_V, K_QK, K_VB, K_F1 };
constexpr unsigned L_XB0 = 120 * 1024, L_XB1 = 80 * 1024;
template <int KIND, class WT, class LATE>
DEV void load_weights(WT& w, const unsigned char* baseA, const unsigned char* baseB, int wave, int lane, int cmask, LATE&& late) {
  constexpr int NG = 20;
  constexpr bool WO = KIND == K_V || KIND == K_VB;
  constexpr unsigned SA = KIND == K_F1 ? 3456 : 8192, SB = WO ? 1280 : SA;
  constexpr int MA = (KIND == K_P || KIND == K_V) ? 2 : 0, MBB = KIND == K_P ? 2 : KIND == K_VB ? 1 : 0;
  const int gB = lane >> 4, q = (lane >> 2) & 3, pp = lane & 3;
  unsigned vo[2][4], rd[2][2];
#pragma unroll
  for (int m = 0; m < 2; ++m) {
    const int mode = m ? MBB : MA;
#pragma unroll
    for (int p = 0; p < 4; ++p) {
      const int key = mode == 0 ? (gB | (p >> 1) << 2) : mode == 1 ? (gB | (p & 1) << 2) : ((gB >> 1) | p << 1);
      vo[m][p] = (wave * 16 + p * 4 + gB) * (m ? SB : SA) + (((lane & 15) ^ (key << 1)) & cmask) * 16;
    }
    const int key = mode == 2 ? ((gB & 1) | (q >> 1) << 1 | (q & 1) << 2) : (q | (gB & 1) << 2);
    const int row = mode == 0 ? 8 * gB + q : mode == 1 ? 4 * gB + q : 16 * (gB >> 1) + 4 * (q >> 1) + 2 * (gB & 1) + 8 * (q & 1);
#pragma unroll
    for (int nt = 0; nt < 2; ++nt) rd[m][nt] = row * 256 + ((4 * wave + 2 * nt + (pp >> 1)) ^ (key << 1)) * 16 + (pp & 1) * 8;
  }
  auto issue = [&](int g) {
    const unsigned char* src = g < 10 ? baseA + (size_t)g * 64 * SA : WO ? baseB + (size_t)((g - 10) & 1) * 64 * SB + ((g - 10) >> 1) * 256 : baseB + (size_t)(g - 10) * 64 * SB;
#pragma unroll
    for (int p = 0; p < 4; ++p) glds(src, vo[g >= 10][p], (g % 7) * 16384 + wave * 4096 + p * 1024);
  };
#pragma unroll
  for (int g = 0; g < 7; ++g) issue(g);
  static_for<NG>([&](auto G) {
    constexpr int g = G, mode = g < 10 ? MA : MBB;
    if constexpr (g == 0) vmwait<24>(); else if constexpr (g < NG - 5) vmwait<20>(); else if constexpr (g == NG - 5) late();
    if constexpr (g <= NG - 5) BAR();
    if constexpr (g >= 1 && g + 6 < NG) issue(g + 6);
    constexpr unsigned phs = mode == 2 ? 256 : mode == 1 ? 16 * 256 : 4 * 256, sl = (g % 7) * 16384;
#pragma unroll
    for (int nt = 0; nt < 2; ++nt) {
      const unsigned rb = rd[g >= 10][nt] + sl;
      u32x2 a = trread(rb), b = trread(rb + phs), c = trread(rb + 8192), d = trread(rb + 8192 + phs);
      if constexpr (KIND == K_P || KIND == K_V) w[g * 2 + nt] = cat8(cat4(a, b), cat4(c, d));
      else { w[g * 4 + nt * 2] = cat4(a, b); w[g * 4 + nt * 2 + 1] = cat4(c, d); }
    }
    if constexpr (KIND == K_P || KIND == K_V) pin_w<g < 16>(w[g * 2], w[g * 2 + 1]);
    else pin_w4<g < 16>(w[g * 4], w[g * 4 + 1], w[g * 4 + 2], w[g * 4 + 3]);
  });
}
DEV void load_w2(u32x4 (&w)[56], const unsigned char* base, int wave, int lane) {
  const int gB = lane >> 4, q = (lane >> 2) & 3, pp = lane & 3;
  const unsigned ring = wave * 32768;
  const unsigned vo = (lane >> 3) * 1280 + ((lane & 7) ^ (((lane >> 4) & 3) << 1)) * 16;
  unsigned rd[4];
#pragma unroll
  for (int c = 0; c < 4; ++c) rd[c] = ring + (4 * gB + q) * 128 + ((2 * c + (pp >> 1)) ^ ((((gB & 1) << 1) | (q >> 1)) << 1)) * 16 + (pp & 1) * 8;
  auto issue = [&](int g) {
    const unsigned char* src = base + (size_t)min(wave * 7 + g, 26) * 64 * 1280;
#pragma unroll
    for (int p = 0; p < 8; ++p) glds(src + p * 8 * 1280, vo, ring + (g & 3) * 8192 + p * 1024);
  };
#pragma unroll
  for (int g = 0; g < 4; ++g) issue(g);
  static_for<7>([&](auto G) {
    constexpr int g = G;
    vmwait<8 * (6 - g < 3 ? 6 - g : 3)>();
#pragma unroll
    for (int c = 0; c < 4; ++c) {
      const unsigned rb = rd[c] + (g & 3) * 8192;
      w[g * 8 + c * 2] = cat4(trread(rb), trread(rb + 2048)); w[g * 8 + c * 2 + 1] = cat4(trread(rb + 4096), trread(rb + 6144));
    }
    pin_w4<true>(w[g * 8], w[g * 8 + 1], w[g * 8 + 2], w[g * 8 + 3]); pin_w4<true>(w[g * 8 + 4], w[g * 8 + 5], w[g * 8 + 6], w[g * 8 + 7]);
    if constexpr (g + 4 < 7) issue(g + 4);
  });
}

#define QKN 8
DEV int first_open(int type, int par, int c) { return (type == 1 || c < QKN) ? 2 * c + par : par ? NSEG : c + QKN; }
DEV unsigned prog_off(int type, int h, int par, int wave) { return OFF_CTL + (((type * 32 + h) * 2 + par) * 4 + wave) * 128; }
DEV unsigned hprog_off(int rep, int slice) { return OFF_CTL + CTL_HP + (rep * 56 + slice) * 128; }
DEV float silu(float x) { return x * __builtin_amdgcn_rcpf(1.f + __builtin_amdgcn_exp2f(-1.4426950408889634f * x)); }
DEV u32x2 pk4(f32x4 v) { return (u32x2){pk(v[0], v[1]), pk(v[2], v[3])}; }

template <int I, int HI, bool WA, bool FIRST, int N>
DEV void mf_w(f32x4& c, const u32x4 (&w)[N], u32x4 x) {
  const u32x4 f = w[2 * I + HI];
  if constexpr (FIRST) {
    if constexpr (I < 32) { if constexpr (WA) asm volatile("v_mfma_f32_16x16x32_bf16 %0, %1, %2, 0" : "=&v"(c) : "a"(f), "v"(x)); else asm volatile("v_mfma_f32_16x16x32_bf16 %0, %2, %1, 0" : "=&v"(c) : "a"(f), "v"(x)); }
    else { if constexpr (WA) asm volatile("v_mfma_f32_16x16x32_bf16 %0, %1, %2, 0" : "=&v"(c) : "v"(f), "v"(x)); else asm volatile("v_mfma_f32_16x16x32_bf16 %0, %2, %1, 0" : "=&v"(c) : "v"(f), "v"(x)); }
  } else {
    if constexpr (I < 32) { if constexpr (WA) asm volatile("v_mfma_f32_16x16x32_bf16 %0, %1, %2, %0" : "+v"(c) : "a"(f), "v"(x)); else asm volatile("v_mfma_f32_16x16x32_bf16 %0, %2, %1, %0" : "+v"(c) : "a"(f), "v"(x)); }
    else { if constexpr (WA) asm volatile("v_mfma_f32_16x16x32_bf16 %0, %1, %2, %0" : "+v"(c) : "v"(f), "v"(x)); else asm volatile("v_mfma_f32_16x16x32_bf16 %0, %2, %1, %0" : "+v"(c) : "v"(f), "v"(x)); }
  }
}

DEV void issue_x(const unsigned char* ws, int seg, unsigned buf, int wave, int lane) {
#pragma unroll
  for (int i = 0; i < 10; ++i) glds_sc1(ws + OFF_XN + (size_t)seg * 40960 + (wave * 10 + i) * 1024, lane * 16, buf + (wave * 10 + i) * 1024);
}
template <int NT, class MID>
DEV void xproj(f32x4 (&acc)[NT][2], const u32x4 (&w)[80], unsigned xa, MID&& mid) {
  u32x4 xr[3][2];
#pragma unroll
  for (int k = 0; k < 2; ++k) { xr[k][0] = ldsr128(xa + k * 2048); xr[k][1] = ldsr128(xa + k * 2048 + 1024); }
  static_for<20>([&](auto K) {
    constexpr int ks = K;
    static_for<2 * NT>([&](auto J) {
      constexpr int j = J, t = j >> 1, wi = ((t >> 1) * 10 + (ks >> 1)) * 2 + (t & 1);
      mf_w<wi, ks & 1, true, ks == 0>(acc[t][j & 1], w, xr[ks % 3][j & 1]);
      if constexpr (ks + 2 < 20 && j == 1) { CFENCE(); xr[(ks + 2) % 3][0] = ldsr128(xa + (ks + 2) * 2048); CFENCE(); }
      if constexpr (ks + 2 < 20 && j == NT / 2 + 1) { CFENCE(); xr[(ks + 2) % 3][1] = ldsr128(xa + (ks + 2) * 2048 + 1024); CFENCE(); }
      if constexpr (j == (NT == 4 ? 5 : 0)) mid(K);
    });
  });
}
template <int NT> DEV void acc_wait(f32x4 (&c)[NT][2]) {
  if constexpr (NT == 4) asm volatile("s_nop 7" : "+v"(c[0][0]), "+v"(c[0][1]), "+v"(c[1][0]), "+v"(c[1][1]), "+v"(c[2][0]), "+v"(c[2][1]), "+v"(c[3][0]), "+v"(c[3][1]));
  else asm volatile("s_nop 7" : "+v"(c[0][0]), "+v"(c[0][1]), "+v"(c[1][0]), "+v"(c[1][1]));
}

template <int KIND>
DEV void role_x(const Args& a, rsrc_t rs, int id, int seg0, int sw, int lim, unsigned po, int wave, int lane, unsigned pol) {
  constexpr int NST = KIND == K_QK ? 4 : KIND == K_F1 ? 2 : 10;
  u32x4 w[80];
  auto late = [&]() {
    if constexpr (KIND != K_F1) {
      int budget = SPIN_CAP;
      for (;;) { const unsigned v = ld32(rs, lane * 128, OFF_CTL + CTL_XP); CFENCE(); if (__all(v == 1) || --budget <= 0) break; }
    }
    VMWAIT(0);
    issue_x(a.ws, seg0, L_XB0, wave, lane);
  };
  if constexpr (KIND == K_QK) load_weights<K_QK>(w, a.wq + id * 256, a.wk + id * 256, wave, lane, 15, late);
  else if constexpr (KIND == K_F1) load_weights<K_F1>(w, a.w1 + (id >> 2) * 256, a.w3 + (id >> 2) * 256, wave, lane, id >= 52 ? 7 : 15, late);
  else load_weights<K_VB>(w, a.wv + id * 256, a.wo + (size_t)id * 128 * 1280, wave, lane, 15, late);
  VMWAIT(0);
  EV(KIND == K_F1 ? 4 : 1);
  const unsigned l16 = lane * 16, fvo = lane < 32 ? po + lane * 4 : OOB;
  const unsigned svo = (KIND == K_F1 && id > 53) ? OOB : l16, bvo = l16 + (id >> 3) * BSTR + (id & 7) * 1024;
  int it = 0;
  for (int seg = seg0, nxt; seg < lim; seg = nxt, ++it) {
    nxt = seg + (seg < sw ? 2 : 1);
    const unsigned buf = (it & 1) ? L_XB1 : L_XB0;
    vmwait<NST>();
    BAR();
    const unsigned char* xsrc = a.ws + OFF_XN + (size_t)(nxt < lim ? nxt : seg) * 40960 + wave * 10240;
    const unsigned xdst = L_XB0 + L_XB1 - buf + wave * 10240;
    const unsigned char *xs0 = xsrc, *xs1 = xsrc + 4096, *xs2 = xsrc + 8192;
    PINS(xs0); PINS(xs1); PINS(xs2);
    auto mid = [&](auto K) {
      if constexpr (K % 5 == 0 && K <= 10) set_m0(xdst + (K / 5) * 4096);
      if constexpr (K % 5 != 0 && K <= 12) glds_off<(K % 5 - 1) * 1024>(K < 5 ? xs0 : K < 10 ? xs1 : xs2, l16);
      if constexpr (K == 16) { if (it > 1) { vmwait<10>(); st32(rs, it, fvo, 0); } }
    };
    if constexpr (KIND != K_VB) {
      f32x4 acc[4][2];
      xproj<4>(acc, w, buf + l16, mid);
      acc_wait(acc);
      if constexpr (KIND == K_QK) {
#pragma unroll
        for (int m = 0; m < 2; ++m)
#pragma unroll
          for (int tt = 0; tt < 2; ++tt) st128a<KST_AUX>(rs, cat4(pk4(acc[2 * m][tt]), pk4(acc[2 * m + 1][tt])), l16, kq_addr(1 - m, seg, id, pol) + (tt * 4 + wave) * 1024);
      } else {
#pragma unroll
        for (int tt = 0; tt < 2; ++tt) {
          f32x4 hv[2];
#pragma unroll
          for (int t = 0; t < 2; ++t)
#pragma unroll
            for (int r = 0; r < 4; ++r) hv[t][r] = rbf(silu(rbf(acc[t][tt][r]))) * rbf(acc[2 + t][tt][r]);
          st128(rs, cat4(pk4(hv[0]), pk4(hv[1])), svo, OFF_HH + (seg * 54 + id) * 2048 + tt * 1024);
        }
      }
    } else {
      f32x4 acc[2][2];
      xproj<2>(acc, w, buf + l16, mid);
      acc_wait(acc);
#pragma unroll
      for (int tt = 0; tt < 2; ++tt) ldsw128((wave * 2 + tt) * 1024 + l16, cat4(pk4(acc[0][tt]), pk4(acc[1][tt])));
      BAR();
      u32x4 vf[4][2];
#pragma unroll
      for (int ks = 0; ks < 4; ++ks) { vf[ks][0] = ldsr128((ks * 2) * 1024 + l16); vf[ks][1] = ldsr128((ks * 2 + 1) * 1024 + l16); }
      f32x4 c[3][2];
      static_for<12>([&](auto CT) {
        constexpr int ct = CT;
        u32x4 o;
        static_for<4>([&](auto KS) {
          constexpr int ks = KS, wi = 20 + ((ct >> 1) * 2 + (ks >> 1)) * 2 + (ct & 1);
          if constexpr (ct < 10) { mf_w<wi, ks & 1, false, ks == 0>(c[ct % 3][0], w, vf[ks][0]); mf_w<wi, ks & 1, false, ks == 0>(c[ct % 3][1], w, vf[ks][1]); }
          if constexpr (ct >= 2) {
            constexpr int e = (ct - 2) % 3;
            if constexpr (ks == 0) { if constexpr (ct == 11) pin2n(c[e][0], c[e][1]); else pin2(c[e][0], c[e][1]); }
            o[ks] = pk(c[e][ks >> 1][2 * (ks & 1)], c[e][ks >> 1][2 * (ks & 1) + 1]); pin1(o[ks]);
          }
        });
        if constexpr (ct >= 2) { CFENCE(); st128a<BST_AUX>(rs, o, bvo, b_addr(seg, ((ct - 2) >> 1) * 8 + wave * 2 + (ct & 1), pol)); CFENCE(); }
      });
    }
    if (it == 0) { vmwait<0>(); st32(rs, 1, fvo, 0); EV(KIND == K_F1 ? 5 : 2); }
  }
  VMWAIT(0);
  st32(rs, it, fvo, 0);
  EV(KIND == K_F1 ? 6 : 3);
  BAR();
}

constexpr int REPL = 4;
DEV unsigned slot_base(int s, unsigned pol) { return OFF_COMM + (s & 3) * MB + ((pol >> ((s >> 1) & 1)) & 1) * 8192; }
DEV int wmax(int v) {
  v = max(v, __builtin_amdgcn_update_dpp(v, v, 0xB1, 0xf, 0xf, false));
  v = max(v, __builtin_amdgcn_update_dpp(v, v, 0x4E, 0xf, 0xf, false));
  v = max(v, __builtin_amdgcn_update_dpp(v, v, 0x124, 0xf, 0xf, false));
  v = max(v, __builtin_amdgcn_update_dpp(v, v, 0x128, 0xf, 0xf, false));
  return max(max(__builtin_amdgcn_readlane(v, 0), __builtin_amdgcn_readlane(v, 16)), max(__builtin_amdgcn_readlane(v, 32), __builtin_amdgcn_readlane(v, 48)));
}
DEV int wmin(int v) { return -wmax(-v); }
DEV void report(rsrc_t rs, int e, int s, unsigned flags, int lane) {
  unsigned long long v, c;
  asm volatile("s_memrealtime %0\n\ts_memtime %1\n\ts_waitcnt lgkmcnt(0)" : "=s"(v), "=s"(c) :: "memory");
  const u32x2 tm = *(volatile LAS u32x2*)(uintptr_t)(L_TM + (s & 1) * 16);
  st128(rs, (u32x4){(unsigned)s + 1, (unsigned)v << 3, (((unsigned)c - tm.x) << 2) | flags | (tm.y & 1), tm.y >> 2}, lane < 8 ? lane * 16 : OOB, OFF_REP + (s * 128 + e) * 128);
}
DEV void sleep_until_turn(rsrc_t rs, Sched& c, int j, int off) {
  for (;;) {
    adopt_cur(c, ld128(rs, 0, OFF_CTL + CTL_CUR), j + 1); CFENCE();
    const int d = (int)(tau_of(c, j) + off - now8());
    if (c.j0 == 0 || d > 200 * MAXWAIT) return;
    if (d < 150 * 8) { wait_until(tau_of(c, j) + off); return; }
    if (d > 500 * 8) __builtin_amdgcn_s_sleep(127); else __builtin_amdgcn_s_sleep(40);
  }
}
DEV int probe_seg(int s, int lane) { return min(s + (3 << (lane & 3)), NSEG - 1); }
DEV unsigned probe_ld(rsrc_t rs, int s, int lane) { return ld32(rs, lane < 4 ? probe_seg(s, lane) * 128 : OOB, OFF_CTL + CTL_RL); }
DEV int probe_rdy(unsigned v, int s, int lane) { return wmax(v != 0 ? probe_seg(s, lane) + 1 : 0); }
DEV void role_agg(rsrc_t rs, int lane) {
  int done = 0, budget = SPIN_CAP;
  while (done < NSEG && --budget > 0) {
    int m = NSEG;
#pragma unroll
    for (int i = 0; i < 8; ++i) m = min(m, first_open(i >> 2, (lane >> 2) & 1, ld32(rs, (i * 64 + lane) * 128, OFF_CTL)));
    CFENCE();
    m = min(wmin(m), NSEG);
    for (; done < m; done = min(done + 8, m)) st128(rs, (u32x4){1, 1, 1, 1}, done + (lane >> 3) < m ? done * 128 + lane * 16 : OOB, OFF_CTL + CTL_RL);
    __builtin_amdgcn_s_sleep(16);
  }
}
DEV unsigned rep_off(int j, int lane) { return ((j >> 1) * 128 + ((j & 1) ? 64 + min(lane, 39) : lane)) * 128; }
#define PTSLEEP 2
#define PSLEEP 2
#define HOLDP 720
#define HOLDC 1080
#define NOPM_(n) asm volatile("s_nop " #n ::: "memory")
#define NOPM(n) NOPM_(n)
#define VSLACK 48
#define PACE 11
#define NOPV(n, x) asm volatile("s_nop " #n : "+v"(x))
template <int N> DEV void zero_acc(f32x4 (&c)[N]) {
#pragma unroll
  for (int t = 0; t < N; ++t) { c[t] = (f32x4){0, 0, 0, 0}; PIN(c[t]); }
}
DEV unsigned memoff(int j) { return j < 8 ? j * 1024 : C_MEM1 + (j - 8) * 1024; }
DEV bool has_nan(f32x4 v) { return __any(__builtin_isunordered(v[0], v[1]) || __builtin_isunordered(v[2], v[3])); }
template <int N> DEV bool has_sent(const u32x4 (&a)[N]) {
  unsigned m = 0;
#pragma unroll
  for (int k = 0; k < N; ++k) m = max(m, mx4(a[k]));
  return __any(m == SENT);
}
DEV int mrow(int i, int half) { return 4 * (i >> 1) + 2 * half + (i & 1); }
DEV unsigned foldpk(f32x4 v) { return pk(v[0] + v[2], v[1] + v[3]); }
DEV void ldsw32(unsigned a, unsigned v) { *(LAS unsigned*)(uintptr_t)a = v; }
DEV void mem_wait(rsrc_t rs, unsigned sb, int lane, int& budget) {
  for (;;) {
    const unsigned u = ld32(rs, lane * 128, sb), v = ld32(rs, C_MEM1 + (lane & 15) * 128, sb);
    CFENCE();
    if (__all(u != SENT && v != SENT) || --budget <= 0) break;
    __builtin_amdgcn_s_sleep(PSLEEP);
  }
}
template <int NT, int NK>
DEV void project(rsrc_t rs, const unsigned (&vo)[10], unsigned sb, const u32x8 (&w)[40], int idx, u32x4 (&am)[10], f32x4 (&acc)[NT]) {
#pragma unroll
  for (int k = 0; k < 8; ++k) {
    am[k] = ld128(rs, vo[k], sb);
    if (k < 7) NOPM(PACE); else CFENCE();
  }
  static_for<NK>([&](auto K) {
    constexpr int ks = K;
    smf_w<ks * 2>(acc[0], am[ks], w, idx); smf_w<ks * 2 + 1>(acc[1], am[ks], w, idx);
    if constexpr (ks < 2) { CFENCE(); am[8 + ks] = ld128(rs, vo[8 + ks], sb); CFENCE(); }
    if constexpr (NT == 4) { smf_w<20 + ks * 2>(acc[2], am[ks], w, idx); smf_w<20 + ks * 2 + 1>(acc[3], am[ks], w, idx); }
  });
}
#define DPL " row_ror:8 row_mask:0xf bank_mask:0x3\n\t"
#define DPH " row_ror:8 row_mask:0xf bank_mask:0xc\n\t"
#define DPA " quad_perm:[2,3,0,1] row_mask:0xf bank_mask:0xf\n\t"
#define FADD2(d0, d1, c) asm volatile("v_add_f32 %0, %2, %4\n\tv_add_f32 %1, %3, %5" : "=&v"(d0), "=&v"(d1) : "v"(c[0]), "v"(c[1]), "v"(c[2]), "v"(c[3]))

constexpr unsigned P_RING = 16384;
#define ROWRED(op) "s_nop 1\n\t" op "_dpp %7, %6, %6" DPA op "_dpp %8, %6, %6" DPA "s_nop 1\n\tv_permlane16_swap_b32 %7, %8\n\t" op " %6, %7, %8\n\t" op " %7, %7, %8\n\t" \
                   "s_nop 1\n\tv_permlane32_swap_b32 %6, %7\n\t" op " %6, %6, %7\n\t"
#define SM_SELM "v_cndmask_b32 %4, %24, %13, %26\n\tv_cndmask_b32 %5, %25, %14, %26\n\t"
#define SM_SELX "v_cndmask_b32 %0, %20, %15, %26\n\tv_cndmask_b32 %1, %21, %16, %26\n\tv_cndmask_b32 %2, %22, %17, %26\n\tv_cndmask_b32 %3, %23, %18, %26\n\t"
#define SM_EXPM "v_fma_f32 %4, %4, %19, -%12\n\tv_fma_f32 %5, %5, %19, -%12\n\tv_exp_f32 %4, %4\n\tv_exp_f32 %5, %5\n\t"
#define SM_EXPX "v_fma_f32 %0, %0, %19, -%12\n\tv_fma_f32 %1, %1, %19, -%12\n\tv_exp_f32 %0, %0\n\tv_fma_f32 %2, %2, %19, -%12\n\tv_exp_f32 %1, %1\n\tv_fma_f32 %3, %3, %19, -%12\n\tv_exp_f32 %2, %2\n\t" \
                "v_add_f32 %6, %4, %5\n\tv_exp_f32 %3, %3\n\tv_add_f32 %7, %0, %1\n\tv_add_f32 %6, %6, %2\n\tv_add_f32 %7, %7, %3\n\tv_add_f32 %6, %6, %7\n\t"
#define SM_OUT "v_rcp_f32 %7, %6\n\tv_med3_f32 %8, %6, %27, %28\n\t" \
               "v_mul_f32 %0, %0, %7\n\tv_mul_f32 %1, %1, %7\n\tv_cmp_eq_f32 %11, %8, %6\n\tv_mul_f32 %2, %2, %7\n\tv_mul_f32 %3, %3, %7\n\t" \
               "v_cvt_pk_bf16_f32 v248, |%0|, |%1|\n\tv_cvt_pk_bf16_f32 v250, |%0|, |%1|\n\tv_cvt_pk_bf16_f32 v249, |%2|, |%3|\n\tv_cvt_pk_bf16_f32 v251, |%2|, |%3|\n\t" \
               "v_permlane32_swap_b32 v248, v250\n\tv_mul_f32 %4, %4, %7\n\tv_permlane32_swap_b32 v249, v251\n\tv_mul_f32 %5, %5, %7"
#define SM_OPS : "=&v"(x0), "=&v"(x1), "=&v"(x2), "=&v"(x3), "=&v"(y0), "=&v"(y1), "=&v"(sum), "=&v"(t1), "=&v"(t2), "=&{v[248:251]}"(ox), "=&v"(t3), "=&s"(okm), "+v"(mref) \
               : "v"(s0[2]), "v"(s0[3]), "v"(s2[0]), "v"(s2[1]), "v"(s2[2]), "v"(s2[3]), "s"(cs), "v"(s1[0]), "v"(s1[1]), "v"(s1[2]), "v"(s1[3]), "v"(s0[0]), "v"(s0[1]), \
                 "s"(0xCCCCCCCCCCCCCCCCull), "v"(lo), "v"(hi)
template <int CLS>
DEV void p_loop(const Args& a, rsrc_t rs, int h, int wave, int lane, unsigned pol, const u32x8 (&w)[40], Sched& sc) {
  const int n = lane & 15, g = lane >> 4;
  const int idx = (lane & 2) ? 0xEEEE : 0x4444;
  const float NEG = -3.0e38f;
  const unsigned l16 = lane * 16, cpy = (h & (REPL - 1)) * 65536;
  unsigned vo[10];
#pragma unroll
  for (int k = 0; k < 10; ++k) { vo[k] = l16 + memoff(k); PIN(vo[k]); }
  f32x4 i1, i2;
#pragma unroll
  for (int r = 0; r < 4; ++r) {
    const bool tri = (4 * g + r) <= n;
    i1[r] = (wave == 1 && !tri) ? NEG : 0.f;
    i2[r] = (wave == 1 || (wave == 2 && !tri)) ? NEG : 0.f;
  }
  unsigned kvo[2];
#pragma unroll
  for (int tl = 0; tl < 2; ++tl) {
    int tok = 16 * tl + n;
    if (CLS == 0) { const int gp = n >> 2, r = n & 3; tok = 16 * (r >> 1) + 8 * (gp & 1) + 4 * (gp >> 1) + 2 * tl + (r & 1); }
    kvo[tl] = P_RING + (tok >> 4) * 4096 + (g * 16 + (tok & 15)) * 16;
  }
  const unsigned qvo = P_RING + 8192 + (wave == 2 ? 4096 : 0) + l16;
  unsigned wra = (32 * wave + n) * 16 + g * 4, wrq = 2 * wra;
  unsigned tra = (4 * g + ((lane >> 2) & 3)) * 16 + (lane & 1) * 8, trq = (4 * g + ((lane >> 2) & 3)) * 32 + (lane & 3) * 8;
  PIN(wra); PIN(wrq); PIN(tra); PIN(trq);
  unsigned vox[2], vom[2];
#pragma unroll
  for (int c = 0; c < 2; ++c) {
    if (CLS == 0) {
      vox[c] = (2 * c + (lane >> 5)) * 65536 + C_PX + (h >> 4) * 16384 + (h & 15) * 512 + (lane & 31) * 16;
      vom[c] = lane < 32 ? (2 * c + (lane >> 4)) * 65536 + C_PM + h * 128 + (lane & 15) * 8 : OOB;
      PIN(vox[c]); PIN(vom[c]);
    } else { vox[c] = (h * 2 + (wave - 1)) * 1024 + l16; vom[c] = lane < 16 ? 65536 + (wave - 1) * 8192 + h * 256 + l16 : OOB; }
  }
  const unsigned pvo = prog_off(0, h, (lane >> 2) & 1, lane & 3);
  const float cs = 0.08838834764831845f * 1.4426950408889634f;
  float mref = 0.f, lo = 0x1p-64f, hi = 0x1p64f;
  PIN(lo); PIN(hi);
  int budget = SPIN_CAP, rdy = 0;
  auto fetch = [&](int seg) {
    while (rdy <= seg && --budget > 0) { rdy = wmin(first_open(0, (lane >> 2) & 1, ld32(rs, pvo, 0))); CFENCE(); }
    static_for<4>([&](auto J) {
      const unsigned char* src = a.ws + kq_addr(J >> 1, seg, h, pol) + (J & 1) * 4096;
      set_m0(P_RING + (seg & 1) * 16384 + J * 4096); NOP(0);
      glds_off<0>(src, l16); glds_off<1024>(src, l16); glds_off<2048>(src, l16); glds_off<3072>(src, l16);
    });
  };
  u32x4 kx[2][4], qf[4];
  auto pull = [&](int seg) {
    const unsigned rb = (seg & 1) * 16384;
#pragma unroll
    for (int ks = 0; ks < 4; ++ks) { kx[0][ks] = ldsr128(kvo[0] + rb + ks * 1024); kx[1][ks] = ldsr128(kvo[1] + rb + ks * 1024); if (CLS == 1) qf[ks] = ldsr128(qvo + rb + ks * 1024); }
  };
  if (CLS == 2) { fetch(0); VMWAIT(0); }
  BAR();
  EV(2);
  if (CLS == 2) fetch(1); else pull(0);
  unsigned hold = now8(), pend = 0;
  for (int s = 0; s < NSEG; ++s) {
    unsigned sb = slot_base(s, pol), sbr = sb + cpy;
    const float n0 = g >= 2 ? NEG : 0.f;
    f32x4 s0 = {n0, n0, n0, n0}, s1 = i1, s2 = i2;
    PINS(sb); PINS(sbr);
    if (CLS < 2) {
      PIN(s0); PIN(s1); PIN(s2);
#pragma unroll
      for (int ks = 0; ks < 4; ++ks) { PIN(kx[0][ks]); PIN(kx[1][ks]); }
    }
    if (CLS == 1) {
      NOP(1);
#pragma unroll
      for (int ks = 0; ks < 4; ++ks) { mf_vv(s1, kx[0][ks], qf[ks]); mf_vv(s2, kx[1][ks], qf[ks]); }
    }
    unsigned cst = 0;
    if (CLS == 2) cst = trigger(rs, sc, 2 * s, hold, [&]() { mem_wait(rs, sbr, lane, budget); });
    f32x4 acc[4];
    zero_acc(acc);
    STAMP_DECL;
    unsigned good = 2;
    if (CLS == 2) release(cst); else BAR();
    STAMP(0);
    for (;;) {
      u32x4 am[10];
      project<4, 8>(rs, vo, sbr, w, idx, am, acc);
      float a0, a1, b0, b1;
      smf_w<16>(acc[0], am[8], w, idx); smf_w<17>(acc[1], am[8], w, idx); smf_w<18>(acc[0], am[9], w, idx); smf_w<19>(acc[1], am[9], w, idx);
      smf_w<36>(acc[2], am[8], w, idx); smf_w<37>(acc[3], am[8], w, idx); smf_w<38>(acc[2], am[9], w, idx);
      FADD2(a0, a1, acc[0]);
      smf_w<39>(acc[3], am[9], w, idx);
      FADD2(b0, b1, acc[1]);
      STAMP(1);
      asm volatile("ds_write2_b32 %0, %1, %1 offset0:0 offset1:1" :: "v"(wrq), "v"(pk(a0, a1)) : "memory");
      asm volatile("ds_write2_b32 %0, %1, %1 offset0:128 offset1:129" :: "v"(wrq), "v"(pk(b0, b1)) : "memory");
      const bool bad = __any(__builtin_isunordered(a0, a1));
      PIN(acc[2]);
      const unsigned f2 = foldpk(acc[2]);
      pin1(f2);
      PIN(acc[3]);
      ldsw32(wra + 4096, f2); ldsw32(wra + 4096 + 256, foldpk(acc[3]));
      if (__builtin_expect(!bad, 1)) break;
      if ((!(good & 2) && !has_sent(am)) || --budget <= 0) break;
      good = 0;
      zero_acc(acc);
      mem_wait(rs, sbr, lane, budget);
    }
    BAR();
    STAMP(2);
    u32x4 km[4];
#define TRQ(ks) qf[ks] = cat4(trread(trq + (ks) * 1024), trread(trq + (ks) * 1024 + 512))
#define TRK(ks) km[ks] = cat4(trread(tra + 4096 + (ks) * 512), trread(tra + 4096 + (ks) * 512 + 256))
    if (CLS == 0) {
      TRQ(0); TRQ(1); TRK(0); TRK(1); CFENCE();
      mf_vv(s1, kx[0][0], qf[0]); CFENCE(); TRQ(2); CFENCE(); mf_vv(s2, kx[1][0], qf[0]); CFENCE(); TRK(2); CFENCE();
      mf_vv(s1, kx[0][1], qf[1]); CFENCE(); TRQ(3); CFENCE(); mf_vv(s2, kx[1][1], qf[1]); CFENCE(); TRK(3); CFENCE();
      mf_vv(s0, km[0], qf[0]); mf_vv(s0, km[1], qf[1]); mf_vv(s0, km[2], qf[2]); mf_vv(s0, km[3], qf[3]);
      mf_vv(s1, kx[0][2], qf[2]); mf_vv(s2, kx[1][2], qf[2]); mf_vv(s1, kx[0][3], qf[3]); mf_vv(s2, kx[1][3], qf[3]);
      STAMP(3);
      float x0, x1, x2, x3, y0, y1, sum, t1, t2, t3;
      u32x4 ox; u32x2 om; unsigned long long okm;
      asm volatile(SM_SELM SM_EXPM "s_nop 1\n\t" SM_SELX SM_EXPX ROWRED("v_add_f32") SM_OUT SM_OPS);
      if (__builtin_expect(okm != ~0ull, 0))
        asm volatile(SM_SELM SM_SELX "v_max3_f32 %6, %4, %5, %0\n\tv_max3_f32 %7, %1, %2, %3\n\tv_max_f32 %6, %6, %7\n\t" ROWRED("v_max_f32") "v_mul_f32 %12, %19, %6\n\t"
                     SM_EXPM SM_EXPX ROWRED("v_add_f32") SM_OUT SM_OPS);
      st128(rs, ox, vox[0], sb); st128(rs, ox, vox[1], sb);
      asm volatile("v_cvt_pk_bf16_f32 v252, |%1|, |%2|\n\tv_cvt_pk_bf16_f32 v253, |%1|, |%2|\n\ts_nop 1\n\tv_permlane16_swap_b32 v252, v253" : "=&{v[252:253]}"(om) : "v"(y0), "v"(y1) : "memory");
      st64(rs, om, vom[0], sb); st64(rs, om, vom[1], sb);
      CFENCE();
      STAMP(4);
      STAMP_OUT(h, s);
      report(rs, h, s, good, lane);
      mref += __builtin_amdgcn_logf(sum);
      if (s >= 1) {
        const unsigned rb = slot_base(s - 1, pol);
#pragma unroll
        for (int c = 0; c < 2; ++c) { st128(rs, (u32x4){SENT, SENT, SENT, SENT}, vox[c], rb); st64(rs, (u32x2){SENT, SENT}, vom[c], rb); }
      }
      if (s + 1 < NSEG) pull(s + 1);
    } else if (CLS == 1) {
      TRK(0); TRK(1); TRK(2); TRK(3);
#pragma unroll
      for (int ks = 0; ks < 4; ++ks) mf_vv(s0, km[ks], qf[ks]);
      asm volatile("s_nop 7" : "+v"(s0), "+v"(s1), "+v"(s2));
      float mxv = fmaxf(fmaxf(fmaxf(s1[0], s1[1]), fmaxf(s1[2], s1[3])), fmaxf(fmaxf(s2[0], s2[1]), fmaxf(s2[2], s2[3])));
      mxv = fmaxf(fmaxf(fmaxf(s0[0], s0[1]), fmaxf(s0[2], s0[3])), mxv);
      mxv = xmax32(xmax16(mxv));
      const float mb = -mxv * cs;
      float sum = 0.f;
#pragma unroll
      for (int r = 0; r < 4; ++r) {
        s0[r] = __builtin_amdgcn_exp2f(fmaf(s0[r], cs, mb)); s1[r] = __builtin_amdgcn_exp2f(fmaf(s1[r], cs, mb)); s2[r] = __builtin_amdgcn_exp2f(fmaf(s2[r], cs, mb));
        sum += s0[r] + s1[r] + s2[r];
      }
      sum = xadd32(xadd16(sum));
      const float inv = __builtin_amdgcn_rcpf(sum);
      s0 *= inv; s1 *= inv; s2 *= inv;
      const unsigned so = OFF_PT + s * 81920;
      const u32x2 pmv = pk4(s0);
      auto r0 = __builtin_amdgcn_permlane16_swap(pmv.x, pmv.x, false, false);
      auto r1 = __builtin_amdgcn_permlane16_swap(pmv.y, pmv.y, false, false);
      const u32x4 ptx = cat4(pk4(s1), pk4(s2));
      if (s + 1 < NSEG) { pull(s + 1); __builtin_amdgcn_s_sleep(PTSLEEP); }
      st128(rs, ptx, vox[0], so);
      st128(rs, (u32x4){(unsigned)r0[0], (unsigned)r1[0], (unsigned)r0[1], (unsigned)r1[1]}, vom[0], so);
      STAMP(4);
      STAMP_OUT(h, s);
    } else {
      const unsigned tend = now8() + 24 * 8;
      const u32x4 cur = ld128(rs, 0, OFF_CTL + CTL_CUR);
      rdy = max(rdy, probe_rdy(pend, s, lane));
      adopt_cur(sc, cur, 2 * s + 2);
      mark_overrun(sc, 2 * s, tend);
      if (s + 2 < NSEG) fetch(s + 2);
      pend = 0;
      if (rdy <= s + 5) pend = probe_ld(rs, s + 1, lane);
      STAMP_OUT(h, s);
      hold = tend + HOLDP; clk_update(sc);
    }
  }
  VMWAIT(0);
  BAR();
  if (CLS == 2) st128(rs, (u32x4){1, 1, 1, 1}, lane < 8 ? lane * 16 : OOB, OFF_REP + (NSEG * 128 + h) * 128);
  EV(3);
}
DEV void role_p(const Args& a, rsrc_t rs, int h, int wave, int lane, unsigned pol) {
  u32x8 w[40];
  Sched sc; clk_init(sc);
  load_weights<K_P>(w, a.wq + h * 256, a.wk + h * 256, wave, lane, 15, []() { VMWAIT(0); });
  BAR();
  EV(1);
  if (wave == 0) p_loop<0>(a, rs, h, wave, lane, pol, w, sc);
  else if (wave < 3) p_loop<1>(a, rs, h, wave, lane, pol, w, sc);
  else p_loop<2>(a, rs, h, wave, lane, pol, w, sc);
}

DEV void role_v(const Args& a, rsrc_t rs, int h, int wave, int lane, unsigned pol) {
  u32x8 w[40];
  Sched sc; clk_init(sc);
  load_weights<K_V>(w, a.wv + h * 256, a.wo + (size_t)h * 128 * 1280, wave, lane, 15, []() { VMWAIT(0); });
  BAR();
  EV(1);
  const int n = lane & 15, g = lane >> 4;
  const int idx = (lane & 2) ? 0xEEEE : 0x4444;
  unsigned wra = (16 * wave + (n >> 2) * 2 + (n & 1)) * 32 + g * 8 + ((n >> 1) & 1) * 4, tra = (8 * g + ((lane >> 2) & 3)) * 32 + (lane & 3) * 8;
  unsigned zv0 = g < 2 ? (wave * 2 + g) * 16384 + h * 256 + n * 16 : OOB;
  unsigned zv1 = (8 + (g >> 1) * 8 + wave * 2 + (g & 1)) * 16384 + h * 256 + n * 16, zv2 = zv1 + 16 * 16384;
  PIN(wra); PIN(tra); PIN(zv0); PIN(zv1); PIN(zv2);
  unsigned vo[10];
#pragma unroll
  for (int k = 0; k < 10; ++k) { vo[k] = lane * 16 + memoff(k); PIN(vo[k]); }
  int budget = SPIN_CAP;
  unsigned hold = now8();
  for (int s = 0; s < NSEG; ++s) {
    unsigned sb = slot_base(s, pol) + (h & (REPL - 1)) * 65536, zb = z_base(s, pol);
    PINS(sb); PINS(zb);
    f32x4 c[10], acc[2];
    zero_acc(c); zero_acc(acc);
    unsigned cst;
    if (wave == 3) cst = trigger(rs, sc, 2 * s, hold, [&]() { mem_wait(rs, sb, lane, budget); }); else cst = cycles();
    STAMP_DECL;
    unsigned good = 2;
    release(__builtin_amdgcn_readfirstlane(cst));
    STAMP(0);
    for (;;) {
      u32x4 am[10];
      project<2, 10>(rs, vo, sb, w, idx, am, acc);
      STAMP(1);
      asm volatile("s_nop 7" : "+v"(acc[0]), "+v"(acc[1]));
      ldsw32(wra, foldpk(acc[0])); ldsw32(wra + 256, foldpk(acc[1]));
      if (__builtin_expect(!has_nan(acc[0]), 1)) break;
      if ((!(good & 2) && !has_sent(am)) || --budget <= 0) break;
      good = 0;
      zero_acc(acc);
      mem_wait(rs, sb, lane, budget);
    }
    BAR();
    STAMP(2);
    u32x4 va[2];
#pragma unroll
    for (int ks = 0; ks < 2; ++ks) va[ks] = cat4(trread(tra + ks * 1024), trread(tra + ks * 1024 + 128));
    CFENCE(); const u32x4 cur = ld128(rs, 0, OFF_CTL + CTL_CUR); CFENCE();
    u32x4 O0, O1, O2;
#define VMM(t, k) smf_w<20 + ((t) >> 1) * 4 + 2 * (k) + ((t) & 1)>(c[t], va[k], w, idx)
#define VEA(t, T) FADD2(T##0, T##1, c[t])
#define VEC2(R, ra, rb, O) asm volatile("v_cvt_pk_bf16_f32 " ra ", %1, %2\n\tv_cvt_pk_bf16_f32 " rb ", %3, %4" : "+{" R "}"(O) : "v"(ta0), "v"(ta1), "v"(tb0), "v"(tb1))
#define VSW(txt, R, O) asm volatile(txt : "+{" R "}"(O))
    float ta0, ta1, tb0, tb1;
    asm volatile("" : "={v[232:235]}"(O0), "={v[236:239]}"(O1), "={v[240:243]}"(O2));
    VMM(0, 0); VMM(1, 0); VMM(0, 1); VMM(1, 1);
    VMM(2, 0); VMM(3, 0); VMM(2, 1); VMM(3, 1);
    VMM(4, 0); NOP(1); VMM(5, 0); VEA(0, ta); VMM(4, 1); VEA(1, tb); VMM(5, 1); VEC2("v[232:235]", "v232", "v233", O0);
    VMM(6, 0); VEA(2, ta); VMM(7, 0); VEA(3, tb); VMM(6, 1); VSW("v_permlane16_swap_b32 v232, v233", "v[232:235]", O0); VMM(7, 1); VEC2("v[236:239]", "v236", "v237", O1);
    VMM(8, 0); VSW("v_permlane32_swap_b32 v232, v234", "v[232:235]", O0); VMM(9, 0); VSW("v_permlane32_swap_b32 v233, v235", "v[232:235]", O0);
    VMM(8, 1); VEA(4, ta); VMM(9, 1); CFENCE(); st128(rs, O0, zv0, zb); CFENCE(); VEA(5, tb);
    VEC2("v[236:239]", "v238", "v239", O1);
    VEA(6, ta); VEA(7, tb);
    VSW("v_permlane32_swap_b32 v236, v238\n\tv_permlane32_swap_b32 v237, v239", "v[236:239]", O1);
    VEC2("v[240:243]", "v240", "v241", O2);
    VSW("v_permlane16_swap_b32 v236, v237\n\tv_permlane16_swap_b32 v238, v239", "v[236:239]", O1);
    CFENCE(); st128(rs, O1, zv1, zb); CFENCE();
    VEA(8, ta); VEA(9, tb); VEC2("v[240:243]", "v242", "v243", O2);
    VSW("s_nop 0\n\tv_permlane32_swap_b32 v240, v242\n\tv_permlane32_swap_b32 v241, v243\n\ts_nop 1\n\tv_permlane16_swap_b32 v240, v241\n\tv_permlane16_swap_b32 v242, v243", "v[240:243]", O2);
    CFENCE(); st128(rs, O2, zv2, zb);
    CFENCE();
    STAMP(4);
    STAMP_OUT(32 + h, s);
    if (wave == 0) report(rs, 32 + h, s, good, lane);
    if (wave == 3) { const unsigned tend = now8(); hold = tend + HOLDP; adopt_cur(sc, cur, 2 * s + 2); mark_overrun(sc, 2 * s, tend - VSLACK); clk_update(sc); }
  }
  VMWAIT(0);
  BAR();
  if (wave == 0) st128(rs, (u32x4){1, 1, 1, 1}, lane < 8 ? lane * 16 : OOB, OFF_REP + (NSEG * 128 + 32 + h) * 128);
}

#define CLOB_A "a0", "a1", "a2", "a3", "a4", "a5", "a6", "a7", "a8", "a9", "a10", "a11", "a12", "a13", "a14", "a15", "a16", "a17", "a18", "a19", "a20", "a21", "a22", "a23", "a24", "a25", "a26", "a27", "a28", "a29", "a30", "a31", "a32", "a33", "a34", "a35", "a36", "a37", "a38", "a39", "a40", "a41", "a42", "a43", "a44", "a45", "a46", "a47", "a48", "a49", "a50", "a51", "a52", "a53", "a54", "a55", "a56", "a57", "a58", "a59", "a60", "a61", "a62", "a63", "a64", "a65", "a66", "a67", "a68", "a69", "a70", "a71", "a72", "a73", "a74", "a75", "a76", "a77", "a78", "a79", "a80", "a81", "a82", "a83", "a84", "a85", "a86", "a87", "a88", "a89", "a90", "a91", "a92", "a93", "a94", "a95", "a96", "a97", "a98", "a99", "a100", "a101", "a102", "a103", "a104", "a105", "a106", "a107", "a108", "a109", "a110", "a111", "a112", "a113", "a114", "a115", "a116", "a117", "a118", "a119", "a120", "a121", "a122", "a123", "a124", "a125", "a126", "a127"
template <int J> DEV void c_ldb(unsigned l16, const unsigned char* src) {
  asm volatile("global_load_dwordx4 a[%c2:%c2+3], %0, %1 sc1\n\tglobal_load_dwordx4 a[%c2+4:%c2+7], %0, %1 offset:1024 sc1\n\t"
               "global_load_dwordx4 a[%c2+8:%c2+11], %0, %1 offset:2048 sc1\n\tglobal_load_dwordx4 a[%c2+12:%c2+15], %0, %1 offset:3072 sc1"
               :: "v"(l16), "s"(src + (J >> 1) * BSTR + (J & 1) * 4096), "n"(J * 16) : "memory", CLOB_A);
}
template <int J> DEV void c_smf(f32x4& acc, u32x4 p, int idx) { asm volatile("v_smfmac_f32_16x16x64_bf16 %0, %1, a[%c3:%c3+7], %2" : "+{v[248:251]}"(acc) : "v"(p), "v"(idx), "n"(J * 8)); }
#define ZFIRST 0
#define ASLEEP -1
#define BDELAY 30
DEV void c_arm(rsrc_t rs, int s, int cb, int lane, unsigned pol) {
  const unsigned zn = z_base(s, pol) + cb * 16384;
#pragma unroll
  for (int j = 0; j < 8; ++j) st128(rs, (u32x4){SENT, SENT, SENT, SENT}, lane * 16 + (j & 3) * 1024, zn + (j >> 2) * 4096);
}
DEV void role_c(const Args& a, rsrc_t rs, int cb, int wave, int lane, unsigned pol) {
  Sched sc; clk_init(sc);
  c_arm(rs, wave, cb, lane, pol);
  VMWAIT(0);
  BAR();
  const unsigned pvo = prog_off(1, lane >> 1, lane & 1, (cb >> 1) & 3);
  if (wave == 3) {
    int rdy = 0;
    for (int s = 0; s < NSEG - 1; ++s) {
      BAR(); BAR();
      unsigned v = 0;
      if (rdy <= s + 4) v = probe_ld(rs, s, lane);
      BAR();
      rdy = max(rdy, probe_rdy(v, s, lane));
      ldsw32(L_TM + 32, rdy);
    }
    return;
  }
  if (wave == 2) {
    const unsigned rvo = (lane >> 4) < REPL ? (lane >> 4) * 65536 + (cb < 32 ? cb * 256 : C_MEM1 + (cb - 32) * 256) + (lane & 15) * 16 : OOB;
    for (int s = 0; s < NSEG - 1; ++s) {
      BAR(); VMWAIT(0); BAR();
      if (ASLEEP >= 0) { __builtin_amdgcn_s_sleep(ASLEEP >= 0 ? ASLEEP : 0); if (s + 4 < NSEG) c_arm(rs, s + 4, cb, lane, pol); }
      BAR();
      st128(rs, (u32x4){SENT, SENT, SENT, SENT}, rvo, slot_base(s, pol));
      if (ASLEEP < 0 && s + 4 < NSEG) c_arm(rs, s + 4, cb, lane, pol);
    }
    return;
  }
  int budget = SPIN_CAP;
  const unsigned cpy = ((cb / 10) & (REPL - 1)) * 65536;
  auto c_wait = [&](unsigned sb, unsigned zb) {
    for (;;) {
      const unsigned u = ld32(rs, C_PM + (lane & 31) * 128, sb), v = ld32(rs, cb * 16384 + lane * 128, zb);
      CFENCE();
      if (__all(u != SENT && v != SENT) || --budget <= 0) break;
      __builtin_amdgcn_s_sleep(PSLEEP);
    }
  };
  if (wave == 1) {
    unsigned hold = now8();
    for (int s = 0; s < NSEG - 1; ++s) {
      release(trigger(rs, sc, 2 * s + 1, hold, [&]() { c_wait(slot_base(s, pol) + cpy, z_base(s, pol)); }));
      clk_update(sc);
      BAR();
      hold = now8();
      adopt_cur(sc, ld128(rs, 0, OFF_CTL + CTL_CUR), 2 * s + 3);
      mark_overrun(sc, 2 * s + 1, hold);
      hold += HOLDC;
      if (sc.j0 > sc.jbad) wait_until(tau_of(sc, 2 * s + 2) + BDELAY * 8);
      BAR();
    }
    return;
  }
  const int n = lane & 15, g = lane >> 4;
  const int idx = (lane & 2) ? 0xEEEE : 0x4444;
  const unsigned l16 = lane * 16;
  unsigned pxvo[4], zvo[2], pmvo = C_PM + 2 * g * 128 + n * 8;
#pragma unroll
  for (int i = 0; i < 4; ++i) { pxvo[i] = C_PX + (i >> 1) * 16384 + (i & 1) * 4096 + l16; PIN(pxvo[i]); }
#pragma unroll
  for (int i = 0; i < 2; ++i) { zvo[i] = cb * 16384 + i * 4096 + l16; PIN(zvo[i]); }
  const unsigned mvo = (cb < 32 ? cb * 256 : C_MEM1 + (cb - 32) * 256) + mrow(2 * g + (n >> 3), (n & 7) >> 2) * 16 + (n & 3) * 4;
  unsigned oob = OOB; PIN(oob); PIN(pmvo);
  {
    const unsigned short* m0 = (const unsigned short*)a.mem0 + (2 * g + (n >> 3)) * 640 + cb * 16 + (n & 7);
    const unsigned v = min(m0[0] | (unsigned)m0[8] << 16, 0xFFFFFFFEu);
#pragma unroll
    for (int c = 0; c < REPL; ++c) st32(rs, v, mvo, slot_base(0, pol) + c * 65536);
  }
  int rdy = 0;
  auto fetch = [&](int seg) {
    while (rdy <= seg && --budget > 0) { rdy = wmin(2 * (int)ld32(rs, pvo, 0) + (lane & 1)); CFENCE(); }
    const unsigned char* src = a.ws + b_addr(seg, cb, pol);
    static_for<8>([&](auto J) { c_ldb<J>(l16, src); });
  };
  fetch(0);
  for (int s = 0; s < NSEG - 1; ++s) {
    unsigned sb = slot_base(s, pol) + cpy, zb = z_base(s, pol), sbn[REPL];
#pragma unroll
    for (int c = 0; c < REPL; ++c) { sbn[c] = slot_base(s + 1, pol) + c * 65536; PINS(sbn[c]); }
    PINS(sb); PINS(zb);
    STAMP_DECL;
    unsigned good = 2;
    BAR();
    STAMP(0);
    for (;;) {
      u32x4 z[8], pm[4], px[16];
#pragma unroll
      for (int j = 0; j < ZFIRST; ++j) { z[j] = ld128(rs, zvo[j >> 2] + (j & 3) * 1024, zb); CFENCE(); }
#pragma unroll
      for (int j = 0; j < 16; ++j) px[j] = ld128(rs, pxvo[j >> 2] + (j & 3) * 1024, sb);
      CFENCE();
#pragma unroll
      for (int j = 0; j < 4; ++j) pm[j] = cat4(ld64(rs, pmvo + j * 1024, sb), ld64(rs, pmvo + j * 1024 + 128, sb));
      CFENCE();
#pragma unroll
      for (int j = ZFIRST; j < 8; ++j) { z[j] = ld128(rs, zvo[j >> 2] + (j & 3) * 1024, zb); CFENCE(); }
      f32x4 acc = {0, 0, 0, 0};
      asm volatile("s_nop 1" : "+{v[248:251]}"(acc));
      static_for<16>([&](auto J) { c_smf<J>(acc, px[J], idx); });
#pragma unroll
      for (int j = 0; j < 4; ++j) asm volatile("v_smfmac_f32_16x16x64_bf16 %0, %1, %2, %3" : "+{v[248:251]}"(acc) : "v"(pm[j]), "v"(cat8(z[2 * j], z[2 * j + 1])), "v"(idx));
      STAMP(1);
      unsigned o, vo2; unsigned long long okm;
      asm volatile(
        "s_nop 7\n\t"
        "v_add_f32 v248, v248, v250\n\t"
        "v_add_f32 v250, v249, v251\n\t"
        "v_add_f32 v249, v249, v251\n\t"
        "v_mov_b32_dpp v250, v248" DPL
        "s_nop 0\n\t"
        "v_mov_b32_dpp v248, v249" DPH
        "v_cmp_o_f32 %3, v248, v250\n\t"
        "v_cvt_pk_bf16_f32 %1, v248, v250\n\t"
        "s_nop 0\n\t"
        "v_cndmask_b32 %2, %5, %4, %3"
        : "+{v[248:251]}"(acc), "=&v"(o), "=&v"(vo2), "=&s"(okm) : "v"(mvo), "v"(oob));
#pragma unroll
      for (int c = 0; c < REPL; ++c) st32(rs, o, vo2, sbn[c]);
      CFENCE();
      if (__builtin_expect(okm == ~0ull, 1)) break;
      if ((!(good & 2) && !(has_sent(z) || has_sent(pm) || has_sent(px))) || --budget <= 0) {
#pragma unroll
        for (int c = 0; c < REPL; ++c) st32(rs, min(o, 0xFFFFFFFEu), mvo, sbn[c]);
        break;
      }
      good = 0;
      c_wait(sb, zb);
    }
    STAMP(4);
    STAMP_OUT(64 + cb, s);
    report(rs, 64 + cb, s, good, lane);
    BAR();
    if (s == 0) EV(2); if (s == NSEG - 2) EV(3);
    if (s > 0) rdy = max(rdy, (int)__builtin_amdgcn_readfirstlane(*(volatile LAS unsigned*)(uintptr_t)(L_TM + 32)));
    BAR();
    if (s + 2 < NSEG) fetch(s + 1);
  }
}

template <int IMG> DEV void u_scatter(const f32x4 (&acc)[4][2], unsigned base) {
  PIN(base);
  LAS char* p = (LAS char*)(uintptr_t)base;
#pragma unroll
  for (int c = 0; c < 4; ++c)
#pragma unroll
    for (int tt = 0; tt < 2; ++tt)
#pragma unroll
      for (int r = 0; r < 4; ++r) *(LAS float*)(p + IMG + (tt * 16 + r) * 272 + c * 64) = acc[c][tt][r];
}
template <int IMG> DEV void u_gather(f32x4& v0, f32x4& v1, unsigned base) {
  PIN(base);
  LAS char* p = (LAS char*)(uintptr_t)base;
  v0 = (f32x4){0, 0, 0, 0}; v1 = v0;
#pragma unroll
  for (int ww = 0; ww < 4; ++ww) { v0 += *(LAS f32x4*)(p + IMG + ww * 8704); v1 += *(LAS f32x4*)(p + IMG + ww * 8704 + 16); }
}
#define UOFF 240
#define UOFF_LAST 240
#define USLEEP 0
#define UPACE 0
#define USPLIT 48
DEV void role_u(const Args& a, rsrc_t rs, int cq, int seg0, int dseg, int lim, int wave, int lane, unsigned pol) {
  const int tid = wave * 64 + lane;
  const unsigned l16 = lane * 16;
  u32x4 w[56];
  load_w2(w, a.w2 + cq * 128, wave, lane);
  BAR();
  EV(7);
  const int nks = wave < 3 ? 14 : 12;
  const unsigned l16x = wave < 3 ? l16 : OOB;
  const unsigned sca = wave * 8704 + (lane >> 4) * 4 * 272 + (lane & 15) * 4, gaa = (tid >> 3) * 272 + (tid & 7) * 32;
  int budget = SPIN_CAP;
  Sched sc; clk_init(sc);
  const unsigned tu0 = rtc();
  for (int seg = seg0; seg < lim; seg += dseg) {
    while ((int)(rtc() - tu0) < seg * UPACE) __builtin_amdgcn_s_sleep(8);
    const bool last = seg == NSEG - 1, ract = lane >= 16 && lane < (last ? 32 : 24);
    const unsigned rvo = ract ? OFF_REP + ((seg + 1) * 128 + ((lane >> 3) & 1) * 32 + wave * 8 + (lane & 7)) * 128 : OOB;
    const bool past = sc.j0 != 0 && (int)(now8() - tau_of(sc, 2 * seg + 3)) > 400 * 8;
    const unsigned pvo = lane < nks ? hprog_off(seg & 1, wave * 14 + lane) : lane == 14 ? OFF_CTL + CTL_RL + seg * 128 : past ? rvo : OOB;
    const unsigned need = lane < nks ? (seg >> 1) + 1 : lane == 14 ? 1 : 0;
    unsigned have;
    for (;;) { have = ld32(rs, pvo, 0); CFENCE(); if (__all(have >= need) || --budget <= 0) break; __builtin_amdgcn_s_sleep(16); }
    const bool chain_ok = __all(!ract || have != 0);
    f32x4 acc[4][2];
    u32x4 bx[4][8];
    auto ldb = [&](int c) {
#pragma unroll
      for (int hh = 0; hh < 8; ++hh) bx[c][hh] = ld128(rs, l16 + (hh & 3) * 1024, b_addr(seg, cq * 4 + c, pol) + wave * BSTR + (hh & 4) * 1024);
    };
    {
      u32x4 hf[14][2];
#pragma unroll
      for (int k = 0; k < 14; ++k)
#pragma unroll
        for (int tt = 0; tt < 2; ++tt) hf[k][tt] = ld128(rs, (k < 12 ? l16 : l16x) + tt * 1024, OFF_HH + (seg * 54 + wave * 14 + k) * 2048);
      CFENCE(); ldb(0); CFENCE();
      static_for<14>([&](auto K) {
        static_for<4>([&](auto C) {
          constexpr int k = K, c = C, wi = ((k >> 1) * 2 + (c >> 1)) * 2 + (c & 1);
          mf_w<wi, k & 1, false, k == 0>(acc[c][0], w, hf[k][0]); mf_w<wi, k & 1, false, k == 0>(acc[c][1], w, hf[k][1]);
        });
        if constexpr (K == 1) { CFENCE(); ldb(1); CFENCE(); }
        if constexpr (K == 5) { CFENCE(); ldb(2); CFENCE(); }
        if constexpr (K == 9) { CFENCE(); ldb(3); CFENCE(); }
      });
    }
    const size_t oo = (size_t)(seg * 32 + (tid >> 3)) * 1280 + cq * 128 + (tid & 7) * 16;
    const u32x4 xv = *(const u32x4*)(a.x + oo);
    acc_wait(acc);
    u_scatter<0>(acc, sca);
#pragma unroll
    for (int hh = 0; hh < 8; ++hh) asm volatile("" : "+a"(bx[0][hh]));
    if (!chain_ok) {
      sleep_until_turn(rs, sc, last ? 2 * seg + 1 : 2 * seg + 3, last ? UOFF_LAST : UOFF);
      for (;;) { const unsigned v = ld32(rs, rvo, 0); CFENCE(); if (__all(!ract || v != 0) || --budget <= 0) break; if (last) __builtin_amdgcn_s_sleep(2); else __builtin_amdgcn_s_sleep(16); }
    }
    EV(8);
    const unsigned so = OFF_PT + seg * 81920;
    f32x4 g0, g1;
    {
      u32x4 px[8][2];
#pragma unroll
      for (int hh = 0; hh < 8; ++hh)
#pragma unroll
        for (int tt = 0; tt < 2; ++tt) px[hh][tt] = ld128(rs, l16 + tt * 1024, so + (wave * 8 + hh) * 2048);
      BAR();
      u_gather<0>(g0, g1, gaa);
#pragma unroll
      for (int r = 0; r < 4; ++r) { g0[r] = rbf(silu(rbf(g0[r]))); g1[r] = rbf(silu(rbf(g1[r]))); }
#pragma unroll
      for (int c = 0; c < 4; ++c) { acc[c][0] = (f32x4){0, 0, 0, 0}; acc[c][1] = (f32x4){0, 0, 0, 0}; }
      acc_wait(acc);
      u32x4 pm[2][2], z[4][2];
#pragma unroll
      for (int hh = 0; hh < 8; ++hh) {
#pragma unroll
        for (int c = 0; c < 4; ++c) { if (c == 0) { mf_va(acc[c][0], px[hh][0], bx[c][hh]); mf_va(acc[c][1], px[hh][1], bx[c][hh]); } else { mf_vv(acc[c][0], px[hh][0], bx[c][hh]); mf_vv(acc[c][1], px[hh][1], bx[c][hh]); } }
        CFENCE();
        if (hh == 0) {
#pragma unroll
          for (int hf = 0; hf < 2; ++hf)
#pragma unroll
            for (int tt = 0; tt < 2; ++tt) pm[hf][tt] = ld128(rs, l16 + hf * 1024, so + 65536 + tt * 8192 + wave * 2048);
        }
        if (hh == 1 || hh == 2) {
#pragma unroll
          for (int c = 2 * hh - 2; c < 2 * hh; ++c)
#pragma unroll
            for (int hf = 0; hf < 2; ++hf) z[c][hf] = ld128(rs, l16 + hf * 1024, z_base(seg, pol) + (cq * 4 + c) * 16384 + wave * 2048);
        }
        CFENCE();
      }
#pragma unroll
      for (int hf = 0; hf < 2; ++hf)
#pragma unroll
        for (int c = 0; c < 4; ++c) { mf_vv(acc[c][0], pm[hf][0], z[c][hf]); mf_vv(acc[c][1], pm[hf][1], z[c][hf]); }
    }
    adopt_cur(sc, ld128(rs, 0, OFF_CTL + CTL_CUR), 2 * NSEG);
    acc_wait(acc);
    u_scatter<34816>(acc, sca);
    BAR();
    f32x4 s0, s1;
    u_gather<34816>(s0, s1, gaa);
    u32x4 o;
#pragma unroll
    for (int j = 0; j < 2; ++j) {
      o[j] = pk(__builtin_bit_cast(float, xv[j] << 16) + rbf(rbf(s0[2 * j]) * g0[2 * j]), __builtin_bit_cast(float, xv[j] & 0xFFFF0000u) + rbf(rbf(s0[2 * j + 1]) * g0[2 * j + 1]));
      o[2 + j] = pk(__builtin_bit_cast(float, xv[2 + j] << 16) + rbf(rbf(s1[2 * j]) * g1[2 * j]), __builtin_bit_cast(float, xv[2 + j] & 0xFFFF0000u) + rbf(rbf(s1[2 * j + 1]) * g1[2 * j + 1]));
    }
    *(u32x4*)(a.out + oo) = o;
    EV(9);
  }
}

#define WDELAY 18
#define WPACE 0
#define WBLAG 1
#define WAUX 16
#define WAHEAD 3
DEV void role_w(rsrc_t rs, int xq, int k, int wave, int lane, unsigned pol) {
  const int cb0 = 10 * xq + 2 * k;
  unsigned sink = 0;
  int budget = SPIN_CAP;
  Sched sc; clk_init(sc);
  for (int seg = 1 + wave; seg < NSEG; seg += 4) {
    const int s = seg - WAHEAD;
    if (s >= 2) {
      while (sc.j0 == 0 && --budget > 0) { adopt_cur(sc, ld128(rs, 0, OFF_CTL + CTL_CUR), 2 * NSEG); CFENCE(); __builtin_amdgcn_s_sleep(8); }
      sleep_until_turn(rs, sc, 2 * s + 1, WDELAY * 8 - 240);
    }
    while (__builtin_amdgcn_readfirstlane(ld32(rs, seg * 128, OFF_CTL + CTL_RL)) == 0 && --budget > 0) { CFENCE(); __builtin_amdgcn_s_sleep(8); }
    if (s >= 2) wait_until(tau_of(sc, 2 * s + 1) + WDELAY * 8);
#define WLANES 64
    constexpr int NI = 64 / WLANES;
    unsigned v[12 * NI];
    const unsigned wvo = lane < WLANES ? lane * 128 : OOB;
#pragma unroll
    for (int j = 0; j < 12 * NI; ++j) {
      const int b = j / NI, u = min(k + 5 * (b - 8), 15);
      const unsigned so = b < 8 ? b_addr(max(seg - WBLAG, 0), cb0 + (b >> 2), pol) + (b & 3) * BSTR : kq_addr(u & 1, seg, (u >> 1) * 4 + xq, pol);
      v[j] = __builtin_amdgcn_raw_buffer_load_b32(rs, wvo, so + (j % NI) * WLANES * 128, WAUX); CFENCE(); __builtin_amdgcn_s_sleep(WPACE);
    }
#pragma unroll
    for (int j = 0; j < 12 * NI; ++j) sink += v[j];
    CFENCE();
  }
  asm volatile("" :: "v"(sink));
}

#define LEAD1 84
#define LEAD2 64
#define GEXTRA 300
#define ATOL 8
#define NADAPT 4
#define TKLAG 900
#define TKOFF 200
#define TKSLEEP 2
#define GPOLL 2600
#define GPOLL0 2600
DEV void role_tk(rsrc_t rs, int lane) {
  Sched sc; clk_init(sc);
  Sched old = sc;
  int budget = SPIN_CAP, n1c = 0, n2c = 0, m1 = 0, m2 = 0, relock = 0, jp1 = -9, jp2 = -9;
  unsigned Mp1 = 0, Mp2 = 0;
  bool locked = false;
  const unsigned tref = now8();
  auto tau_at = [&](int j) { return sc.j0 <= j ? tau_of(sc, j) : tau_of(old, j); };
  auto publish = [&](int j) {
    st128(rs, (u32x4){(unsigned)sc.j0, sc.base, (unsigned)sc.T, (unsigned)sc.D1 | (unsigned)sc.rx << 16}, lane < 8 ? lane * 16 : OOB, OFF_CTL + CTL_CUR);
    n1c = n2c = m1 = m2 = 0;
  };
  for (int j = 0; j < 2 * NSEG - 6; ++j) {
    if (locked) {
      while ((int)(now8() - tau_at(j + 2)) > TKOFF - 160) ++j;
      if (j >= 2 * NSEG - 6) break;
      wait_until(tau_at(j + 1) + TKOFF);
    }
    const unsigned vo = rep_off(j, lane);
    u32x4 e;
    for (;;) { e = ld128(rs, vo, OFF_REP); CFENCE(); if (__all(e.x != 0) || --budget <= 0) break; __builtin_amdgcn_s_sleep(TKSLEEP); }
    const bool zrow = !(j & 1) && lane >= 32;
    const unsigned M = tref + (unsigned)wmax((int)(e.y - tref) - (zrow ? VSLACK : 0));
    locked = __all(e.z & 1) && j >= relock;
    if (j + 1 >= relock) {
      const bool off = sc.j0 == 0 || (int)(M - tau_at(j + 1)) > LATE_THR || (j >= relock && !__any(e.z & 1));
      if (off) locked = false;
      if (off && (int)(now8() - M) < TKLAG) {
        int gap = sc.j0 == 0 ? GPOLL0 : GPOLL;
        if (j <= relock && jp2 == j - 2) gap = max(gap, min((int)(M - Mp2) + GEXTRA, 3600));
        sc.rx = j + 3;
        sc.base += M + gap - tau_of(sc, j + 3); sc.j0 = relock = j + 3;
        publish(j);
      }
    }
    Mp2 = Mp1; Mp1 = M; jp2 = jp1; jp1 = j;
    if (locked && sc.j0 <= j && __all((e.z & 3) == 3)) {
      const int d = wmax((int)((float)(int)(e.z >> 2) * 2048.f * __builtin_amdgcn_rcpf((float)e.w)) - (zrow ? VSLACK : 0));
      if (j & 1) { if (n2c < NADAPT) { m2 += d; ++n2c; } } else if (n1c < NADAPT) { m1 += d; ++n1c; }
      if (n1c == NADAPT && n2c == NADAPT) {
        const int n1 = m1 / NADAPT + LEAD1, n2 = m2 / NADAPT + LEAD2, sn = (j >> 1) + 3;
        if (abs(n1 - sc.D1) > ATOL || abs(n2 - (sc.T - sc.D1)) > ATOL) {
          old = sc;
          sc.base += sn * (sc.T - (n1 + n2)); sc.T = n1 + n2; sc.D1 = n1; sc.j0 = 2 * sn;
          publish(j);
        }
        n1c = n2c = m1 = m2 = 0;
      }
    }
  }
}

DEV float rowsum16(float v) {
  v += __builtin_bit_cast(float, __builtin_amdgcn_update_dpp(0, __builtin_bit_cast(int, v), 0xB1, 0xf, 0xf, false));
  v += __builtin_bit_cast(float, __builtin_amdgcn_update_dpp(0, __builtin_bit_cast(int, v), 0x4E, 0xf, 0xf, false));
  v += __builtin_bit_cast(float, __builtin_amdgcn_update_dpp(0, __builtin_bit_cast(int, v), 0x124, 0xf, 0xf, false));
  v += __builtin_bit_cast(float, __builtin_amdgcn_update_dpp(0, __builtin_bit_cast(int, v), 0x128, 0xf, 0xf, false));
  return v;
}
DEV void layernorm(const Args& a, rsrc_t rs, int seg, int wave, int lane) {
  const int sub = lane & 15;
  u32x4 v[2][5];
#pragma unroll
  for (int p = 0; p < 2; ++p)
#pragma unroll
    for (int i = 0; i < 5; ++i) v[p][i] = *(const u32x4*)(a.x + (size_t)(seg * 32 + wave * 8 + p * 4 + (lane >> 4)) * 1280 + (sub + 16 * i) * 16);
#pragma unroll
  for (int p = 0; p < 2; ++p) {
    float f[40], sum = 0.f;
#pragma unroll
    for (int i = 0; i < 5; ++i)
#pragma unroll
      for (int j = 0; j < 4; ++j) { f[i * 8 + 2 * j] = __builtin_bit_cast(float, v[p][i][j] << 16); f[i * 8 + 2 * j + 1] = __builtin_bit_cast(float, v[p][i][j] & 0xFFFF0000u); sum += f[i * 8 + 2 * j] + f[i * 8 + 2 * j + 1]; }
    const float mean = rowsum16(sum) * (1.f / 640.f);
    float sq = 0.f;
#pragma unroll
    for (int i = 0; i < 40; ++i) { const float d = f[i] - mean; sq = fmaf(d, d, sq); }
    const float mb = rbf(mean), vb = rbf(rowsum16(sq) * (1.f / 640.f));
    const float inv = 1.f / rbf(sqrtf(rbf(vb + 1e-5f)));
    const int row = wave * 8 + p * 4 + (lane >> 4);
#pragma unroll
    for (int i = 0; i < 5; ++i) {
      u32x4 o;
#pragma unroll
      for (int j = 0; j < 4; ++j) o[j] = pk(rbf(f[i * 8 + 2 * j] - mb) * inv, rbf(f[i * 8 + 2 * j + 1] - mb) * inv);
      const int c = sub + 16 * i;
      ldsw128(((c >> 2) * 2 + (row >> 4)) * 1024 + ((c & 3) * 16 + (row & 15)) * 16, o);
    }
  }
  BAR();
#pragma unroll
  for (int i = 0; i < 10; ++i) st128(rs, ldsr128((wave * 10 + i) * 1024 + lane * 16), lane * 16, OFF_XN + seg * 40960 + (wave * 10 + i) * 1024);
  VMWAIT(0);
  BAR();
  if (wave == 0) st32(rs, 1, lane < 32 ? seg * 128 + lane * 4 : OOB, OFF_CTL + CTL_XP);
}

__global__ void __launch_bounds__(256, 1) k_main(Args a) {
  const int lane = threadIdx.x & 63, wave = __builtin_amdgcn_readfirstlane(threadIdx.x >> 6);
  const rsrc_t rs = __builtin_amdgcn_make_buffer_rsrc(a.ws, 0, WS_BYTES, 0x00020000);
  EV(10);
  const u32x4 pr = ld128(rs, 0, OFF_CTL + CTL_POL);
  const unsigned xoff = __builtin_amdgcn_readfirstlane(ld32(rs, 16, OFF_CTL + CTL_POL));
  const int b = blockIdx.x, xq = (b + xoff) & 7, ix = b >> 3;
  EV(0);
  const unsigned pol = __builtin_amdgcn_readfirstlane((pr.y < pr.x ? 1u : 0u) | (pr.w < pr.z ? 2u : 0u)) ^ ((xoff & 4) ? 3u : 0u);
  if (xq < 4) {
    const int r = ix * 4 + xq;
    if (r < 32) role_p(a, rs, r, wave, lane, pol);
    else if (r < 64) role_v(a, rs, r - 32, wave, lane, pol);
    else {
      layernorm(a, rs, r - 64, wave, lane);
      EV(1);
      if (r < 104) role_c(a, rs, 10 * xq + ((r - 64) >> 2), wave, lane, pol);
      else if (r < 124) role_w(rs, xq, (r - 104) >> 2, wave, lane, pol);
      else if (r == 124 && wave == 0) role_tk(rs, lane);
      else if (r == 125 && wave == 0) role_agg(rs, lane);
    }
  } else {
    const int pid = (xq - 4) * 16 + (ix >> 1), par = ix & 1;
    const int h = pid >> 1;
    int ui = -1;
    if (pid & 1) {
      role_x<K_VB>(a, rs, h, par, NSEG, NSEG, prog_off(1, h, par, wave), wave, lane, pol);
      if (h < 30) ui = h * 2 + par;
    } else {
      role_x<K_QK>(a, rs, h, par, 2 * QKN, par ? 2 * QKN : NSEG, prog_off(0, h, par, wave), wave, lane, pol);
      if (par && h < 28) role_x<K_F1>(a, rs, (h % 14) * 4 + wave, h / 14, NSEG, NSEG, hprog_off(h / 14, (h % 14) * 4 + wave), wave, lane, pol);
      if (par ? h >= 12 : h < 30) ui = par ? 60 + h - 12 : 80 + h;
    }
    if (ui >= 0) role_u(a, rs, ui % 10, ui < 60 ? ui / 10 : USPLIT + ui / 10 - 6, ui < 60 ? 6 : 5, ui < 60 ? USPLIT : NSEG, wave, lane, pol);
  }
}

__global__ void __launch_bounds__(256) k_pro(Args a) {
  const int b = blockIdx.x, tid = threadIdx.x, lane = tid & 63, wave = __builtin_amdgcn_readfirstlane(tid >> 6);
  if (b < 32) {
    if ((b & 7) == 0 && wave == 0) {
      const int j = b >> 3;
      const rsrc_t rs = __builtin_amdgcn_make_buffer_rsrc(a.ws, 0, WS_BYTES, 0x00020000);
      const unsigned base = OFF_COMM + (j >> 1) * 2 * MB + 48 * 16384 + (j & 1) * 8192;
      st128(rs, (u32x4){1, 2, 3, 4}, lane * 16, base);
      VMWAIT(0);
      unsigned best = ~0u;
#pragma unroll
      for (int r = 0; r < 3; ++r) {
        unsigned long long c0, c1; unsigned v;
        asm volatile("s_memtime %0\n\ts_waitcnt lgkmcnt(0)" : "=s"(c0) :: "memory");
        v = ld32(rs, r * 256, base);
        asm volatile("s_waitcnt vmcnt(0)\n\ts_memtime %0\n\ts_waitcnt lgkmcnt(0)" : "=s"(c1) : "v"(v) : "memory");
        best = min(best, (unsigned)(c1 - c0));
      }
      if (lane == 0) { *(unsigned*)(a.ws + OFF_CTL + CTL_POL + 4 * j) = best; if (j == 0) *(unsigned*)(a.ws + OFF_CTL + CTL_POL + 16) = xcc_id(); }
    }
  } else if (b < 160) {
#pragma unroll
    for (int i = 0; i < 2; ++i) {
      const unsigned c = ((b - 32) * 2 + i) * 256 + tid;
      *(u32x4*)(a.ws + OFF_COMM + (c >> 14) * MB + (c & 16383) * 16) = (u32x4){SENT, SENT, SENT, SENT};
    }
  } else {
#pragma unroll
    for (int k = 0; k < 4; ++k) {
      const unsigned i = ((b - 160) * 4 + k) * 256 + tid;
      if (i < 12800) { if ((i >> 3) * 128 != CTL_POL) *(u32x4*)(a.ws + OFF_CTL + i * 16) = (u32x4){0, 0, 0, 0}; }
      else if (i < 12800 + 65 * 1024) *(u32x4*)(a.ws + OFF_REP + (i - 12800) * 16) = (u32x4){0, 0, 0, 0};
    }
  }
}

extern "C" void kernel_launch(void* const* d_in, const int* in_sizes, int n_in, void* d_out, int out_size, void* d_ws, size_t ws_size, hipStream_t stream) {
  Args a;
  a.x = (const unsigned char*)d_in[0]; a.mem0 = (const unsigned char*)d_in[1]; a.wq = (const unsigned char*)d_in[2]; a.wk = (const unsigned char*)d_in[3];
  a.wv = (const unsigned char*)d_in[4]; a.wo = (const unsigned char*)d_in[5]; a.w1 = (const unsigned char*)d_in[8]; a.w3 = (const unsigned char*)d_in[9];
  a.w2 = (const unsigned char*)d_in[10]; a.out = (unsigned char*)d_out; a.ws = (unsigned char*)d_ws;
  (void)hipFuncSetAttribute((const void*)k_main, hipFuncAttributeMaxDynamicSharedMemorySize, 163840);
  k_pro<<<240, 256, 0, stream>>>(a);
  k_main<<<256, 256, 163840, stream>>>(a);
}
```

```cpp
#include <hip/hip_runtime.h>
#include <stdint.h>

typedef __bf16 bf16x8 __attribute__((ext_vector_type(8)));
typedef __bf16 bf16x16 __attribute__((ext_vector_type(16)));
typedef __bf16 bf16x2 __attribute__((ext_vector_type(2)));
typedef float f32x2 __attribute__((ext_vector_type(2)));
typedef float f32x4 __attribute__((ext_vector_type(4)));
typedef unsigned u32x2 __attribute__((ext_vector_type(2)));
typedef unsigned u32x4 __attribute__((ext_vector_type(4)));
typedef unsigned u32x8 __attribute__((ext_vector_type(8)));
typedef short s16x4 __attribute__((ext_vector_type(4)));
#define LAS __attribute__((address_space(3)))
#define DEV __device__ __forceinline__

constexpr int NSEG = 64;
constexpr unsigned MB = 1u << 20;
constexpr unsigned OFF_COMM = 0;
constexpr unsigned OFF_CTL = 4 * MB;
constexpr unsigned OFF_XN = 5 * MB;
constexpr unsigned OFF_BLK = 32 * MB;
constexpr unsigned OFF_DBG = 28 * MB;
constexpr unsigned WS_BYTES = 256 * MB;
constexpr unsigned OOB = 0x80000000u;
constexpr unsigned C_MEM1 = 16384, C_PM = 16384 + 2048, C_PX = 2 * 16384, C_Z = 4 * 16384;
constexpr unsigned CTL_POL = 65536;
constexpr unsigned CTL_RL = 65536 + 4096;
constexpr unsigned CTL_HP = 131072;
constexpr unsigned SENT = 0xFFFFFFFFu;
constexpr int SPIN_CAP = 1 << 20;
constexpr int SC1 = 16;

typedef __amdgpu_buffer_rsrc_t rsrc_t;
DEV u32x4 ld128(rsrc_t r, unsigned vo, unsigned so) { return __builtin_amdgcn_raw_buffer_load_b128(r, vo, so, SC1); }
DEV u32x2 ld64(rsrc_t r, unsigned vo, unsigned so) { return __builtin_amdgcn_raw_buffer_load_b64(r, vo, so, SC1); }
DEV unsigned ld32(rsrc_t r, unsigned vo, unsigned so) { return __builtin_amdgcn_raw_buffer_load_b32(r, vo, so, SC1); }
DEV void st128(rsrc_t r, u32x4 v, unsigned vo, unsigned so) { __builtin_amdgcn_raw_buffer_store_b128(v, r, vo, so, SC1); }
#define BST_AUX 16
#define BLD_AUX 16
#define KST_AUX 16
template <int AUX> DEV void st128a(rsrc_t r, u32x4 v, unsigned vo, unsigned so) { __builtin_amdgcn_raw_buffer_store_b128(v, r, vo, so, AUX); }
template <int AUX> DEV u32x4 ld128a(rsrc_t r, unsigned vo, unsigned so) { return __builtin_amdgcn_raw_buffer_load_b128(r, vo, so, AUX); }
DEV void st64(rsrc_t r, u32x2 v, unsigned vo, unsigned so) { __builtin_amdgcn_raw_buffer_store_b64(v, r, vo, so, SC1); }
DEV void st32(rsrc_t r, unsigned v, unsigned vo, unsigned so) { __builtin_amdgcn_raw_buffer_store_b32(v, r, vo, so, SC1); }
DEV unsigned pk(float a, float b) { bf16x2 v = {(__bf16)a, (__bf16)b}; return __builtin_bit_cast(unsigned, v); }
DEV unsigned pks(float a, float b) { return min(pk(a, b), 0xFFFFFFFEu); }
DEV unsigned mx4(u32x4 v) { return max(max(v.x, v.y), max(v.z, v.w)); }
DEV f32x4 mfma(u32x4 a, u32x4 b, f32x4 c) { return __builtin_amdgcn_mfma_f32_16x16x32_bf16(__builtin_bit_cast(bf16x8, a), __builtin_bit_cast(bf16x8, b), c, 0, 0, 0); }
DEV f32x4 smf(u32x4 a, u32x8 b, f32x4 c, int idx) { return __builtin_amdgcn_smfmac_f32_16x16x64_bf16(__builtin_bit_cast(bf16x8, a), __builtin_bit_cast(bf16x16, b), c, idx, 0, 0); }
DEV u32x4 lo4(u32x8 v) { return __builtin_shufflevector(v, v, 0, 1, 2, 3); }
DEV u32x4 hi4(u32x8 v) { return __builtin_shufflevector(v, v, 4, 5, 6, 7); }
DEV u32x8 cat8(u32x4 a, u32x4 b) { return __builtin_shufflevector(a, b, 0, 1, 2, 3, 4, 5, 6, 7); }
DEV u32x4 cat4(u32x2 a, u32x2 b) { return __builtin_shufflevector(a, b, 0, 1, 2, 3); }
DEV u32x2 trread(unsigned lds_addr) { return __builtin_bit_cast(u32x2, __builtin_amdgcn_ds_read_tr16_b64_v4i16((LAS s16x4*)(uintptr_t)lds_addr)); }
DEV u32x4 ldsr128(unsigned a) { return *(LAS u32x4*)(uintptr_t)a; }
DEV void ldsw128(unsigned a, u32x4 v) { *(LAS u32x4*)(uintptr_t)a = v; }
DEV void ldsw64(unsigned a, u32x2 v) { *(LAS u32x2*)(uintptr_t)a = v; }
#define BAR() asm volatile("s_waitcnt lgkmcnt(0)\n\ts_barrier" ::: "memory")
#define VMWAIT(n) asm volatile("s_waitcnt vmcnt(" #n ")" ::: "memory")
#define LGWAIT() asm volatile("s_waitcnt lgkmcnt(0)" ::: "memory")
#define CFENCE() asm volatile("" ::: "memory")
template <int N> DEV void vmwait() { asm volatile("s_waitcnt vmcnt(%0)" :: "n"(N) : "memory"); }
DEV void glds(const void* sbase, unsigned voff, unsigned ldst) {
  unsigned keep;
  asm volatile("s_mov_b32 %0, m0\n\ts_mov_b32 m0, %3\n\ts_nop 0\n\tglobal_load_lds_dwordx4 %1, %2\n\ts_mov_b32 m0, %0" : "=&s"(keep) : "v"(voff), "s"(sbase), "s"(ldst) : "memory");
}
constexpr unsigned CTL_XP = 196608;
DEV void glds_sc1(const void* sbase, unsigned voff, unsigned ldst) {
  unsigned keep;
  asm volatile("s_mov_b32 %0, m0\n\ts_mov_b32 m0, %3\n\ts_nop 0\n\tglobal_load_lds_dwordx4 %1, %2 sc1\n\ts_mov_b32 m0, %0" : "=&s"(keep) : "v"(voff), "s"(sbase), "s"(ldst) : "memory");
}
DEV void set_m0(unsigned v) { asm volatile("s_mov_b32 m0, %0" :: "s"(v) : "memory"); }
template <int OFF> DEV void glds_off(const void* sbase, unsigned voff) { asm volatile("global_load_lds_dwordx4 %0, %1 offset:%2 sc1" :: "v"(voff), "s"(sbase), "n"(OFF) : "memory"); }
template <int V> struct IC { static constexpr int value = V; constexpr operator int() const { return V; } };
template <int N, int I = 0, class F> DEV void static_for(F&& f) { if constexpr (I < N) { f(IC<I>{}); static_for<N, I + 1>(f); } }
DEV float fold32(float a, float b) {
  auto r = __builtin_amdgcn_permlane32_swap(__builtin_bit_cast(unsigned, a), __builtin_bit_cast(unsigned, b), false, false);
  return __builtin_bit_cast(float, (unsigned)r[0]) + __builtin_bit_cast(float, (unsigned)r[1]);
}
DEV float xmax16(float v) { auto r = __builtin_amdgcn_permlane16_swap(__builtin_bit_cast(unsigned, v), __builtin_bit_cast(unsigned, v), false, false); return fmaxf(__builtin_bit_cast(float, (unsigned)r[0]), __builtin_bit_cast(float, (unsigned)r[1])); }
DEV float xmax32(float v) { auto r = __builtin_amdgcn_permlane32_swap(__builtin_bit_cast(unsigned, v), __builtin_bit_cast(unsigned, v), false, false); return fmaxf(__builtin_bit_cast(float, (unsigned)r[0]), __builtin_bit_cast(float, (unsigned)r[1])); }
DEV float xadd16(float v) { auto r = __builtin_amdgcn_permlane16_swap(__builtin_bit_cast(unsigned, v), __builtin_bit_cast(unsigned, v), false, false); return __builtin_bit_cast(float, (unsigned)r[0]) + __builtin_bit_cast(float, (unsigned)r[1]); }
DEV float xadd32(float v) { auto r = __builtin_amdgcn_permlane32_swap(__builtin_bit_cast(unsigned, v), __builtin_bit_cast(unsigned, v), false, false); return __builtin_bit_cast(float, (unsigned)r[0]) + __builtin_bit_cast(float, (unsigned)r[1]); }
DEV float bf2f(unsigned short u) { return __builtin_bit_cast(float, (unsigned)u << 16); }
DEV float rbf(float x) { return (float)(__bf16)x; }

DEV void smf_a(f32x4& c, u32x4 a, const u32x8& w, int idx) { asm volatile("v_smfmac_f32_16x16x64_bf16 %0, %1, %2, %3" : "+v"(c) : "v"(a), "a"(w), "v"(idx)); }
DEV void smf_v(f32x4& c, u32x4 a, const u32x8& w, int idx) { asm volatile("v_smfmac_f32_16x16x64_bf16 %0, %1, %2, %3" : "+v"(c) : "v"(a), "v"(w), "v"(idx)); }
template <int I> DEV void smf_w(f32x4& c, u32x4 a, const u32x8 (&w)[40], int idx) { if constexpr (I < 32) smf_a(c, a, w[I], idx); else smf_v(c, a, w[I], idx); }
DEV void mf_va0(f32x4& c, u32x4 a, u32x4 b) { asm volatile("v_mfma_f32_16x16x32_bf16 %0, %1, %2, 0" : "=&v"(c) : "v"(a), "a"(b)); }
DEV void mf_vv0(f32x4& c, u32x4 a, u32x4 b) { asm volatile("v_mfma_f32_16x16x32_bf16 %0, %1, %2, 0" : "=&v"(c) : "v"(a), "v"(b)); }
DEV void mf_va(f32x4& c, u32x4 a, u32x4 b) { asm volatile("v_mfma_f32_16x16x32_bf16 %0, %1, %2, %0" : "+v"(c) : "v"(a), "a"(b)); }
DEV void mf_vv(f32x4& c, u32x4 a, u32x4 b) { asm volatile("v_mfma_f32_16x16x32_bf16 %0, %1, %2, %0" : "+v"(c) : "v"(a), "v"(b)); }
template <bool A> DEV void pin_w(u32x8& x, u32x8& y) {
  if constexpr (A) asm volatile("s_waitcnt lgkmcnt(0)" : "+a"(x), "+a"(y) :: "memory"); else asm volatile("s_waitcnt lgkmcnt(0)" : "+v"(x), "+v"(y) :: "memory");
}
template <bool A> DEV void pin_w4(u32x4& x, u32x4& y, u32x4& z, u32x4& u) {
  if constexpr (A) asm volatile("s_waitcnt lgkmcnt(0)" : "+a"(x), "+a"(y), "+a"(z), "+a"(u) :: "memory"); else asm volatile("s_waitcnt lgkmcnt(0)" : "+v"(x), "+v"(y), "+v"(z), "+v"(u) :: "memory");
}
DEV void pin2(f32x4& x, f32x4& y) { asm volatile("" : "+v"(x), "+v"(y)); }
DEV void pin2n(f32x4& x, f32x4& y) { asm volatile("s_nop 3" : "+v"(x), "+v"(y)); }
DEV unsigned pin1(unsigned x) { asm volatile("" :: "v"(x)); return x; }
#define NOP(n) asm volatile("s_nop " #n)
#define PIN(x) asm volatile("" : "+v"(x))
#define PINS(x) asm volatile("" : "+s"(x))
DEV unsigned xcc_id() { return (unsigned)__builtin_amdgcn_s_getreg(20 | (3 << 11)) & 0xfu; }
DEV unsigned rtc() { unsigned long long t; asm volatile("s_memrealtime %0\n\ts_waitcnt lgkmcnt(0)" : "=s"(t) :: "memory"); return (unsigned)t; }
constexpr unsigned CTL_CUR = 65536 + 12288 + 1024;
constexpr unsigned OFF_REP = 8 * MB;
constexpr int MAXWAIT = 400 * 8;
#define D1_INIT 840
#define D2_INIT 510
struct Sched { int j0, jbad, rx; unsigned base; int T, D1; unsigned c0, v0; int R; unsigned ct, tt; };
DEV unsigned now8() { return rtc() << 3; }
DEV unsigned cycles() { unsigned long long t; asm volatile("s_memtime %0\n\ts_waitcnt lgkmcnt(0)" : "=s"(t) :: "memory"); return (unsigned)t; }
DEV void clk_init(Sched& c) { c.j0 = 0; c.jbad = 0; c.rx = 0; c.base = 0; c.T = D1_INIT + D2_INIT; c.D1 = D1_INIT; c.v0 = rtc(); c.c0 = cycles(); c.R = 24 * 256; c.ct = c.c0; c.tt = 0; }
DEV void clk_update(Sched& c) {
  const unsigned v = rtc() - c.v0, y = cycles() - c.c0;
  if (v > 300) c.R = __builtin_amdgcn_readfirstlane((int)((float)y * 256.f * __builtin_amdgcn_rcpf((float)v)));
}
#define RELAX 120
#define NRELAX 2
DEV unsigned tau_of(const Sched& c, int j) { return c.base + (j >> 1) * c.T + ((j & 1) ? c.D1 : 0) + RELAX * min(max(j - c.rx, 0), NRELAX); }
DEV void adopt_cur(Sched& c, u32x4 v, int j) {
  const int j0 = __builtin_amdgcn_readfirstlane(v.x);
  if (j0 > c.j0 && j0 <= j) { c.j0 = j0; c.base = __builtin_amdgcn_readfirstlane(v.y); c.T = __builtin_amdgcn_readfirstlane(v.z); const int w = __builtin_amdgcn_readfirstlane(v.w); c.D1 = w & 0xffff; c.rx = w >> 16; }
}
DEV bool wait_until(unsigned tau) {
  int d = (int)(tau - now8());
  if (d <= 0 || d > MAXWAIT) return false;
  while ((int)(tau - now8()) > 0) {}
  return true;
}
#define NOP16 "s_nop 0\n\ts_nop 0\n\ts_nop 0\n\ts_nop 0\n\ts_nop 0\n\ts_nop 0\n\ts_nop 0\n\ts_nop 0\n\ts_nop 0\n\ts_nop 0\n\ts_nop 0\n\ts_nop 0\n\ts_nop 0\n\ts_nop 0\n\ts_nop 0\n\ts_nop 0\n\t"
#define FK 96
DEV unsigned est_cycle(unsigned tau, int R, unsigned c0, int best) {
  unsigned long long v, c; unsigned t; int rem;
  asm volatile(
    "1:\n\t"
    "s_memrealtime %0\n\ts_memtime %1\n\ts_waitcnt lgkmcnt(0)\n\t"
    "s_lshl_b32 %2, s94, 3\n\t"
    "s_sub_i32 %2, %5, %2\n\t"
    "s_mul_i32 %2, %2, %6\n\t"
    "s_ashr_i32 %2, %2, 11\n\t"
    "s_sub_i32 %3, s96, %7\n\t"
    "s_add_i32 %2, %2, %3\n\t"
    "s_min_i32 %4, %4, %2\n\t"
    "s_sub_i32 %2, %2, 100\n\t"
    "s_max_i32 %4, %4, %2\n\t"
    "s_sub_i32 %3, %4, %3\n\t"
    "s_cmp_gt_i32 %3, 520\n\t"
    "s_cbranch_scc1 1b"
    : "=&{s[94:95]}"(v), "=&{s[96:97]}"(c), "=&s"(t), "=&s"(rem), "+s"(best)
    : "s"(tau), "s"(R), "s"(c0) : "memory", "scc");
  return c0 + best;
}
DEV void release(unsigned cs) {
  unsigned long long c, pc; int rem;
  asm volatile(
    "1:\n\t"
    "s_memtime %0\n\ts_waitcnt lgkmcnt(0)\n\t"
    "s_sub_i32 %2, %3, s96\n\t"
    "s_cmp_gt_i32 %2, %4\n\t"
    "s_cbranch_scc1 1b\n\t"
    "s_sub_i32 %2, %2, %5\n\t"
    "s_max_i32 %2, %2, 0\n\t"
    "s_and_b32 %2, %2, -4\n\t"
    "s_sub_i32 %2, 140, %2\n\t"
    "s_getpc_b64 %1\n\t"
    "s_add_u32 s98, s98, %2\n\t"
    "s_addc_u32 s99, s99, 0\n\t"
    "s_setpc_b64 %1\n\t"
    NOP16 NOP16 "s_barrier"
    : "=&{s[96:97]}"(c), "=&{s[98:99]}"(pc), "=&s"(rem)
    : "s"(cs), "n"(FK + 124), "n"(FK) : "memory", "scc");
}
#define DRIFT 10
#define CSLEEP 3
#define LATE_THR 120
constexpr unsigned L_TM = 61440;
DEV void mark_overrun(Sched& c, int j, unsigned t) { if ((int)(t - tau_of(c, j + 1)) > LATE_THR + 40) c.jbad = c.j0; }
template <class F>
DEV unsigned trigger(rsrc_t rs, Sched& c, int j, unsigned hold, F&& poll) {
  unsigned cs, timed = 0;
  for (;;) {
    const unsigned tau = tau_of(c, j), now = now8();
    const int d = (int)(tau - now);
    const bool ok = c.j0 > c.jbad && d > -30 * 8 && d <= MAXWAIT;
    if (ok && d <= 0) { cs = cycles(); break; }
    if (ok && d <= 50 * 8) {
      const unsigned dt = tau - c.tt;
      const int prior = dt < 4096 ? (int)((dt * (unsigned)c.R) >> 11) + DRIFT : 0x7fffffff;
      cs = est_cycle(__builtin_amdgcn_readfirstlane(tau), __builtin_amdgcn_readfirstlane(c.R), __builtin_amdgcn_readfirstlane(c.ct), __builtin_amdgcn_readfirstlane(prior));
      c.ct = cs; c.tt = tau; timed = 1;
      break;
    }
    if (!ok && (unsigned)(hold - now) > (unsigned)MAXWAIT) { poll(); cs = cycles(); break; }
    if (d > 100 * 8 || !ok) { adopt_cur(c, ld128(rs, 0, OFF_CTL + CTL_CUR), j); CFENCE(); __builtin_amdgcn_s_sleep(CSLEEP); }
  }
  ldsw64(L_TM + (j & 2) * 8, (u32x2){cs, timed | (unsigned)c.R << 2});
  return __builtin_amdgcn_readfirstlane(cs);
}
#define STAMP(k)
#define STV(k) stdummy
#define STAMP_DECL unsigned stdummy
#define STAMP_OUT(e, s)

#define BMODE 0
#define KQMODE 0
constexpr unsigned BSTR = BMODE == 2 ? 8192 : 16384;
constexpr int ZBLK0 = BMODE == 0 ? 0 : BMODE == 1 ? 10240 : 5376;
DEV unsigned blk_addr(int blk, unsigned pol, unsigned far) { return OFF_BLK + blk * 16384 + (((pol >> ((blk >> 7) & 1)) ^ far) & 1) * 8192; }
DEV unsigned z_base(int s, unsigned pol) { const int pg = s / 3; return blk_addr(ZBLK0 + pg * 128 + (s - 3 * pg) * 40, pol, 0); }
DEV unsigned kq_addr(int isq, int seg, int h, unsigned pol) {
  if (KQMODE == 2) return OFF_BLK + (ZBLK0 + 3072) * 16384 + (isq * 2048 + seg * 32 + h) * 8192;
  return BMODE == 1 ? blk_addr(isq * 2048 + seg * 32 + h, pol, 1) : blk_addr(ZBLK0 + 3072 + isq * 2048 + seg * 32 + h, pol, 0);
}
DEV unsigned b_addr(int seg, int cb, unsigned pol) { return BMODE == 2 ? OFF_BLK + (seg * 40 + cb) * 32768 : blk_addr((seg * 40 + cb) * 4, pol, 1 - BMODE); }

#define HFAR 1
DEV constexpr unsigned spread(unsigned o) { return (o >> 13) * 16384 + (o & 8191); }
DEV unsigned h_base(int seg, unsigned pol) { return blk_addr(10240 + seg * 32, pol, HFAR); }
DEV unsigned pt_base(int seg, unsigned pol) { return blk_addr(10240 + seg * 32 + 16, pol, HFAR); }
#define EV(slot)
struct Args {
  const unsigned char* x; const unsigned char* mem0; const unsigned char* wq; const unsigned char* wk; const unsigned char* wv; const unsigned char* wo;
  const unsigned char* w1; const unsigned char* w3; const unsigned char* w2; unsigned char* out; unsigned char* ws;
};

constexpr unsigned CTL_EP = CTL_XP + 8192 + 512, CTL_HINT = CTL_EP + 128;
struct Boot { unsigned t0, t1, xoff, pol; };
DEV bool boot_hint(rsrc_t rs, Boot& bt, bool block) {
  for (int budget = SPIN_CAP; budget > 0; --budget) {
    const u32x4 v = ld128(rs, 0, OFF_CTL + CTL_HINT);
    CFENCE();
    if (__builtin_amdgcn_readfirstlane(v.x) == bt.t0 && __builtin_amdgcn_readfirstlane(v.y) == bt.t1) {
      const unsigned x = __builtin_amdgcn_readfirstlane(v.z) & 7;
      const bool same = x == bt.xoff;
      bt.xoff = x;
      return same;
    }
    if (!block) break;
    __builtin_amdgcn_s_sleep(2);
  }
  return true;
}
DEV bool boot_wait(rsrc_t rs, Boot& bt, int lane) {
  if (!boot_hint(rs, bt, true)) return false;
  u32x4 v;
  for (int budget = SPIN_CAP; budget > 0; --budget) {
    v = ld128(rs, lane * 128, OFF_CTL + CTL_XP);
    CFENCE();
    if (__all(v.x == bt.t0 && v.y == bt.t1)) break;
    __builtin_amdgcn_s_sleep(4);
  }
  const int d0 = __builtin_amdgcn_readlane(v.z, 60) + __builtin_amdgcn_readlane(v.z, 61), d1 = __builtin_amdgcn_readlane(v.z, 62) + __builtin_amdgcn_readlane(v.z, 63);
  bt.pol = d0 > d1 ? 1u : 2u;
  return true;
}
DEV int boot_probe(rsrc_t rs, int j, int lane) {
  const unsigned base = OFF_COMM + (j >> 1) * 2 * MB + (48 + (j & 1)) * 16384;
  st128(rs, (u32x4){1, 2, 3, 4}, (lane >> 5) * 8192 + (lane & 31) * 16, base);
  VMWAIT(0);
  unsigned best[2] = {~0u, ~0u};
#pragma unroll
  for (int r = 0; r < 6; ++r) {
    unsigned long long c0, c1; unsigned v;
    asm volatile("s_memtime %0\n\ts_waitcnt lgkmcnt(0)" : "=s"(c0) :: "memory");
    v = ld32(rs, (r & 1) * 8192 + (r >> 1) * 128, base);
    asm volatile("s_waitcnt vmcnt(0)\n\ts_memtime %0\n\ts_waitcnt lgkmcnt(0)" : "=s"(c1) : "v"(v) : "memory");
    best[r & 1] = min(best[r & 1], (unsigned)(c1 - c0));
  }
  return min(max((int)best[0] - (int)best[1], -300), 300);
}
DEV void boot_init(rsrc_t rs, int k, int wave, int lane) {
#pragma unroll
  for (int i = 0; i < 10; ++i) {
    const unsigned p = (k * 4 + wave) + i * 240;
    const unsigned so = p < 1024 ? OFF_COMM + (p >> 8) * MB + (p & 255) * 1024 : p < 1216 ? OFF_CTL + (p - 1024) * 1024 : OFF_REP + (p - 1216) * 1024;
    const unsigned f = p < 1024 ? SENT : 0u;
    st128(rs, (u32x4){f, f, f, f}, p < 2256 ? lane * 16 : OOB, so);
  }
}

DEV float rowsum16(float v) {
  v += __builtin_bit_cast(float, __builtin_amdgcn_update_dpp(0, __builtin_bit_cast(int, v), 0xB1, 0xf, 0xf, false));
  v += __builtin_bit_cast(float, __builtin_amdgcn_update_dpp(0, __builtin_bit_cast(int, v), 0x4E, 0xf, 0xf, false));
  v += __builtin_bit_cast(float, __builtin_amdgcn_update_dpp(0, __builtin_bit_cast(int, v), 0x124, 0xf, 0xf, false));
  v += __builtin_bit_cast(float, __builtin_amdgcn_update_dpp(0, __builtin_bit_cast(int, v), 0x128, 0xf, 0xf, false));
  return v;
}
DEV void ln_load(const Args& a, u32x4 (&v)[2][5], int seg, int wave, int lane) {
#pragma unroll
  for (int p = 0; p < 2; ++p)
#pragma unroll
    for (int i = 0; i < 5; ++i) v[p][i] = *(const u32x4*)(a.x + (size_t)(seg * 32 + wave * 8 + p * 4 + (lane >> 4)) * 1280 + ((lane & 15) + 16 * i) * 16);
}
DEV void ln_compute(const u32x4 (&v)[2][5], unsigned dst, int wave, int lane) {
  const int sub = lane & 15;
#pragma unroll
  for (int p = 0; p < 2; ++p) {
    float f[40], sum = 0.f;
#pragma unroll
    for (int i = 0; i < 5; ++i)
#pragma unroll
      for (int j = 0; j < 4; ++j) { f[i * 8 + 2 * j] = __builtin_bit_cast(float, v[p][i][j] << 16); f[i * 8 + 2 * j + 1] = __builtin_bit_cast(float, v[p][i][j] & 0xFFFF0000u); sum += f[i * 8 + 2 * j] + f[i * 8 + 2 * j + 1]; }
    const float mean = rowsum16(sum) * (1.f / 640.f);
    float sq = 0.f;
#pragma unroll
    for (int i = 0; i < 40; ++i) { const float d = f[i] - mean; sq = fmaf(d, d, sq); }
    const float mb = rbf(mean), vb = rbf(rowsum16(sq) * (1.f / 640.f));
    const float inv = 1.f / rbf(sqrtf(rbf(vb + 1e-5f)));
    const int row = wave * 8 + p * 4 + (lane >> 4);
#pragma unroll
    for (int i = 0; i < 5; ++i) {
      u32x4 o;
#pragma unroll
      for (int j = 0; j < 4; ++j) o[j] = pk(rbf(f[i * 8 + 2 * j] - mb) * inv, rbf(f[i * 8 + 2 * j + 1] - mb) * inv);
      const int c = sub + 16 * i;
      ldsw128(dst + ((c >> 2) * 2 + (row >> 4)) * 1024 + ((c & 3) * 16 + (row & 15)) * 16, o);
    }
  }
}
DEV void ln_publish(rsrc_t rs, unsigned src, int seg, int wave, int lane, const Boot& bt) {
  BAR();
#pragma unroll
  for (int i = 0; i < 10; ++i) st128(rs, ldsr128(src + (wave * 10 + i) * 1024 + lane * 16), lane * 16, OFF_XN + seg * 40960 + (wave * 10 + i) * 1024);
  VMWAIT(0);
  BAR();
  if (wave == 0) st128(rs, (u32x4){bt.t0, bt.t1, 0, 0}, lane < 8 ? (seg < 60 ? seg : seg + 4) * 128 + lane * 16 : OOB, OFF_CTL + CTL_XP);
}

enum { K_P, K_V, K_QK, K_VB, K_F1 };
constexpr unsigned L_XB0 = 120 * 1024, L_XB1 = 80 * 1024;
template <int KIND, class WT, class LATE>
DEV void load_weights(WT& w, const unsigned char* baseA, const unsigned char* baseB, int wave, int lane, int cmask, LATE&& late) {
  constexpr int NG = 20;
  constexpr bool WO = KIND == K_V || KIND == K_VB;
  constexpr unsigned SA = KIND == K_F1 ? 3456 : 8192, SB = WO ? 1280 : SA;
  constexpr int MA = (KIND == K_P || KIND == K_V) ? 2 : 0, MBB = KIND == K_P ? 2 : KIND == K_VB ? 1 : 0;
  const int gB = lane >> 4, q = (lane >> 2) & 3, pp = lane & 3;
  unsigned vo[2][4], rd[2][2];
#pragma unroll
  for (int m = 0; m < 2; ++m) {
    const int mode = m ? MBB : MA;
#pragma unroll
    for (int p = 0; p < 4; ++p) {
      const int key = mode == 0 ? (gB | (p >> 1) << 2) : mode == 1 ? (gB | (p & 1) << 2) : ((gB >> 1) | p << 1);
      vo[m][p] = (wave * 16 + p * 4 + gB) * (m ? SB : SA) + (((lane & 15) ^ (key << 1)) & cmask) * 16;
    }
    const int key = mode == 2 ? ((gB & 1) | (q >> 1) << 1 | (q & 1) << 2) : (q | (gB & 1) << 2);
    const int row = mode == 0 ? 8 * gB + q : mode == 1 ? 4 * gB + q : 16 * (gB >> 1) + 4 * (q >> 1) + 2 * (gB & 1) + 8 * (q & 1);
#pragma unroll
    for (int nt = 0; nt < 2; ++nt) rd[m][nt] = row * 256 + ((4 * wave + 2 * nt + (pp >> 1)) ^ (key << 1)) * 16 + (pp & 1) * 8;
  }
  auto issue = [&](int g) {
    const unsigned char* src = g < 10 ? baseA + (size_t)g * 64 * SA : WO ? baseB + (size_t)((g - 10) & 1) * 64 * SB + ((g - 10) >> 1) * 256 : baseB + (size_t)(g - 10) * 64 * SB;
#pragma unroll
    for (int p = 0; p < 4; ++p) glds(src, vo[g >= 10][p], (g % 7) * 16384 + wave * 4096 + p * 1024);
  };
#pragma unroll
  for (int g = 0; g < 7; ++g) issue(g);
  static_for<NG>([&](auto G) {
    constexpr int g = G, mode = g < 10 ? MA : MBB;
    if constexpr (g == 0) vmwait<24>(); else if constexpr (g < NG - 5) vmwait<20>(); else if constexpr (g == NG - 5) late();
    if constexpr (g <= NG - 5) BAR();
    if constexpr (g >= 1 && g + 6 < NG) issue(g + 6);
    constexpr unsigned phs = mode == 2 ? 256 : mode == 1 ? 16 * 256 : 4 * 256, sl = (g % 7) * 16384;
#pragma unroll
    for (int nt = 0; nt < 2; ++nt) {
      const unsigned rb = rd[g >= 10][nt] + sl;
      u32x2 a = trread(rb), b = trread(rb + phs), c = trread(rb + 8192), d = trread(rb + 8192 + phs);
      if constexpr (KIND == K_P || KIND == K_V) w[g * 2 + nt] = cat8(cat4(a, b), cat4(c, d));
      else { w[g * 4 + nt * 2] = cat4(a, b); w[g * 4 + nt * 2 + 1] = cat4(c, d); }
    }
    if constexpr (KIND == K_P || KIND == K_V) pin_w<g < 16>(w[g * 2], w[g * 2 + 1]);
    else pin_w4<g < 16>(w[g * 4], w[g * 4 + 1], w[g * 4 + 2], w[g * 4 + 3]);
  });
}
DEV void load_w2(u32x4 (&w)[56], const unsigned char* base, int wave, int lane) {
  const int gB = lane >> 4, q = (lane >> 2) & 3, pp = lane & 3;
  const unsigned ring = wave * 32768;
  const unsigned vo = (lane >> 3) * 1280 + ((lane & 7) ^ (((lane >> 4) & 3) << 1)) * 16;
  unsigned rd[4];
#pragma unroll
  for (int c = 0; c < 4; ++c) rd[c] = ring + (4 * gB + q) * 128 + ((2 * c + (pp >> 1)) ^ ((((gB & 1) << 1) | (q >> 1)) << 1)) * 16 + (pp & 1) * 8;
  auto issue = [&](int g) {
    const unsigned char* src = base + (size_t)min(wave * 7 + g, 26) * 64 * 1280;
#pragma unroll
    for (int p = 0; p < 8; ++p) glds(src + p * 8 * 1280, vo, ring + (g & 3) * 8192 + p * 1024);
  };
#pragma unroll
  for (int g = 0; g < 4; ++g) issue(g);
  static_for<7>([&](auto G) {
    constexpr int g = G;
    vmwait<8 * (6 - g < 3 ? 6 - g : 3)>();
#pragma unroll
    for (int c = 0; c < 4; ++c) {
      const unsigned rb = rd[c] + (g & 3) * 8192;
      w[g * 8 + c * 2] = cat4(trread(rb), trread(rb + 2048)); w[g * 8 + c * 2 + 1] = cat4(trread(rb + 4096), trread(rb + 6144));
    }
    pin_w4<true>(w[g * 8], w[g * 8 + 1], w[g * 8 + 2], w[g * 8 + 3]); pin_w4<true>(w[g * 8 + 4], w[g * 8 + 5], w[g * 8 + 6], w[g * 8 + 7]);
    if constexpr (g + 4 < 7) issue(g + 4);
  });
}

#define QKN 8
DEV int first_open(int type, int par, int c) { return (type == 1 || c < QKN) ? 2 * c + par : par ? NSEG : c + QKN; }
DEV unsigned prog_off(int type, int h, int par, int wave) { return OFF_CTL + (((type * 32 + h) * 2 + par) * 4 + wave) * 128; }
DEV unsigned hprog_off(int rep, int slice) { return OFF_CTL + CTL_HP + (rep * 56 + slice) * 128; }
DEV float silu(float x) { return x * __builtin_amdgcn_rcpf(1.f + __builtin_amdgcn_exp2f(-1.4426950408889634f * x)); }
DEV u32x2 pk4(f32x4 v) { return (u32x2){pk(v[0], v[1]), pk(v[2], v[3])}; }

template <int I, int HI, bool WA, bool FIRST, int N>
DEV void mf_w(f32x4& c, const u32x4 (&w)[N], u32x4 x) {
  const u32x4 f = w[2 * I + HI];
  if constexpr (FIRST) {
    if constexpr (I < 32) { if constexpr (WA) asm volatile("v_mfma_f32_16x16x32_bf16 %0, %1, %2, 0" : "=&v"(c) : "a"(f), "v"(x)); else asm volatile("v_mfma_f32_16x16x32_bf16 %0, %2, %1, 0" : "=&v"(c) : "a"(f), "v"(x)); }
    else { if constexpr (WA) asm volatile("v_mfma_f32_16x16x32_bf16 %0, %1, %2, 0" : "=&v"(c) : "v"(f), "v"(x)); else asm volatile("v_mfma_f32_16x16x32_bf16 %0, %2, %1, 0" : "=&v"(c) : "v"(f), "v"(x)); }
  } else {
    if constexpr (I < 32) { if constexpr (WA) asm volatile("v_mfma_f32_16x16x32_bf16 %0, %1, %2, %0" : "+v"(c) : "a"(f), "v"(x)); else asm volatile("v_mfma_f32_16x16x32_bf16 %0, %2, %1, %0" : "+v"(c) : "a"(f), "v"(x)); }
    else { if constexpr (WA) asm volatile("v_mfma_f32_16x16x32_bf16 %0, %1, %2, %0" : "+v"(c) : "v"(f), "v"(x)); else asm volatile("v_mfma_f32_16x16x32_bf16 %0, %2, %1, %0" : "+v"(c) : "v"(f), "v"(x)); }
  }
}

DEV void issue_x(const unsigned char* ws, int seg, unsigned buf, int wave, int lane) {
#pragma unroll
  for (int i = 0; i < 10; ++i) glds_sc1(ws + OFF_XN + (size_t)seg * 40960 + (wave * 10 + i) * 1024, lane * 16, buf + (wave * 10 + i) * 1024);
}
template <int NT, bool NEXT, class SIDE>
DEV void xproj(f32x4 (&acc)[NT][2], const u32x4 (&w)[80], u32x4 (&xr)[4][2], unsigned xa, unsigned xn, SIDE&& side) {
  static_for<20>([&](auto K) {
    constexpr int ks = K;
    static_for<2 * NT>([&](auto J) {
      constexpr int j = J, t = j >> 1, wi = ((t >> 1) * 10 + (ks >> 1)) * 2 + (t & 1);
      mf_w<wi, ks & 1, true, ks == 0>(acc[t][j & 1], w, xr[ks & 3][j & 1]);
      if constexpr (j == 1 || j == NT / 2 + 1) {
        constexpr int tt = j == 1 ? 0 : 1;
        if constexpr (ks < 18) { CFENCE(); xr[(ks + 2) & 3][tt] = ldsr128(xa + (ks + 2) * 2048 + tt * 1024); CFENCE(); }
        else if constexpr (NEXT) { CFENCE(); xr[ks - 18][tt] = ldsr128(xn + (ks - 18) * 2048 + tt * 1024); CFENCE(); }
      }
      side(IC<ks * 2 * NT + j>{});
    });
  });
}
template <int NT> DEV void acc_wait(f32x4 (&c)[NT][2]) {
  if constexpr (NT == 4) asm volatile("s_nop 7" : "+v"(c[0][0]), "+v"(c[0][1]), "+v"(c[1][0]), "+v"(c[1][1]), "+v"(c[2][0]), "+v"(c[2][1]), "+v"(c[3][0]), "+v"(c[3][1]));
  else asm volatile("s_nop 7" : "+v"(c[0][0]), "+v"(c[0][1]), "+v"(c[1][0]), "+v"(c[1][1]));
}
#define NFAST 2
DEV void pinf(float& x) { asm volatile("" : "+v"(x)); }
DEV void pinu(unsigned& x) { asm volatile("" : "+v"(x)); }

template <int KIND>
DEV bool role_x(const Args& a, rsrc_t rs, int id, int seg0, int sw, int lim, unsigned po, int wave, int lane, Boot& bt) {
  u32x4 w[80];
  bool agreed = true;
  auto late = [&]() {
    if constexpr (KIND != K_F1) agreed = boot_wait(rs, bt, lane);
    VMWAIT(0);
    issue_x(a.ws, seg0, L_XB0, wave, lane);
  };
  if constexpr (KIND == K_QK) load_weights<K_QK>(w, a.wq + id * 256, a.wk + id * 256, wave, lane, 15, late);
  else if constexpr (KIND == K_F1) load_weights<K_F1>(w, a.w1 + (id >> 2) * 256, a.w3 + (id >> 2) * 256, wave, lane, id >= 52 ? 7 : 15, late);
  else load_weights<K_VB>(w, a.wv + id * 256, a.wo + (size_t)id * 128 * 1280, wave, lane, 15, late);
  VMWAIT(0);
  BAR();
  if (!agreed) return false;
  const unsigned pol = bt.pol;
  EV(KIND == K_F1 ? 4 : 1);
  const unsigned l16 = lane * 16, fvo = lane < 32 ? po + lane * 4 : OOB;
  const unsigned svo = KIND == K_VB ? l16 + (id >> 3) * BSTR + (id & 7) * 1024 : (KIND == K_F1 && id > 53) ? OOB : l16;
  u32x4 xr[4][2];
#pragma unroll
  for (int k = 0; k < 2; ++k) { xr[k][0] = ldsr128(L_XB0 + l16 + k * 2048); xr[k][1] = ldsr128(L_XB0 + l16 + k * 2048 + 1024); }
  int it = 0, prev = seg0;
  unsigned pvo = OOB;
  u32x4 pk4b[4];
  f32x4 c[3][2];
  u32x4 vf[4][2], o;
#pragma unroll
  for (int i = 0; i < 4; ++i) pk4b[i] = (u32x4){0, 0, 0, 0};
#pragma unroll
  for (int i = 0; i < 3; ++i) { c[i][0] = (f32x4){0, 0, 0, 0}; c[i][1] = c[i][0]; }
  float e0, e1, g0, g1;
  auto out_prev = [&](auto N) {
    constexpr int n = N;
    if constexpr (KIND == K_QK) {
      if constexpr (n < 4) { CFENCE(); st128(rs, pk4b[n], pvo, kq_addr(1 - (n >> 1), prev, id, pol) + ((n & 1) * 4 + wave) * 1024); CFENCE(); }
    } else if constexpr (KIND == K_F1) {
      constexpr int p = n / 10, st = n % 10, tt = p >> 2, q = p & 3;
      if constexpr (p < 8) {
        if constexpr (st == 0) { g0 = __builtin_bit_cast(float, pk4b[tt][q] << 16); g1 = __builtin_bit_cast(float, pk4b[tt][q] & 0xFFFF0000u); pinf(g0); pinf(g1); }
        if constexpr (st == 1) { e0 = -1.4426950408889634f * g0; e1 = -1.4426950408889634f * g1; pinf(e0); pinf(e1); }
        if constexpr (st == 2) { e0 = __builtin_amdgcn_exp2f(e0); pinf(e0); }
        if constexpr (st == 3) { e1 = __builtin_amdgcn_exp2f(e1); pinf(e1); }
        if constexpr (st == 4) { e0 += 1.f; e1 += 1.f; pinf(e0); pinf(e1); }
        if constexpr (st == 5) { e0 = __builtin_amdgcn_rcpf(e0); pinf(e0); }
        if constexpr (st == 6) { e1 = __builtin_amdgcn_rcpf(e1); pinf(e1); }
        if constexpr (st == 7) { unsigned u = pk(g0 * e0, g1 * e1); pinu(u); pk4b[tt][q] = u; }
        if constexpr (st == 8) { g0 = __builtin_bit_cast(float, pk4b[tt][q] << 16) * __builtin_bit_cast(float, pk4b[2 + tt][q] << 16); pinf(g0); }
        if constexpr (st == 9) {
          unsigned u = pk(g0, __builtin_bit_cast(float, pk4b[tt][q] & 0xFFFF0000u) * __builtin_bit_cast(float, pk4b[2 + tt][q] & 0xFFFF0000u)); pinu(u); pk4b[tt][q] = u;
          if constexpr (q == 3) { CFENCE(); st128(rs, pk4b[tt], pvo, h_base(prev, pol) + spread(id * 2048) + tt * 1024); CFENCE(); }
        }
      }
    }
  };
  auto vb_ep = [&](auto CT, auto KS, int seg, unsigned vo) {
    constexpr int ct = CT, ks = KS, e = ct % 3;
    if constexpr (ks == 0) pin2(c[e][0], c[e][1]);
    o[ks] = pk(c[e][ks >> 1][2 * (ks & 1)], c[e][ks >> 1][2 * (ks & 1) + 1]); pin1(o[ks]);
    if constexpr (ks == 3) { CFENCE(); st128(rs, o, vo, b_addr(seg, (ct >> 1) * 8 + wave * 2 + (ct & 1), pol)); CFENCE(); }
  };
  auto vb_p2 = [&](int seg, unsigned vo, auto&& side) {
    static_for<10>([&](auto CT) {
      static_for<4>([&](auto KS) {
        constexpr int ct = CT, ks = KS, wi = 20 + ((ct >> 1) * 2 + (ks >> 1)) * 2 + (ct & 1);
        mf_w<wi, ks & 1, false, ks == 0>(c[ct % 3][0], w, vf[ks][0]);
        side(IC<ct * 4 + ks>{});
        mf_w<wi, ks & 1, false, ks == 0>(c[ct % 3][1], w, vf[ks][1]);
        if constexpr (ct >= 2) vb_ep(IC<ct - 2>{}, KS, seg, vo);
      });
    });
  };
  for (int seg = seg0, nxt; seg < lim; seg = nxt, ++it) {
    nxt = seg + (seg < sw ? 2 : 1);
    if (it == 4)
      for (int budget = SPIN_CAP; budget > 0; --budget) { const u32x2 v = ld64(rs, (64 + (lane & 3)) * 128, OFF_CTL + CTL_XP); CFENCE(); if (__all(v.x == bt.t0 && v.y == bt.t1)) break; }
    const unsigned buf = (it & 1) ? L_XB1 : L_XB0, nbuf = L_XB0 + L_XB1 - buf;
    const unsigned char* xsrc = a.ws + OFF_XN + (size_t)(nxt < lim ? nxt : seg) * 40960 + wave * 10240;
    const unsigned xdst = nbuf + wave * 10240;
    const unsigned char *xs0 = xsrc, *xs1 = xsrc + 4096, *xs2 = xsrc + 8192;
    PINS(xs0); PINS(xs1); PINS(xs2);
    auto dma = [&](auto K) {
      if constexpr (K % 5 == 0 && K <= 10) set_m0(xdst + (K / 5) * 4096);
      if constexpr (K % 5 != 0 && K <= 12) glds_off<(K % 5 - 1) * 1024>(K < 5 ? xs0 : K < 10 ? xs1 : xs2, l16);
    };
    if constexpr (KIND != K_VB) {
      f32x4 acc[4][2];
      xproj<4, true>(acc, w, xr, buf + l16, nbuf + l16, [&](auto S) {
        constexpr int ks = S / 8, j = S % 8;
        if constexpr (j == 5) dma(IC<ks>{});
        if constexpr (KIND == K_QK && j == 7) out_prev(IC<ks>{});
        if constexpr (KIND == K_F1 && (j == 0 || j == 2 || j == 4 || j >= 6)) out_prev(IC<ks * 5 + (j < 6 ? j / 2 : j - 3)>{});
        if constexpr (KIND == K_QK && ks == 11 && j == 7) { vmwait<6>(); st32(rs, it, it > NFAST ? fvo : OOB, 0); }
        if constexpr (ks == 18 && j == 0) {
          if constexpr (KIND == K_QK) { vmwait<0>(); BAR(); } else { vmwait<1>(); BAR(); st32(rs, it - 1, it > 1 ? fvo : OOB, 0); }
        }
      });
      acc_wait(acc);
#pragma unroll
      for (int m = 0; m < 2; ++m)
#pragma unroll
        for (int tt = 0; tt < 2; ++tt) pk4b[2 * m + tt] = cat4(pk4(acc[2 * m][tt]), pk4(acc[2 * m + 1][tt]));
      prev = seg; pvo = svo;
      if (KIND == K_QK && it < NFAST) {
        static_for<4>([&](auto N) { out_prev(N); });
        vmwait<0>(); st32(rs, it + 1, fvo, 0); pvo = OOB; if (it == 0) EV(2);
      }
    } else {
      f32x4 acc[2][2];
      xproj<2, false>(acc, w, xr, buf + l16, 0, [&](auto S) {
        constexpr int ks = S / 4, j = S % 4;
        if constexpr (j == 0 && ks >= 2) dma(IC<ks - 2>{});
        if constexpr (j == 3 && ks < 8) vb_ep(IC<8 + ks / 4>{}, IC<ks % 4>{}, prev, pvo);
        if constexpr (j == 3 && ks == 17) { vmwait<6>(); st32(rs, it, it > NFAST ? fvo : OOB, 0); }
      });
      acc_wait(acc);
#pragma unroll
      for (int tt = 0; tt < 2; ++tt) ldsw128(l16 + (wave * 2 + tt) * 1024, cat4(pk4(acc[0][tt]), pk4(acc[1][tt])));
      BAR();
#pragma unroll
      for (int k = 0; k < 8; ++k) vf[k >> 1][k & 1] = ldsr128(l16 + k * 1024);
      prev = seg; pvo = svo;
      vb_p2(seg, svo, [&](auto S) {
        constexpr int n = S;
        if constexpr (n == 32) { vmwait<6>(); BAR(); }
        if constexpr (n >= 33 && n < 37) { CFENCE(); xr[(n - 33) >> 1][(n - 33) & 1] = ldsr128(nbuf + l16 + (n - 33) * 1024); CFENCE(); }
      });
      if (it < NFAST) {
        NOP(7);
        static_for<8>([&](auto N) { vb_ep(IC<8 + N / 4>{}, IC<N % 4>{}, prev, pvo); });
        vmwait<0>(); st32(rs, it + 1, fvo, 0); pvo = OOB; if (it == 0) EV(2);
      }
    }
  }
  if constexpr (KIND == K_QK) static_for<4>([&](auto N) { out_prev(N); });
  else if constexpr (KIND == K_F1) static_for<80>([&](auto N) { out_prev(N); });
  else {
    NOP(7);
    static_for<8>([&](auto N) { vb_ep(IC<8 + N / 4>{}, IC<N % 4>{}, prev, pvo); });
  }
  VMWAIT(0);
  st32(rs, it, fvo, 0);
  EV(KIND == K_F1 ? 6 : 3);
  BAR();
  return true;
}

constexpr int REPL = 4;
DEV unsigned slot_base(int s, unsigned pol) { return OFF_COMM + (s & 3) * MB + ((pol >> ((s >> 1) & 1)) & 1) * 8192; }
DEV int wmax(int v) {
  v = max(v, __builtin_amdgcn_update_dpp(v, v, 0xB1, 0xf, 0xf, false));
  v = max(v, __builtin_amdgcn_update_dpp(v, v, 0x4E, 0xf, 0xf, false));
  v = max(v, __builtin_amdgcn_update_dpp(v, v, 0x124, 0xf, 0xf, false));
  v = max(v, __builtin_amdgcn_update_dpp(v, v, 0x128, 0xf, 0xf, false));
  return max(max(__builtin_amdgcn_readlane(v, 0), __builtin_amdgcn_readlane(v, 16)), max(__builtin_amdgcn_readlane(v, 32), __builtin_amdgcn_readlane(v, 48)));
}
DEV int wmin(int v) { return -wmax(-v); }
DEV void report(rsrc_t rs, int e, int s, unsigned flags, int lane) {
  unsigned long long v, c;
  asm volatile("s_memrealtime %0\n\ts_memtime %1\n\ts_waitcnt lgkmcnt(0)" : "=s"(v), "=s"(c) :: "memory");
  const u32x2 tm = *(volatile LAS u32x2*)(uintptr_t)(L_TM + (s & 1) * 16);
  st128(rs, (u32x4){(unsigned)s + 1, (unsigned)v << 3, (((unsigned)c - tm.x) << 2) | flags | (tm.y & 1), tm.y >> 2}, lane < 8 ? lane * 16 : OOB, OFF_REP + (s * 128 + e) * 128);
}
DEV void sleep_until_turn(rsrc_t rs, Sched& c, int j, int off) {
  for (;;) {
    adopt_cur(c, ld128(rs, 0, OFF_CTL + CTL_CUR), j + 1); CFENCE();
    const int d = (int)(tau_of(c, j) + off - now8());
    if (c.j0 == 0 || d > 200 * MAXWAIT) return;
    if (d < 150 * 8) { wait_until(tau_of(c, j) + off); return; }
    if (d > 500 * 8) __builtin_amdgcn_s_sleep(127); else __builtin_amdgcn_s_sleep(40);
  }
}
DEV int probe_seg(int s, int lane) { return min(s + (3 << (lane & 3)), NSEG - 1); }
DEV unsigned probe_ld(rsrc_t rs, int s, int lane) { return ld32(rs, lane < 4 ? probe_seg(s, lane) * 128 : OOB, OFF_CTL + CTL_RL); }
DEV int probe_rdy(unsigned v, int s, int lane) { return wmax(v != 0 ? probe_seg(s, lane) + 1 : 0); }
DEV void role_agg(rsrc_t rs, int lane) {
  int done = 0, budget = SPIN_CAP;
  while (done < NSEG && --budget > 0) {
    int m = NSEG;
#pragma unroll
    for (int i = 0; i < 8; ++i) m = min(m, first_open(i >> 2, (lane >> 2) & 1, ld32(rs, (i * 64 + lane) * 128, OFF_CTL)));
    CFENCE();
    m = min(wmin(m), NSEG);
    for (; done < m; done = min(done + 8, m)) st128(rs, (u32x4){1, 1, 1, 1}, done + (lane >> 3) < m ? done * 128 + lane * 16 : OOB, OFF_CTL + CTL_RL);
    __builtin_amdgcn_s_sleep(16);
  }
}
DEV unsigned rep_off(int j, int lane) { return ((j >> 1) * 128 + ((j & 1) ? 64 + min(lane, 39) : lane)) * 128; }
#define PTSLEEP 2
#define PSLEEP 2
#define HOLDP 720
#define HOLDC 1080
#define NOPM_(n) asm volatile("s_nop " #n ::: "memory")
#define NOPM(n) NOPM_(n)
#define VSLACK 48
#define PACE 11
#define NOPV(n, x) asm volatile("s_nop " #n : "+v"(x))
template <int N> DEV void zero_acc(f32x4 (&c)[N]) {
#pragma unroll
  for (int t = 0; t < N; ++t) { c[t] = (f32x4){0, 0, 0, 0}; PIN(c[t]); }
}
DEV unsigned memoff(int j) { return j < 8 ? j * 1024 : C_MEM1 + (j - 8) * 1024; }
DEV bool has_nan(f32x4 v) { return __any(__builtin_isunordered(v[0], v[1]) || __builtin_isunordered(v[2], v[3])); }
template <int N> DEV bool has_sent(const u32x4 (&a)[N]) {
  unsigned m = 0;
#pragma unroll
  for (int k = 0; k < N; ++k) m = max(m, mx4(a[k]));
  return __any(m == SENT);
}
DEV int mrow(int i, int half) { return 4 * (i >> 1) + 2 * half + (i & 1); }
DEV unsigned foldpk(f32x4 v) { return pk(v[0] + v[2], v[1] + v[3]); }
DEV void ldsw32(unsigned a, unsigned v) { *(LAS unsigned*)(uintptr_t)a = v; }
DEV void mem_wait(rsrc_t rs, unsigned sb, int lane, int& budget) {
  for (;;) {
    const unsigned u = ld32(rs, lane * 128, sb), v = ld32(rs, C_MEM1 + (lane & 15) * 128, sb);
    CFENCE();
    if (__all(u != SENT && v != SENT) || --budget <= 0) break;
    __builtin_amdgcn_s_sleep(PSLEEP);
  }
}
template <int NT, int NK>
DEV void project(rsrc_t rs, const unsigned (&vo)[10], unsigned sb, const u32x8 (&w)[40], int idx, u32x4 (&am)[10], f32x4 (&acc)[NT]) {
#pragma unroll
  for (int k = 0; k < 8; ++k) {
    am[k] = ld128(rs, vo[k], sb);
    if (k < 7) NOPM(PACE); else CFENCE();
  }
  static_for<NK>([&](auto K) {
    constexpr int ks = K;
    smf_w<ks * 2>(acc[0], am[ks], w, idx); smf_w<ks * 2 + 1>(acc[1], am[ks], w, idx);
    if constexpr (ks < 2) { CFENCE(); am[8 + ks] = ld128(rs, vo[8 + ks], sb); CFENCE(); }
    if constexpr (NT == 4) { smf_w<20 + ks * 2>(acc[2], am[ks], w, idx); smf_w<20 + ks * 2 + 1>(acc[3], am[ks], w, idx); }
  });
}
#define DPL " row_ror:8 row_mask:0xf bank_mask:0x3\n\t"
#define DPH " row_ror:8 row_mask:0xf bank_mask:0xc\n\t"
#define DPA " quad_perm:[2,3,0,1] row_mask:0xf bank_mask:0xf\n\t"
#define FADD2(d0, d1, c) asm volatile("v_add_f32 %0, %2, %4\n\tv_add_f32 %1, %3, %5" : "=&v"(d0), "=&v"(d1) : "v"(c[0]), "v"(c[1]), "v"(c[2]), "v"(c[3]))

constexpr unsigned P_RING = 16384;
#define ROWRED(op) "s_nop 1\n\t" op "_dpp %7, %6, %6" DPA op "_dpp %8, %6, %6" DPA "s_nop 1\n\tv_permlane16_swap_b32 %7, %8\n\t" op " %6, %7, %8\n\t" op " %7, %7, %8\n\t" \
                   "s_nop 1\n\tv_permlane32_swap_b32 %6, %7\n\t" op " %6, %6, %7\n\t"
#define SM_SELM "v_cndmask_b32 %4, %24, %13, %26\n\tv_cndmask_b32 %5, %25, %14, %26\n\t"
#define SM_SELX "v_cndmask_b32 %0, %20, %15, %26\n\tv_cndmask_b32 %1, %21, %16, %26\n\tv_cndmask_b32 %2, %22, %17, %26\n\tv_cndmask_b32 %3, %23, %18, %26\n\t"
#define SM_EXPM "v_fma_f32 %4, %4, %19, -%12\n\tv_fma_f32 %5, %5, %19, -%12\n\tv_exp_f32 %4, %4\n\tv_exp_f32 %5, %5\n\t"
#define SM_EXPX "v_fma_f32 %0, %0, %19, -%12\n\tv_fma_f32 %1, %1, %19, -%12\n\tv_exp_f32 %0, %0\n\tv_fma_f32 %2, %2, %19, -%12\n\tv_exp_f32 %1, %1\n\tv_fma_f32 %3, %3, %19, -%12\n\tv_exp_f32 %2, %2\n\t" \
                "v_add_f32 %6, %4, %5\n\tv_exp_f32 %3, %3\n\tv_add_f32 %7, %0, %1\n\tv_add_f32 %6, %6, %2\n\tv_add_f32 %7, %7, %3\n\tv_add_f32 %6, %6, %7\n\t"
#define SM_OUT "v_rcp_f32 %7, %6\n\tv_med3_f32 %8, %6, %27, %28\n\t" \
               "v_mul_f32 %0, %0, %7\n\tv_mul_f32 %1, %1, %7\n\tv_cmp_eq_f32 %11, %8, %6\n\tv_mul_f32 %2, %2, %7\n\tv_mul_f32 %3, %3, %7\n\t" \
               "v_cvt_pk_bf16_f32 v248, |%0|, |%1|\n\tv_cvt_pk_bf16_f32 v250, |%0|, |%1|\n\tv_cvt_pk_bf16_f32 v249, |%2|, |%3|\n\tv_cvt_pk_bf16_f32 v251, |%2|, |%3|\n\t" \
               "v_permlane32_swap_b32 v248, v250\n\tv_mul_f32 %4, %4, %7\n\tv_permlane32_swap_b32 v249, v251\n\tv_mul_f32 %5, %5, %7"
#define SM_OPS : "=&v"(x0), "=&v"(x1), "=&v"(x2), "=&v"(x3), "=&v"(y0), "=&v"(y1), "=&v"(sum), "=&v"(t1), "=&v"(t2), "=&{v[248:251]}"(ox), "=&v"(t3), "=&s"(okm), "+v"(mref) \
               : "v"(s0[2]), "v"(s0[3]), "v"(s2[0]), "v"(s2[1]), "v"(s2[2]), "v"(s2[3]), "s"(cs), "v"(s1[0]), "v"(s1[1]), "v"(s1[2]), "v"(s1[3]), "v"(s0[0]), "v"(s0[1]), \
                 "s"(0xCCCCCCCCCCCCCCCCull), "v"(lo), "v"(hi)
template <int CLS>
DEV void p_loop(const Args& a, rsrc_t rs, int h, int wave, int lane, unsigned pol, const u32x8 (&w)[40], Sched& sc) {
  const int n = lane & 15, g = lane >> 4;
  const int idx = (lane & 2) ? 0xEEEE : 0x4444;
  const float NEG = -3.0e38f;
  const unsigned l16 = lane * 16, cpy = (h & (REPL - 1)) * 65536;
  unsigned vo[10];
#pragma unroll
  for (int k = 0; k < 10; ++k) { vo[k] = l16 + memoff(k); PIN(vo[k]); }
  f32x4 i1, i2;
#pragma unroll
  for (int r = 0; r < 4; ++r) {
    const bool tri = (4 * g + r) <= n;
    i1[r] = (wave == 1 && !tri) ? NEG : 0.f;
    i2[r] = (wave == 1 || (wave == 2 && !tri)) ? NEG : 0.f;
  }
  unsigned kvo[2];
#pragma unroll
  for (int tl = 0; tl < 2; ++tl) {
    int tok = 16 * tl + n;
    if (CLS == 0) { const int gp = n >> 2, r = n & 3; tok = 16 * (r >> 1) + 8 * (gp & 1) + 4 * (gp >> 1) + 2 * tl + (r & 1); }
    kvo[tl] = P_RING + (tok >> 4) * 4096 + (g * 16 + (tok & 15)) * 16;
  }
  const unsigned qvo = P_RING + 8192 + (wave == 2 ? 4096 : 0) + l16;
  unsigned wra = (32 * wave + n) * 16 + g * 4, wrq = 2 * wra;
  unsigned tra = (4 * g + ((lane >> 2) & 3)) * 16 + (lane & 1) * 8, trq = (4 * g + ((lane >> 2) & 3)) * 32 + (lane & 3) * 8;
  PIN(wra); PIN(wrq); PIN(tra); PIN(trq);
  unsigned vox[2], vom[2];
#pragma unroll
  for (int c = 0; c < 2; ++c) {
    if (CLS == 0) {
      vox[c] = (2 * c + (lane >> 5)) * 65536 + C_PX + (h >> 4) * 16384 + (h & 15) * 512 + (lane & 31) * 16;
      vom[c] = lane < 32 ? (2 * c + (lane >> 4)) * 65536 + C_PM + h * 128 + (lane & 15) * 8 : OOB;
      PIN(vox[c]); PIN(vom[c]);
    } else { vox[c] = spread((h * 2 + (wave - 1)) * 1024) + l16; vom[c] = lane < 16 ? (8 + wave - 1) * 16384 + h * 256 + l16 : OOB; }
  }
  const unsigned pvo = prog_off(0, h, (lane >> 2) & 1, lane & 3);
  const float cs = 0.08838834764831845f * 1.4426950408889634f;
  float mref = 0.f, lo = 0x1p-64f, hi = 0x1p64f;
  PIN(lo); PIN(hi);
  int budget = SPIN_CAP, rdy = 0;
  auto fetch = [&](int seg) {
    while (rdy <= seg && --budget > 0) { rdy = wmin(first_open(0, (lane >> 2) & 1, ld32(rs, pvo, 0))); CFENCE(); }
    static_for<4>([&](auto J) {
      const unsigned char* src = a.ws + kq_addr(J >> 1, seg, h, pol) + (J & 1) * 4096;
      set_m0(P_RING + (seg & 1) * 16384 + J * 4096); NOP(0);
      glds_off<0>(src, l16); glds_off<1024>(src, l16); glds_off<2048>(src, l16); glds_off<3072>(src, l16);
    });
  };
  u32x4 kx[2][4], qf[4];
  auto pull = [&](int seg) {
    const unsigned rb = (seg & 1) * 16384;
#pragma unroll
    for (int ks = 0; ks < 4; ++ks) { kx[0][ks] = ldsr128(kvo[0] + rb + ks * 1024); kx[1][ks] = ldsr128(kvo[1] + rb + ks * 1024); if (CLS == 1) qf[ks] = ldsr128(qvo + rb + ks * 1024); }
  };
#pragma unroll
  for (int ks = 0; ks < 4; ++ks) { kx[0][ks] = (u32x4){0, 0, 0, 0}; kx[1][ks] = kx[0][ks]; qf[ks] = kx[0][ks]; }
  unsigned hold = now8(), pend = 0;
  for (int s = 0; s < NSEG; ++s) {
    unsigned sb = slot_base(s, pol), sbr = sb + cpy;
    const float n0 = g >= 2 ? NEG : 0.f;
    f32x4 s0 = {n0, n0, n0, n0}, s1 = i1, s2 = i2;
    PINS(sb); PINS(sbr);
    if (CLS < 2) {
      PIN(s0); PIN(s1); PIN(s2);
#pragma unroll
      for (int ks = 0; ks < 4; ++ks) { PIN(kx[0][ks]); PIN(kx[1][ks]); }
    }
    if (CLS == 1) {
      NOP(1);
#pragma unroll
      for (int ks = 0; ks < 4; ++ks) { mf_vv(s1, kx[0][ks], qf[ks]); mf_vv(s2, kx[1][ks], qf[ks]); }
    }
    unsigned cst = 0;
    if (CLS == 2) cst = trigger(rs, sc, 2 * s, hold, [&]() { mem_wait(rs, sbr, lane, budget); });
    f32x4 acc[4];
    zero_acc(acc);
    STAMP_DECL;
    unsigned good = 2;
    if (CLS == 2) release(cst); else BAR();
    STAMP(0);
    for (;;) {
      u32x4 am[10];
      project<4, 8>(rs, vo, sbr, w, idx, am, acc);
      float a0, a1, b0, b1;
      smf_w<16>(acc[0], am[8], w, idx); smf_w<17>(acc[1], am[8], w, idx); smf_w<18>(acc[0], am[9], w, idx); smf_w<19>(acc[1], am[9], w, idx);
      smf_w<36>(acc[2], am[8], w, idx); smf_w<37>(acc[3], am[8], w, idx); smf_w<38>(acc[2], am[9], w, idx);
      FADD2(a0, a1, acc[0]);
      smf_w<39>(acc[3], am[9], w, idx);
      FADD2(b0, b1, acc[1]);
      STAMP(1);
      asm volatile("ds_write2_b32 %0, %1, %1 offset0:0 offset1:1" :: "v"(wrq), "v"(pk(a0, a1)) : "memory");
      asm volatile("ds_write2_b32 %0, %1, %1 offset0:128 offset1:129" :: "v"(wrq), "v"(pk(b0, b1)) : "memory");
      const bool bad = __any(__builtin_isunordered(a0, a1));
      PIN(acc[2]);
      const unsigned f2 = foldpk(acc[2]);
      pin1(f2);
      PIN(acc[3]);
      ldsw32(wra + 4096, f2); ldsw32(wra + 4096 + 256, foldpk(acc[3]));
      if (__builtin_expect(!bad, 1)) break;
      if ((!(good & 2) && !has_sent(am)) || --budget <= 0) break;
      good = 0;
      zero_acc(acc);
      mem_wait(rs, sbr, lane, budget);
    }
    if (__builtin_expect(s == 0, 0)) {
      if (CLS == 2) { fetch(0); VMWAIT(0); }
      BAR();
      EV(2);
      if (CLS < 2) pull(0);
      if (CLS == 1) {
#pragma unroll
        for (int ks = 0; ks < 4; ++ks) { mf_vv(s1, kx[0][ks], qf[ks]); mf_vv(s2, kx[1][ks], qf[ks]); }
      }
    }
    BAR();
    STAMP(2);
    u32x4 km[4];
#define TRQ(ks) qf[ks] = cat4(trread(trq + (ks) * 1024), trread(trq + (ks) * 1024 + 512))
#define TRK(ks) km[ks] = cat4(trread(tra + 4096 + (ks) * 512), trread(tra + 4096 + (ks) * 512 + 256))
    if (CLS == 0) {
      TRQ(0); TRQ(1); TRK(0); TRK(1); CFENCE();
      mf_vv(s1, kx[0][0], qf[0]); CFENCE(); TRQ(2); CFENCE(); mf_vv(s2, kx[1][0], qf[0]); CFENCE(); TRK(2); CFENCE();
      mf_vv(s1, kx[0][1], qf[1]); CFENCE(); TRQ(3); CFENCE(); mf_vv(s2, kx[1][1], qf[1]); CFENCE(); TRK(3); CFENCE();
      mf_vv(s0, km[0], qf[0]); mf_vv(s0, km[1], qf[1]); mf_vv(s0, km[2], qf[2]); mf_vv(s0, km[3], qf[3]);
      mf_vv(s1, kx[0][2], qf[2]); mf_vv(s2, kx[1][2], qf[2]); mf_vv(s1, kx[0][3], qf[3]); mf_vv(s2, kx[1][3], qf[3]);
      STAMP(3);
      float x0, x1, x2, x3, y0, y1, sum, t1, t2, t3;
      u32x4 ox; u32x2 om; unsigned long long okm;
      asm volatile(SM_SELM SM_EXPM "s_nop 1\n\t" SM_SELX SM_EXPX ROWRED("v_add_f32") SM_OUT SM_OPS);
      if (__builtin_expect(okm != ~0ull, 0))
        asm volatile(SM_SELM SM_SELX "v_max3_f32 %6, %4, %5, %0\n\tv_max3_f32 %7, %1, %2, %3\n\tv_max_f32 %6, %6, %7\n\t" ROWRED("v_max_f32") "v_mul_f32 %12, %19, %6\n\t"
                     SM_EXPM SM_EXPX ROWRED("v_add_f32") SM_OUT SM_OPS);
      st128(rs, ox, vox[0], sb); st128(rs, ox, vox[1], sb);
      asm volatile("v_cvt_pk_bf16_f32 v252, |%1|, |%2|\n\tv_cvt_pk_bf16_f32 v253, |%1|, |%2|\n\ts_nop 1\n\tv_permlane16_swap_b32 v252, v253" : "=&{v[252:253]}"(om) : "v"(y0), "v"(y1) : "memory");
      st64(rs, om, vom[0], sb); st64(rs, om, vom[1], sb);
      CFENCE();
      STAMP(4);
      STAMP_OUT(h, s);
      report(rs, h, s, good, lane);
      mref += __builtin_amdgcn_logf(sum);
      if (s >= 1) {
        const unsigned rb = slot_base(s - 1, pol);
#pragma unroll
        for (int c = 0; c < 2; ++c) { st128(rs, (u32x4){SENT, SENT, SENT, SENT}, vox[c], rb); st64(rs, (u32x2){SENT, SENT}, vom[c], rb); }
      }
      if (s == 0) BAR();
      if (s + 1 < NSEG) pull(s + 1);
    } else if (CLS == 1) {
      TRK(0); TRK(1); TRK(2); TRK(3);
#pragma unroll
      for (int ks = 0; ks < 4; ++ks) mf_vv(s0, km[ks], qf[ks]);
      asm volatile("s_nop 7" : "+v"(s0), "+v"(s1), "+v"(s2));
      float mxv = fmaxf(fmaxf(fmaxf(s1[0], s1[1]), fmaxf(s1[2], s1[3])), fmaxf(fmaxf(s2[0], s2[1]), fmaxf(s2[2], s2[3])));
      mxv = fmaxf(fmaxf(fmaxf(s0[0], s0[1]), fmaxf(s0[2], s0[3])), mxv);
      mxv = xmax32(xmax16(mxv));
      const float mb = -mxv * cs;
      float sum = 0.f;
#pragma unroll
      for (int r = 0; r < 4; ++r) {
        s0[r] = __builtin_amdgcn_exp2f(fmaf(s0[r], cs, mb)); s1[r] = __builtin_amdgcn_exp2f(fmaf(s1[r], cs, mb)); s2[r] = __builtin_amdgcn_exp2f(fmaf(s2[r], cs, mb));
        sum += s0[r] + s1[r] + s2[r];
      }
      sum = xadd32(xadd16(sum));
      const float inv = __builtin_amdgcn_rcpf(sum);
      s0 *= inv; s1 *= inv; s2 *= inv;
      const unsigned so = pt_base(s, pol);
      const u32x2 pmv = pk4(s0);
      auto r0 = __builtin_amdgcn_permlane16_swap(pmv.x, pmv.x, false, false);
      auto r1 = __builtin_amdgcn_permlane16_swap(pmv.y, pmv.y, false, false);
      const u32x4 ptx = cat4(pk4(s1), pk4(s2));
      if (s == 0) BAR();
      if (s + 1 < NSEG) { pull(s + 1); __builtin_amdgcn_s_sleep(PTSLEEP); }
      st128(rs, ptx, vox[0], so);
      st128(rs, (u32x4){(unsigned)r0[0], (unsigned)r1[0], (unsigned)r0[1], (unsigned)r1[1]}, vom[0], so);
      STAMP(4);
      STAMP_OUT(h, s);
    } else {
      const unsigned tend = now8() + 24 * 8;
      if (s == 0) { fetch(1); VMWAIT(0); BAR(); }
      const u32x4 cur = ld128(rs, 0, OFF_CTL + CTL_CUR);
      rdy = max(rdy, probe_rdy(pend, s, lane));
      adopt_cur(sc, cur, 2 * s + 2);
      mark_overrun(sc, 2 * s, tend);
      if (s + 2 < NSEG) fetch(s + 2);
      pend = 0;
      if (rdy <= s + 5) pend = probe_ld(rs, s + 1, lane);
      STAMP_OUT(h, s);
      hold = tend + HOLDP; clk_update(sc);
    }
  }
  VMWAIT(0);
  BAR();
  if (CLS == 2) st128(rs, (u32x4){1, 1, 1, 1}, lane < 8 ? lane * 16 : OOB, OFF_REP + (NSEG * 128 + h) * 128);
  EV(3);
}
DEV bool role_p(const Args& a, rsrc_t rs, int h, int wave, int lane, Boot& bt) {
  u32x8 w[40];
  Sched sc; clk_init(sc);
  load_weights<K_P>(w, a.wq + h * 256, a.wk + h * 256, wave, lane, 15, []() { VMWAIT(0); });
  BAR();
  if (!boot_wait(rs, bt, lane)) return false;
  const unsigned pol = bt.pol;
  EV(1);
  if (wave == 0) p_loop<0>(a, rs, h, wave, lane, pol, w, sc);
  else if (wave < 3) p_loop<1>(a, rs, h, wave, lane, pol, w, sc);
  else p_loop<2>(a, rs, h, wave, lane, pol, w, sc);
  return true;
}

DEV bool role_v(const Args& a, rsrc_t rs, int h, int wave, int lane, Boot& bt) {
  u32x8 w[40];
  Sched sc; clk_init(sc);
  load_weights<K_V>(w, a.wv + h * 256, a.wo + (size_t)h * 128 * 1280, wave, lane, 15, []() { VMWAIT(0); });
  BAR();
  if (!boot_wait(rs, bt, lane)) return false;
  const unsigned pol = bt.pol;
  EV(1);
  const int n = lane & 15, g = lane >> 4;
  const int idx = (lane & 2) ? 0xEEEE : 0x4444;
  unsigned wra = (16 * wave + (n >> 2) * 2 + (n & 1)) * 32 + g * 8 + ((n >> 1) & 1) * 4, tra = (8 * g + ((lane >> 2) & 3)) * 32 + (lane & 3) * 8;
  unsigned zv0 = g < 2 ? (wave * 2 + g) * 16384 + h * 256 + n * 16 : OOB;
  unsigned zv1 = (8 + (g >> 1) * 8 + wave * 2 + (g & 1)) * 16384 + h * 256 + n * 16, zv2 = zv1 + 16 * 16384;
  PIN(wra); PIN(tra); PIN(zv0); PIN(zv1); PIN(zv2);
  unsigned vo[10];
#pragma unroll
  for (int k = 0; k < 10; ++k) { vo[k] = lane * 16 + memoff(k); PIN(vo[k]); }
  int budget = SPIN_CAP;
  unsigned hold = now8();
  for (int s = 0; s < NSEG; ++s) {
    unsigned sb = slot_base(s, pol) + (h & (REPL - 1)) * 65536, zb = z_base(s, pol);
    PINS(sb); PINS(zb);
    f32x4 c[10], acc[2];
    zero_acc(c); zero_acc(acc);
    unsigned cst;
    if (wave == 3) cst = trigger(rs, sc, 2 * s, hold, [&]() { mem_wait(rs, sb, lane, budget); }); else cst = cycles();
    STAMP_DECL;
    unsigned good = 2;
    release(__builtin_amdgcn_readfirstlane(cst));
    STAMP(0);
    for (;;) {
      u32x4 am[10];
      project<2, 10>(rs, vo, sb, w, idx, am, acc);
      STAMP(1);
      asm volatile("s_nop 7" : "+v"(acc[0]), "+v"(acc[1]));
      ldsw32(wra, foldpk(acc[0])); ldsw32(wra + 256, foldpk(acc[1]));
      if (__builtin_expect(!has_nan(acc[0]), 1)) break;
      if ((!(good & 2) && !has_sent(am)) || --budget <= 0) break;
      good = 0;
      zero_acc(acc);
      mem_wait(rs, sb, lane, budget);
    }
    BAR();
    STAMP(2);
    u32x4 va[2];
#pragma unroll
    for (int ks = 0; ks < 2; ++ks) va[ks] = cat4(trread(tra + ks * 1024), trread(tra + ks * 1024 + 128));
    CFENCE(); const u32x4 cur = ld128(rs, 0, OFF_CTL + CTL_CUR); CFENCE();
    u32x4 O0, O1, O2;
#define VMM(t, k) smf_w<20 + ((t) >> 1) * 4 + 2 * (k) + ((t) & 1)>(c[t], va[k], w, idx)
#define VEA(t, T) FADD2(T##0, T##1, c[t])
#define VEC2(R, ra, rb, O) asm volatile("v_cvt_pk_bf16_f32 " ra ", %1, %2\n\tv_cvt_pk_bf16_f32 " rb ", %3, %4" : "+{" R "}"(O) : "v"(ta0), "v"(ta1), "v"(tb0), "v"(tb1))
#define VSW(txt, R, O) asm volatile(txt : "+{" R "}"(O))
    float ta0, ta1, tb0, tb1;
    asm volatile("" : "={v[232:235]}"(O0), "={v[236:239]}"(O1), "={v[240:243]}"(O2));
    VMM(0, 0); VMM(1, 0); VMM(0, 1); VMM(1, 1);
    VMM(2, 0); VMM(3, 0); VMM(2, 1); VMM(3, 1);
    VMM(4, 0); NOP(1); VMM(5, 0); VEA(0, ta); VMM(4, 1); VEA(1, tb); VMM(5, 1); VEC2("v[232:235]", "v232", "v233", O0);
    VMM(6, 0); VEA(2, ta); VMM(7, 0); VEA(3, tb); VMM(6, 1); VSW("v_permlane16_swap_b32 v232, v233", "v[232:235]", O0); VMM(7, 1); VEC2("v[236:239]", "v236", "v237", O1);
    VMM(8, 0); VSW("v_permlane32_swap_b32 v232, v234", "v[232:235]", O0); VMM(9, 0); VSW("v_permlane32_swap_b32 v233, v235", "v[232:235]", O0);
    VMM(8, 1); VEA(4, ta); VMM(9, 1); CFENCE(); st128(rs, O0, zv0, zb); CFENCE(); VEA(5, tb);
    VEC2("v[236:239]", "v238", "v239", O1);
    VEA(6, ta); VEA(7, tb);
    VSW("v_permlane32_swap_b32 v236, v238\n\tv_permlane32_swap_b32 v237, v239", "v[236:239]", O1);
    VEC2("v[240:243]", "v240", "v241", O2);
    VSW("v_permlane16_swap_b32 v236, v237\n\tv_permlane16_swap_b32 v238, v239", "v[236:239]", O1);
    CFENCE(); st128(rs, O1, zv1, zb); CFENCE();
    VEA(8, ta); VEA(9, tb); VEC2("v[240:243]", "v242", "v243", O2);
    VSW("s_nop 0\n\tv_permlane32_swap_b32 v240, v242\n\tv_permlane32_swap_b32 v241, v243\n\ts_nop 1\n\tv_permlane16_swap_b32 v240, v241\n\tv_permlane16_swap_b32 v242, v243", "v[240:243]", O2);
    CFENCE(); st128(rs, O2, zv2, zb);
    CFENCE();
    STAMP(4);
    STAMP_OUT(32 + h, s);
    if (wave == 0) report(rs, 32 + h, s, good, lane);
    if (wave == 3) { const unsigned tend = now8(); hold = tend + HOLDP; adopt_cur(sc, cur, 2 * s + 2); mark_overrun(sc, 2 * s, tend - VSLACK); clk_update(sc); }
  }
  VMWAIT(0);
  BAR();
  if (wave == 0) st128(rs, (u32x4){1, 1, 1, 1}, lane < 8 ? lane * 16 : OOB, OFF_REP + (NSEG * 128 + 32 + h) * 128);
  return true;
}

#define CLOB_A "a0", "a1", "a2", "a3", "a4", "a5", "a6", "a7", "a8", "a9", "a10", "a11", "a12", "a13", "a14", "a15", "a16", "a17", "a18", "a19", "a20", "a21", "a22", "a23", "a24", "a25", "a26", "a27", "a28", "a29", "a30", "a31", "a32", "a33", "a34", "a35", "a36", "a37", "a38", "a39", "a40", "a41", "a42", "a43", "a44", "a45", "a46", "a47", "a48", "a49", "a50", "a51", "a52", "a53", "a54", "a55", "a56", "a57", "a58", "a59", "a60", "a61", "a62", "a63", "a64", "a65", "a66", "a67", "a68", "a69", "a70", "a71", "a72", "a73", "a74", "a75", "a76", "a77", "a78", "a79", "a80", "a81", "a82", "a83", "a84", "a85", "a86", "a87", "a88", "a89", "a90", "a91", "a92", "a93", "a94", "a95", "a96", "a97", "a98", "a99", "a100", "a101", "a102", "a103", "a104", "a105", "a106", "a107", "a108", "a109", "a110", "a111", "a112", "a113", "a114", "a115", "a116", "a117", "a118", "a119", "a120", "a121", "a122", "a123", "a124", "a125", "a126", "a127"
template <int J> DEV void c_ldb(unsigned l16, const unsigned char* src) {
  asm volatile("global_load_dwordx4 a[%c2:%c2+3], %0, %1 sc1\n\tglobal_load_dwordx4 a[%c2+4:%c2+7], %0, %1 offset:1024 sc1\n\t"
               "global_load_dwordx4 a[%c2+8:%c2+11], %0, %1 offset:2048 sc1\n\tglobal_load_dwordx4 a[%c2+12:%c2+15], %0, %1 offset:3072 sc1"
               :: "v"(l16), "s"(src + (J >> 1) * BSTR + (J & 1) * 4096), "n"(J * 16) : "memory", CLOB_A);
}
template <int J> DEV void c_smf(f32x4& acc, u32x4 p, int idx) { asm volatile("v_smfmac_f32_16x16x64_bf16 %0, %1, a[%c3:%c3+7], %2" : "+{v[248:251]}"(acc) : "v"(p), "v"(idx), "n"(J * 8)); }
#define ZFIRST 0
#define ASLEEP -1
#define BDELAY 30
DEV void c_arm(rsrc_t rs, int s, int cb, int lane, unsigned pol) {
  const unsigned zn = z_base(s, pol) + cb * 16384;
#pragma unroll
  for (int j = 0; j < 8; ++j) st128(rs, (u32x4){SENT, SENT, SENT, SENT}, lane * 16 + (j & 3) * 1024, zn + (j >> 2) * 4096);
}
DEV void role_c(const Args& a, rsrc_t rs, int cb, int wave, int lane, unsigned pol) {
  Sched sc; clk_init(sc);
  c_arm(rs, wave, cb, lane, pol);
  VMWAIT(0);
  BAR();
  const unsigned pvo = prog_off(1, lane >> 1, lane & 1, (cb >> 1) & 3);
  if (wave == 3) {
    int rdy = 0;
    for (int s = 0; s < NSEG - 1; ++s) {
      BAR(); BAR();
      unsigned v = 0;
      if (rdy <= s + 4) v = probe_ld(rs, s, lane);
      BAR();
      rdy = max(rdy, probe_rdy(v, s, lane));
      ldsw32(L_TM + 32, rdy);
    }
    return;
  }
  if (wave == 2) {
    const unsigned rvo = (lane >> 4) < REPL ? (lane >> 4) * 65536 + (cb < 32 ? cb * 256 : C_MEM1 + (cb - 32) * 256) + (lane & 15) * 16 : OOB;
    for (int s = 0; s < NSEG - 1; ++s) {
      BAR(); VMWAIT(0); BAR();
      if (ASLEEP >= 0) { __builtin_amdgcn_s_sleep(ASLEEP >= 0 ? ASLEEP : 0); if (s + 4 < NSEG) c_arm(rs, s + 4, cb, lane, pol); }
      BAR();
      st128(rs, (u32x4){SENT, SENT, SENT, SENT}, rvo, slot_base(s, pol));
      if (ASLEEP < 0 && s + 4 < NSEG) c_arm(rs, s + 4, cb, lane, pol);
    }
    return;
  }
  int budget = SPIN_CAP;
  const unsigned cpy = ((cb / 10) & (REPL - 1)) * 65536;
  auto c_wait = [&](unsigned sb, unsigned zb) {
    for (;;) {
      const unsigned u = ld32(rs, C_PM + (lane & 31) * 128, sb), v = ld32(rs, cb * 16384 + lane * 128, zb);
      CFENCE();
      if (__all(u != SENT && v != SENT) || --budget <= 0) break;
      __builtin_amdgcn_s_sleep(PSLEEP);
    }
  };
  if (wave == 1) {
    unsigned hold = now8();
    for (int s = 0; s < NSEG - 1; ++s) {
      release(trigger(rs, sc, 2 * s + 1, hold, [&]() { c_wait(slot_base(s, pol) + cpy, z_base(s, pol)); }));
      clk_update(sc);
      BAR();
      hold = now8();
      adopt_cur(sc, ld128(rs, 0, OFF_CTL + CTL_CUR), 2 * s + 3);
      mark_overrun(sc, 2 * s + 1, hold);
      hold += HOLDC;
      if (sc.j0 > sc.jbad) wait_until(tau_of(sc, 2 * s + 2) + BDELAY * 8);
      BAR();
    }
    return;
  }
  const int n = lane & 15, g = lane >> 4;
  const int idx = (lane & 2) ? 0xEEEE : 0x4444;
  const unsigned l16 = lane * 16;
  unsigned pxvo[4], zvo[2], pmvo = C_PM + 2 * g * 128 + n * 8;
#pragma unroll
  for (int i = 0; i < 4; ++i) { pxvo[i] = C_PX + (i >> 1) * 16384 + (i & 1) * 4096 + l16; PIN(pxvo[i]); }
#pragma unroll
  for (int i = 0; i < 2; ++i) { zvo[i] = cb * 16384 + i * 4096 + l16; PIN(zvo[i]); }
  const unsigned mvo = (cb < 32 ? cb * 256 : C_MEM1 + (cb - 32) * 256) + mrow(2 * g + (n >> 3), (n & 7) >> 2) * 16 + (n & 3) * 4;
  unsigned oob = OOB; PIN(oob); PIN(pmvo);
  {
    const unsigned short* m0 = (const unsigned short*)a.mem0 + (2 * g + (n >> 3)) * 640 + cb * 16 + (n & 7);
    const unsigned v = min(m0[0] | (unsigned)m0[8] << 16, 0xFFFFFFFEu);
#pragma unroll
    for (int c = 0; c < REPL; ++c) st32(rs, v, mvo, slot_base(0, pol) + c * 65536);
  }
  int rdy = 0;
  auto fetch = [&](int seg) {
    while (rdy <= seg && --budget > 0) { rdy = wmin(2 * (int)ld32(rs, pvo, 0) + (lane & 1)); CFENCE(); }
    const unsigned char* src = a.ws + b_addr(seg, cb, pol);
    static_for<8>([&](auto J) { c_ldb<J>(l16, src); });
  };
  fetch(0);
  for (int s = 0; s < NSEG - 1; ++s) {
    unsigned sb = slot_base(s, pol) + cpy, zb = z_base(s, pol), sbn[REPL];
#pragma unroll
    for (int c = 0; c < REPL; ++c) { sbn[c] = slot_base(s + 1, pol) + c * 65536; PINS(sbn[c]); }
    PINS(sb); PINS(zb);
    STAMP_DECL;
    unsigned good = 2;
    BAR();
    STAMP(0);
    for (;;) {
      u32x4 z[8], pm[4], px[16];
#pragma unroll
      for (int j = 0; j < ZFIRST; ++j) { z[j] = ld128(rs, zvo[j >> 2] + (j & 3) * 1024, zb); CFENCE(); }
#pragma unroll
      for (int j = 0; j < 16; ++j) px[j] = ld128(rs, pxvo[j >> 2] + (j & 3) * 1024, sb);
      CFENCE();
#pragma unroll
      for (int j = 0; j < 4; ++j) pm[j] = cat4(ld64(rs, pmvo + j * 1024, sb), ld64(rs, pmvo + j * 1024 + 128, sb));
      CFENCE();
#pragma unroll
      for (int j = ZFIRST; j < 8; ++j) { z[j] = ld128(rs, zvo[j >> 2] + (j & 3) * 1024, zb); CFENCE(); }
      f32x4 acc = {0, 0, 0, 0};
      asm volatile("s_nop 1" : "+{v[248:251]}"(acc));
      static_for<16>([&](auto J) { c_smf<J>(acc, px[J], idx); });
#pragma unroll
      for (int j = 0; j < 4; ++j) asm volatile("v_smfmac_f32_16x16x64_bf16 %0, %1, %2, %3" : "+{v[248:251]}"(acc) : "v"(pm[j]), "v"(cat8(z[2 * j], z[2 * j + 1])), "v"(idx));
      STAMP(1);
      unsigned o, vo2; unsigned long long okm;
      asm volatile(
        "s_nop 7\n\t"
        "v_add_f32 v248, v248, v250\n\t"
        "v_add_f32 v250, v249, v251\n\t"
        "v_add_f32 v249, v249, v251\n\t"
        "v_mov_b32_dpp v250, v248" DPL
        "s_nop 0\n\t"
        "v_mov_b32_dpp v248, v249" DPH
        "v_cmp_o_f32 %3, v248, v250\n\t"
        "v_cvt_pk_bf16_f32 %1, v248, v250\n\t"
        "s_nop 0\n\t"
        "v_cndmask_b32 %2, %5, %4, %3"
        : "+{v[248:251]}"(acc), "=&v"(o), "=&v"(vo2), "=&s"(okm) : "v"(mvo), "v"(oob));
#pragma unroll
      for (int c = 0; c < REPL; ++c) st32(rs, o, vo2, sbn[c]);
      CFENCE();
      if (__builtin_expect(okm == ~0ull, 1)) break;
      if ((!(good & 2) && !(has_sent(z) || has_sent(pm) || has_sent(px))) || --budget <= 0) {
#pragma unroll
        for (int c = 0; c < REPL; ++c) st32(rs, min(o, 0xFFFFFFFEu), mvo, sbn[c]);
        break;
      }
      good = 0;
      c_wait(sb, zb);
    }
    STAMP(4);
    STAMP_OUT(64 + cb, s);
    report(rs, 64 + cb, s, good, lane);
    BAR();
    if (s == 0) EV(2); if (s == NSEG - 2) EV(3);
    if (s > 0) rdy = max(rdy, (int)__builtin_amdgcn_readfirstlane(*(volatile LAS unsigned*)(uintptr_t)(L_TM + 32)));
    BAR();
    if (s + 2 < NSEG) fetch(s + 1);
  }
}

template <int IMG, int C0, int C1> DEV void u_scatter(const f32x4 (&acc)[4][2], unsigned base) {
  PIN(base);
  LAS char* p = (LAS char*)(uintptr_t)base;
#pragma unroll
  for (int c = C0; c < C1; ++c)
#pragma unroll
    for (int tt = 0; tt < 2; ++tt)
#pragma unroll
      for (int r = 0; r < 4; ++r) *(LAS float*)(p + IMG + (tt * 16 + r) * 272 + c * 64) = acc[c][tt][r];
}
template <int IMG> DEV void u_gather(f32x4& v0, f32x4& v1, unsigned base) {
  PIN(base);
  LAS char* p = (LAS char*)(uintptr_t)base;
  v0 = (f32x4){0, 0, 0, 0}; v1 = v0;
#pragma unroll
  for (int ww = 0; ww < 4; ++ww) { v0 += *(LAS f32x4*)(p + IMG + ww * 8704); v1 += *(LAS f32x4*)(p + IMG + ww * 8704 + 16); }
}
#define UOFF 240
#define UOFF_LAST 320
#define USLEEP 0
#define UPACE 0
constexpr unsigned CTL_UT = 65536 + 16384;
constexpr unsigned L_UT = 69632;
DEV void role_u(const Args& a, rsrc_t rs, int cq, int wave, int lane, unsigned pol) {
  const int tid = wave * 64 + lane;
  const unsigned l16 = lane * 16;
  u32x4 w[56];
  auto ticket = [&]() -> unsigned {
    unsigned t = 0;
    if (lane == 0) t = __hip_atomic_fetch_add((__attribute__((address_space(1))) unsigned*)(a.ws + OFF_CTL + CTL_UT + cq * 128), 1u, __ATOMIC_RELAXED, __HIP_MEMORY_SCOPE_AGENT);
    return t;
  };
  load_w2(w, a.w2 + cq * 128, wave, lane);
  BAR();
  if (wave == 0) { const unsigned t = ticket(); if (lane == 0) ldsw32(L_UT, t); }
  BAR();
  const int seg0 = __builtin_amdgcn_readfirstlane(*(volatile LAS unsigned*)(uintptr_t)L_UT);
  if (seg0 >= NSEG) return;
  EV(7);
  const int nks = wave < 3 ? 14 : 12;
  const unsigned sca = wave * 8704 + (lane >> 4) * 4 * 272 + (lane & 15) * 4, gaa = (tid >> 3) * 272 + (tid & 7) * 32;
  int budget = SPIN_CAP;
  Sched sc; clk_init(sc);
  auto rep_vo = [&](int seg) { return (lane >= 16 && lane < (seg == NSEG - 1 ? 32 : 24)) ? OFF_REP + ((seg + 1) * 128 + ((lane >> 3) & 1) * 32 + wave * 8 + (lane & 7)) * 128 : OOB; };
  auto chk_vo = [&](int seg) {
    const bool past = sc.j0 != 0 && (int)(now8() - tau_of(sc, 2 * seg + 3)) > 200 * 8;
    return lane < nks ? hprog_off(seg & 1, wave * 14 + lane) : lane == 14 ? OFF_CTL + CTL_RL + seg * 128 : past ? rep_vo(seg) : OOB;
  };
  auto need_of = [&](int seg) -> unsigned { return lane < nks ? (seg >> 1) + 1 : lane == 14 ? 1 : 0; };
  u32x4 hf[14][2];
  auto start = [&](int seg, unsigned& have, bool on) {
    const unsigned need = need_of(seg);
    while (!__all(have >= need) && --budget > 0) { __builtin_amdgcn_s_sleep(16); have = ld32(rs, chk_vo(seg), 0); CFENCE(); }
    const unsigned hb = h_base(seg, pol), va = on ? l16 : OOB, vb = (on && wave < 3) ? l16 : OOB;
#pragma unroll
    for (int k = 0; k < 14; ++k)
#pragma unroll
      for (int tt = 0; tt < 2; ++tt) hf[k][tt] = ld128(rs, (k < 12 ? va : vb) + tt * 1024, hb + spread((wave * 14 + k) * 2048));
    CFENCE();
  };
  adopt_cur(sc, ld128(rs, 0, OFF_CTL + CTL_CUR), 2 * NSEG);
  unsigned have = ld32(rs, chk_vo(seg0), 0);
  start(seg0, have, true);
  const unsigned tvo = tid == 0 ? OFF_CTL + CTL_UT + cq * 128 : OOB;
  for (int seg = seg0;;) {
    const bool last = seg == NSEG - 1;
    const unsigned rvo = rep_vo(seg);
    const bool ract = rvo != OOB, chain_ok = __all(!ract || have != 0);
    unsigned tk;
    f32x4 acc[4][2];
    u32x4 bx[4][8];
    unsigned ba[4];
#pragma unroll
    for (int c = 0; c < 4; ++c) ba[c] = b_addr(seg, cq * 4 + c, pol) + wave * BSTR;
    static_for<112>([&](auto N) {
      constexpr int n = N, k = n >> 3, c = (n >> 1) & 3, wi = ((k >> 1) * 2 + (c >> 1)) * 2 + (c & 1);
      mf_w<wi, k & 1, false, k == 0>(acc[c][n & 1], w, hf[k][n & 1]);
      if constexpr (n == 2) { CFENCE(); tk = __builtin_amdgcn_raw_ptr_buffer_atomic_add_i32(chain_ok ? 1 : 0, rs, tvo, 0, 0); CFENCE(); }
      if constexpr (n % 7 == 1 || n % 7 == 4) {
        constexpr int q = (n / 7) * 2 + (n % 7 == 4), cc = q >> 3, hh = q & 7;
        CFENCE(); bx[cc][hh] = ld128(rs, l16 + (hh & 3) * 1024, ba[cc] + (hh & 4) * 1024); CFENCE();
      }
    });
    int nxt; bool late;
    f32x4 g0, g1;
    auto gate = [&]() {
      if (tid == 0) ldsw64(L_UT + 8, (u32x2){tk, chain_ok ? 0u : 1u});
      BAR();
      const u32x2 t = *(volatile LAS u32x2*)(uintptr_t)(L_UT + 8);
      nxt = __builtin_amdgcn_readfirstlane(t.x); late = __builtin_amdgcn_readfirstlane(t.y);
      u_gather<0>(g0, g1, gaa);
#pragma unroll
      for (int r = 0; r < 4; ++r) { g0[r] = rbf(silu(rbf(g0[r]))); g1[r] = rbf(silu(rbf(g1[r]))); }
    };
    const unsigned so = pt_base(seg, pol), zo = z_base(seg, pol) + cq * 65536 + wave * 2048;
    const unsigned oo = (seg * 32 + (tid >> 3)) * 1280 + cq * 128 + (tid & 7) * 16;
    u32x4 px[8][2], xv;
    acc_wait(acc);
#pragma unroll
    for (int hh = 0; hh < 8; ++hh) asm volatile("" : "+a"(bx[0][hh]));
    if (!chain_ok) {
      u_scatter<0, 0, 4>(acc, sca);
      gate();
      sleep_until_turn(rs, sc, last ? 2 * seg + 1 : 2 * seg + 3, last ? UOFF_LAST : UOFF);
      for (;;) { const unsigned v = ld32(rs, rvo, 0); CFENCE(); if (__all(!ract || v != 0) || --budget <= 0) break; if (last) __builtin_amdgcn_s_sleep(2); else __builtin_amdgcn_s_sleep(16); }
    }
    EV(8);
    static_for<4>([&](auto C) {
#pragma unroll
      for (int q = 0; q < 4; ++q) px[2 * C + (q >> 1)][q & 1] = ld128(rs, l16 + (q & 1) * 1024, so + wave * 32768 + spread((2 * C + (q >> 1)) * 2048));
      CFENCE();
      if (chain_ok) u_scatter<0, C, C + 1>(acc, sca);
      CFENCE();
    });
    if (chain_ok) gate();
    const bool more = !late && nxt < NSEG;
    const int pre = more ? nxt : seg;
    have = ld32(rs, chk_vo(pre), 0);
    bool on = false;
    unsigned va = OOB, vb = OOB;
    const unsigned hb = h_base(pre, pol);
    {
      u32x4 pm[2][2], z[4][2], cur;
      static_for<80>([&](auto N) {
        constexpr int n = N, hh = n >> 3, c = (n >> 1) & 3, tt = n & 1;
        if constexpr (n < 8) { if constexpr (c == 0) mf_va0(acc[c][tt], px[0][tt], bx[c][0]); else mf_vv0(acc[c][tt], px[0][tt], bx[c][0]); }
        else if constexpr (hh < 8) { if constexpr (c == 0) mf_va(acc[c][tt], px[hh][tt], bx[c][hh]); else mf_vv(acc[c][tt], px[hh][tt], bx[c][hh]); }
        else mf_vv(acc[c][tt], pm[hh - 8][tt], z[c][hh - 8]);
        if constexpr ((n & 1) == 1) {
          constexpr int q = n >> 1;
          CFENCE();
          if constexpr (q < 4) pm[q >> 1][q & 1] = ld128(rs, l16 + (q >> 1) * 1024, so + (8 + (q & 1)) * 16384 + wave * 2048);
          else if constexpr (q < 12) z[(q - 4) >> 1][q & 1] = ld128(rs, l16 + (q & 1) * 1024, zo + ((q - 4) >> 1) * 16384);
          else if constexpr (q == 12) { cur = ld128(rs, 0, OFF_CTL + CTL_CUR); xv = *(const u32x4*)(a.x + oo); }
          else if constexpr (q == 13) { on = more && __all(have >= need_of(pre)); va = on ? l16 : OOB; vb = (on && wave < 3) ? l16 : OOB; }
          else if constexpr (q - 14 < 26) hf[(q - 14) >> 1][q & 1] = ld128(rs, ((q - 14) < 24 ? va : vb) + (q & 1) * 1024, hb + spread((wave * 14 + ((q - 14) >> 1)) * 2048));
          CFENCE();
        }
      });
      hf[13][0] = ld128(rs, vb, hb + spread((wave * 14 + 13) * 2048)); hf[13][1] = ld128(rs, vb + 1024, hb + spread((wave * 14 + 13) * 2048));
      adopt_cur(sc, cur, 2 * NSEG);
    }
    acc_wait(acc);
    u_scatter<34816, 0, 4>(acc, sca);
    BAR();
    f32x4 s0, s1;
    u_gather<34816>(s0, s1, gaa);
    u32x4 o;
#pragma unroll
    for (int j = 0; j < 2; ++j) {
      o[j] = pk(__builtin_bit_cast(float, xv[j] << 16) + rbf(rbf(s0[2 * j]) * g0[2 * j]), __builtin_bit_cast(float, xv[j] & 0xFFFF0000u) + rbf(rbf(s0[2 * j + 1]) * g0[2 * j + 1]));
      o[2 + j] = pk(__builtin_bit_cast(float, xv[2 + j] << 16) + rbf(rbf(s1[2 * j]) * g1[2 * j]), __builtin_bit_cast(float, xv[2 + j] & 0xFFFF0000u) + rbf(rbf(s1[2 * j + 1]) * g1[2 * j + 1]));
    }
    *(u32x4*)(a.out + oo) = o;
    EV(9);
    if (nxt >= NSEG) return;
    if (late) {
      if (wave == 0) { const unsigned t = ticket(); if (lane == 0) ldsw32(L_UT, t); }
      BAR();
      nxt = __builtin_amdgcn_readfirstlane(*(volatile LAS unsigned*)(uintptr_t)L_UT);
      if (nxt >= NSEG) return;
      have = ld32(rs, chk_vo(nxt), 0);
    }
    if (!on) start(nxt, have, true);
    seg = nxt;
  }
}

#define WDELAY 18
#define WPACE 0
#define WBLAG 1
#define WAUX 16
#define WAHEAD 3
DEV void role_w(rsrc_t rs, int xq, int k, int wave, int lane, unsigned pol) {
  const int cb0 = 10 * xq + 2 * k;
  unsigned sink = 0;
  int budget = SPIN_CAP;
  Sched sc; clk_init(sc);
  for (int seg = 1 + wave; seg < NSEG; seg += 4) {
    const int s = seg - WAHEAD;
    if (s >= 2) {
      while (sc.j0 == 0 && --budget > 0) { adopt_cur(sc, ld128(rs, 0, OFF_CTL + CTL_CUR), 2 * NSEG); CFENCE(); __builtin_amdgcn_s_sleep(8); }
      sleep_until_turn(rs, sc, 2 * s + 1, WDELAY * 8 - 240);
    }
    while (__builtin_amdgcn_readfirstlane(ld32(rs, seg * 128, OFF_CTL + CTL_RL)) == 0 && --budget > 0) { CFENCE(); __builtin_amdgcn_s_sleep(8); }
    if (s >= 2) wait_until(tau_of(sc, 2 * s + 1) + WDELAY * 8);
#define WLANES 64
    constexpr int NI = 64 / WLANES;
    unsigned v[12 * NI];
    const unsigned wvo = lane < WLANES ? lane * 128 : OOB;
#pragma unroll
    for (int j = 0; j < 12 * NI; ++j) {
      const int b = j / NI, u = min(k + 5 * (b - 8), 15);
      const unsigned so = b < 8 ? b_addr(max(seg - WBLAG, 0), cb0 + (b >> 2), pol) + (b & 3) * BSTR : kq_addr(u & 1, seg, (u >> 1) * 4 + xq, pol);
      v[j] = __builtin_amdgcn_raw_buffer_load_b32(rs, wvo, so + (j % NI) * WLANES * 128, WAUX); CFENCE(); __builtin_amdgcn_s_sleep(WPACE);
    }
#pragma unroll
    for (int j = 0; j < 12 * NI; ++j) sink += v[j];
    CFENCE();
  }
  asm volatile("" :: "v"(sink));
}

#define LEAD1 78
#define LEAD2 60
#define GEXTRA 300
#define ATOL 8
#define NADAPT 4
#define TKLAG 900
#define TKOFF 200
#define TKSLEEP 2
#define GPOLL 2600
#define GPOLL0 2700
DEV void role_tk(rsrc_t rs, int lane) {
  Sched sc; clk_init(sc);
  Sched old = sc;
  int budget = SPIN_CAP, n1c = 0, n2c = 0, m1 = 0, m2 = 0, relock = 0, jp1 = -9, jp2 = -9;
  unsigned Mp1 = 0, Mp2 = 0;
  bool locked = false;
  const unsigned tref = now8();
  auto tau_at = [&](int j) { return sc.j0 <= j ? tau_of(sc, j) : tau_of(old, j); };
  auto publish = [&](int j) {
    st128(rs, (u32x4){(unsigned)sc.j0, sc.base, (unsigned)sc.T, (unsigned)sc.D1 | (unsigned)sc.rx << 16}, lane < 8 ? lane * 16 : OOB, OFF_CTL + CTL_CUR);
    n1c = n2c = m1 = m2 = 0;
  };
  for (int j = 0; j < 2 * NSEG - 6; ++j) {
    if (locked) {
      while ((int)(now8() - tau_at(j + 2)) > TKOFF - 160) ++j;
      if (j >= 2 * NSEG - 6) break;
      wait_until(tau_at(j + 1) + TKOFF);
    }
    const unsigned vo = rep_off(j, lane);
    u32x4 e;
    for (;;) { e = ld128(rs, vo, OFF_REP); CFENCE(); if (__all(e.x != 0) || --budget <= 0) break; __builtin_amdgcn_s_sleep(TKSLEEP); }
    const bool zrow = !(j & 1) && lane >= 32;
    const unsigned M = tref + (unsigned)wmax((int)(e.y - tref) - (zrow ? VSLACK : 0));
    locked = __all(e.z & 1) && j >= relock;
    if (j + 1 >= relock) {
      const bool off = sc.j0 == 0 || (int)(M - tau_at(j + 1)) > LATE_THR || (j >= relock && !__any(e.z & 1));
      if (off) locked = false;
      if (off && (int)(now8() - M) < TKLAG) {
        int gap = sc.j0 == 0 ? GPOLL0 : GPOLL;
        if (j <= relock && jp2 == j - 2) gap = max(gap, min((int)(M - Mp2) + GEXTRA, 3600));
        sc.rx = j + 3;
        sc.base += M + gap - tau_of(sc, j + 3); sc.j0 = relock = j + 3;
        publish(j);
      }
    }
    Mp2 = Mp1; Mp1 = M; jp2 = jp1; jp1 = j;
    if (locked && sc.j0 <= j && __all((e.z & 3) == 3)) {
      const int d = wmax((int)((float)(int)(e.z >> 2) * 2048.f * __builtin_amdgcn_rcpf((float)e.w)) - (zrow ? VSLACK : 0));
      if (j & 1) { if (n2c < NADAPT) { m2 += d; ++n2c; } } else if (n1c < NADAPT) { m1 += d; ++n1c; }
      if (n1c == NADAPT && n2c == NADAPT) {
        const int n1 = m1 / NADAPT + LEAD1, n2 = m2 / NADAPT + LEAD2, sn = (j >> 1) + 3;
        if (abs(n1 - sc.D1) > ATOL || abs(n2 - (sc.T - sc.D1)) > ATOL) {
          old = sc;
          sc.base += sn * (sc.T - (n1 + n2)); sc.T = n1 + n2; sc.D1 = n1; sc.j0 = 2 * sn;
          publish(j);
        }
        n1c = n2c = m1 = m2 = 0;
      }
    }
  }
}

DEV bool run_roles(Args a, Boot& bt, int b, int ix, int wave, int lane) {
    PIN(lane); PINS(wave); PINS(ix); PINS(a.ws); PINS(a.x); PINS(a.wq); PINS(a.wk); PINS(a.wv); PINS(a.wo); PINS(a.w1); PINS(a.w3); PINS(a.w2); PINS(a.out);
    const rsrc_t rs = __builtin_amdgcn_make_buffer_rsrc(a.ws, 0, WS_BYTES, 0x00020000);
    const int xq = (b + bt.xoff) & 7;
    if (xq < 4) {
      const int r = ix * 4 + xq;
      if (r < 32) { if (!role_p(a, rs, r, wave, lane, bt)) return false; }
      else if (r < 64) { if (!role_v(a, rs, r - 32, wave, lane, bt)) return false; }
      else {
        if (r >= 124) {
          int extra = 0;
          if (wave == 0) extra = boot_probe(rs, r - 124, lane);
          BAR();
          if (!boot_hint(rs, bt, true)) return false;
          if (wave == 0) st128(rs, (u32x4){bt.t0, bt.t1, (unsigned)extra, 0}, lane < 8 ? (r - 64) * 128 + lane * 16 : OOB, OFF_CTL + CTL_XP);
        }
        u32x4 xv[2][5];
        ln_load(a, xv, r - 64, wave, lane);
        if (!boot_hint(rs, bt, true)) return false;
        if (r < 124) boot_init(rs, r - 64, wave, lane);
        ln_compute(xv, 0, wave, lane);
        ln_publish(rs, 0, r - 64, wave, lane, bt);
        EV(1);
        if (r >= 126 || (r >= 124 && wave > 0)) return true;
        boot_wait(rs, bt, lane);
        if (r < 104) role_c(a, rs, 10 * xq + ((r - 64) >> 2), wave, lane, bt.pol);
        else if (r < 124) role_w(rs, xq, (r - 104) >> 2, wave, lane, bt.pol);
        else if (r == 124) { role_tk(rs, lane); st64(rs, (u32x2){bt.t0, bt.t1}, lane < 16 ? lane * 8 : OOB, OFF_CTL + CTL_EP); }
        else role_agg(rs, lane);
      }
    } else {
      const int pid = (xq - 4) * 16 + (ix >> 1), par = ix & 1;
      const int h = pid >> 1;
      int cq;
      if (pid & 1) {
        if (!role_x<K_VB>(a, rs, h, par, NSEG, NSEG, prog_off(1, h, par, wave), wave, lane, bt)) return false;
        const int v = h * 2 + par;
        cq = v < 50 ? v % 10 : 4 + (v - 50) % 6;
      } else {
        if (!role_x<K_QK>(a, rs, h, par, 2 * QKN, par ? 2 * QKN : NSEG, prog_off(0, h, par, wave), wave, lane, bt)) return false;
        if (par && h < 28) role_x<K_F1>(a, rs, (h % 14) * 4 + wave, h / 14, NSEG, NSEG, hprog_off(h / 14, (h % 14) * 4 + wave), wave, lane, bt);
        cq = par ? (h < 28 ? (h + 6) % 10 : h - 28) : h % 10;
      }
      role_u(a, rs, cq, wave, lane, bt.pol);
    }
    return true;
}

__global__ void __launch_bounds__(256, 1) k_main(Args a) {
  int lane = threadIdx.x & 63, wave = __builtin_amdgcn_readfirstlane(threadIdx.x >> 6);
  const rsrc_t rs = __builtin_amdgcn_make_buffer_rsrc(a.ws, 0, WS_BYTES, 0x00020000);
  EV(10);
  {
    unsigned long long pc; unsigned junk; int len;
    asm volatile("s_getpc_b64 %0\ncode_here_%=:\n\ts_mov_b32 %1, code_end_mark-code_here_%=" : "=s"(pc), "=s"(len));
    const unsigned off = min((int)((blockIdx.x >> 3) * 64 + lane) * 128, max(len, 0));
    if (wave == 3) asm volatile("global_load_dword %0, %1, %2\n\ts_waitcnt vmcnt(0)" : "=&v"(junk) : "v"(off), "s"(pc) : "memory");
  }
  const int b = blockIdx.x;
  int ix = b >> 3;
  Boot bt;
  {
    const u32x2 ep = ld64(rs, 0, OFF_CTL + CTL_EP);
    bt.t0 = __builtin_amdgcn_readfirstlane(ep.x) + 1; bt.t1 = __builtin_amdgcn_readfirstlane(ep.y);
    bt.xoff = (xcc_id() - b) & 7; bt.pol = 0;
    if (b == 0 && wave == 0) st128(rs, (u32x4){bt.t0, bt.t1, bt.xoff, 0}, lane < 8 ? lane * 16 : OOB, OFF_CTL + CTL_HINT);
  }
  EV(0);
  if (__builtin_expect(!run_roles(a, bt, b, ix, wave, lane), 0)) run_roles(a, bt, b, ix, wave, lane);
}
__global__ void k_end() { asm volatile("code_end_mark:"); }

extern "C" void kernel_launch(void* const* d_in, const int* in_sizes, int n_in, void* d_out, int out_size, void* d_ws, size_t ws_size, hipStream_t stream) {
  Args a;
  a.x = (const unsigned char*)d_in[0]; a.mem0 = (const unsigned char*)d_in[1]; a.wq = (const unsigned char*)d_in[2]; a.wk = (const unsigned char*)d_in[3];
  a.wv = (const unsigned char*)d_in[4]; a.wo = (const unsigned char*)d_in[5]; a.w1 = (const unsigned char*)d_in[8]; a.w3 = (const unsigned char*)d_in[9];
  a.w2 = (const unsigned char*)d_in[10]; a.out = (unsigned char*)d_out; a.ws = (unsigned char*)d_ws;
  (void)hipFuncSetAttribute((const void*)k_main, hipFuncAttributeMaxDynamicSharedMemorySize, 163840);
  k_main<<<256, 256, 163840, stream>>>(a);
}
```

```cpp
#include <hip/hip_runtime.h>
#include <stdint.h>

typedef __bf16 bf16x8 __attribute__((ext_vector_type(8)));
typedef __bf16 bf16x16 __attribute__((ext_vector_type(16)));
typedef __bf16 bf16x2 __attribute__((ext_vector_type(2)));
typedef float f32x2 __attribute__((ext_vector_type(2)));
typedef float f32x4 __attribute__((ext_vector_type(4)));
typedef unsigned u32x2 __attribute__((ext_vector_type(2)));
typedef unsigned u32x4 __attribute__((ext_vector_type(4)));
typedef unsigned u32x8 __attribute__((ext_vector_type(8)));
typedef short s16x4 __attribute__((ext_vector_type(4)));
#define LAS __attribute__((address_space(3)))
#define GAS __attribute__((address_space(1)))
#define DEV __device__ __forceinline__

constexpr int NSEG = 64;
constexpr unsigned MB = 1u << 20;
constexpr unsigned OFF_COMM = 0;
constexpr unsigned OFF_CTL = 4 * MB;
constexpr unsigned OFF_XN = 5 * MB;
constexpr unsigned OFF_BLK = 32 * MB;
constexpr unsigned OFF_DBG = 28 * MB;
constexpr unsigned WS_BYTES = 256 * MB;
constexpr unsigned OOB = 0x80000000u;
constexpr unsigned C_MEM1 = 16384, C_PM = 16384 + 2048, C_PX = 2 * 16384, C_Z = 4 * 16384;
constexpr unsigned CTL_POL = 65536;
constexpr unsigned CTL_RL = 65536 + 4096;
constexpr unsigned CTL_HP = 131072;
constexpr unsigned SENT = 0xFFFFFFFFu;
constexpr int SPIN_CAP = 1 << 20;
constexpr int SC1 = 16;

typedef __amdgpu_buffer_rsrc_t rsrc_t;
DEV u32x4 ld128(rsrc_t r, unsigned vo, unsigned so) { return __builtin_amdgcn_raw_buffer_load_b128(r, vo, so, SC1); }
DEV u32x2 ld64(rsrc_t r, unsigned vo, unsigned so) { return __builtin_amdgcn_raw_buffer_load_b64(r, vo, so, SC1); }
DEV unsigned ld32(rsrc_t r, unsigned vo, unsigned so) { return __builtin_amdgcn_raw_buffer_load_b32(r, vo, so, SC1); }
DEV void st128(rsrc_t r, u32x4 v, unsigned vo, unsigned so) { __builtin_amdgcn_raw_buffer_store_b128(v, r, vo, so, SC1); }
#define BST_AUX 16
#define BLD_AUX 16
#define KST_AUX 16
template <int AUX> DEV void st128a(rsrc_t r, u32x4 v, unsigned vo, unsigned so) { __builtin_amdgcn_raw_buffer_store_b128(v, r, vo, so, AUX); }
template <int AUX> DEV u32x4 ld128a(rsrc_t r, unsigned vo, unsigned so) { return __builtin_amdgcn_raw_buffer_load_b128(r, vo, so, AUX); }
DEV void st64(rsrc_t r, u32x2 v, unsigned vo, unsigned so) { __builtin_amdgcn_raw_buffer_store_b64(v, r, vo, so, SC1); }
DEV void st32(rsrc_t r, unsigned v, unsigned vo, unsigned so) { __builtin_amdgcn_raw_buffer_store_b32(v, r, vo, so, SC1); }
DEV unsigned pk(float a, float b) { bf16x2 v = {(__bf16)a, (__bf16)b}; return __builtin_bit_cast(unsigned, v); }
DEV unsigned pks(float a, float b) { return min(pk(a, b), 0xFFFFFFFEu); }
DEV unsigned mx4(u32x4 v) { return max(max(v.x, v.y), max(v.z, v.w)); }
DEV f32x4 mfma(u32x4 a, u32x4 b, f32x4 c) { return __builtin_amdgcn_mfma_f32_16x16x32_bf16(__builtin_bit_cast(bf16x8, a), __builtin_bit_cast(bf16x8, b), c, 0, 0, 0); }
DEV f32x4 smf(u32x4 a, u32x8 b, f32x4 c, int idx) { return __builtin_amdgcn_smfmac_f32_16x16x64_bf16(__builtin_bit_cast(bf16x8, a), __builtin_bit_cast(bf16x16, b), c, idx, 0, 0); }
DEV u32x4 lo4(u32x8 v) { return __builtin_shufflevector(v, v, 0, 1, 2, 3); }
DEV u32x4 hi4(u32x8 v) { return __builtin_shufflevector(v, v, 4, 5, 6, 7); }
DEV u32x8 cat8(u32x4 a, u32x4 b) { return __builtin_shufflevector(a, b, 0, 1, 2, 3, 4, 5, 6, 7); }
DEV u32x4 cat4(u32x2 a, u32x2 b) { return __builtin_shufflevector(a, b, 0, 1, 2, 3); }
DEV u32x2 trread(unsigned lds_addr) { return __builtin_bit_cast(u32x2, __builtin_amdgcn_ds_read_tr16_b64_v4i16((LAS s16x4*)(uintptr_t)lds_addr)); }
DEV u32x4 ldsr128(unsigned a) { return *(LAS u32x4*)(uintptr_t)a; }
DEV void ldsw128(unsigned a, u32x4 v) { *(LAS u32x4*)(uintptr_t)a = v; }
DEV void ldsw64(unsigned a, u32x2 v) { *(LAS u32x2*)(uintptr_t)a = v; }
#define BAR() asm volatile("s_waitcnt lgkmcnt(0)\n\ts_barrier" ::: "memory")
#define VMWAIT(n) asm volatile("s_waitcnt vmcnt(" #n ")" ::: "memory")
#define LGWAIT() asm volatile("s_waitcnt lgkmcnt(0)" ::: "memory")
#define CFENCE() asm volatile("" ::: "memory")
template <int N> DEV void vmwait() { asm volatile("s_waitcnt vmcnt(%0)" :: "n"(N) : "memory"); }
DEV void glds(const void* sbase, unsigned voff, unsigned ldst) {
  unsigned keep;
  asm volatile("s_mov_b32 %0, m0\n\ts_mov_b32 m0, %3\n\ts_nop 0\n\tglobal_load_lds_dwordx4 %1, %2\n\ts_mov_b32 m0, %0" : "=&s"(keep) : "v"(voff), "s"(sbase), "s"(ldst) : "memory");
}
constexpr unsigned CTL_XP = 196608;
DEV void glds_sc1(const void* sbase, unsigned voff, unsigned ldst) {
  unsigned keep;
  asm volatile("s_mov_b32 %0, m0\n\ts_mov_b32 m0, %3\n\ts_nop 0\n\tglobal_load_lds_dwordx4 %1, %2 sc1\n\ts_mov_b32 m0, %0" : "=&s"(keep) : "v"(voff), "s"(sbase), "s"(ldst) : "memory");
}
DEV void set_m0(unsigned v) { asm volatile("s_mov_b32 m0, %0" :: "s"(v) : "memory"); }
template <int OFF> DEV void glds_off(const void* sbase, unsigned voff) { asm volatile("global_load_lds_dwordx4 %0, %1 offset:%2 sc1" :: "v"(voff), "s"(sbase), "n"(OFF) : "memory"); }
template <int V> struct IC { static constexpr int value = V; constexpr operator int() const { return V; } };
template <int N, int I = 0, class F> DEV void static_for(F&& f) { if constexpr (I < N) { f(IC<I>{}); static_for<N, I + 1>(f); } }
DEV float fold32(float a, float b) {
  auto r = __builtin_amdgcn_permlane32_swap(__builtin_bit_cast(unsigned, a), __builtin_bit_cast(unsigned, b), false, false);
  return __builtin_bit_cast(float, (unsigned)r[0]) + __builtin_bit_cast(float, (unsigned)r[1]);
}
DEV float xmax16(float v) { auto r = __builtin_amdgcn_permlane16_swap(__builtin_bit_cast(unsigned, v), __builtin_bit_cast(unsigned, v), false, false); return fmaxf(__builtin_bit_cast(float, (unsigned)r[0]), __builtin_bit_cast(float, (unsigned)r[1])); }
DEV float xmax32(float v) { auto r = __builtin_amdgcn_permlane32_swap(__builtin_bit_cast(unsigned, v), __builtin_bit_cast(unsigned, v), false, false); return fmaxf(__builtin_bit_cast(float, (unsigned)r[0]), __builtin_bit_cast(float, (unsigned)r[1])); }
DEV float xadd16(float v) { auto r = __builtin_amdgcn_permlane16_swap(__builtin_bit_cast(unsigned, v), __builtin_bit_cast(unsigned, v), false, false); return __builtin_bit_cast(float, (unsigned)r[0]) + __builtin_bit_cast(float, (unsigned)r[1]); }
DEV float xadd32(float v) { auto r = __builtin_amdgcn_permlane32_swap(__builtin_bit_cast(unsigned, v), __builtin_bit_cast(unsigned, v), false, false); return __builtin_bit_cast(float, (unsigned)r[0]) + __builtin_bit_cast(float, (unsigned)r[1]); }
DEV float bf2f(unsigned short u) { return __builtin_bit_cast(float, (unsigned)u << 16); }
DEV float blo(unsigned u) { return __builtin_bit_cast(float, u << 16); }
DEV float bhi(unsigned u) { return __builtin_bit_cast(float, u & 0xFFFF0000u); }
DEV float rbf(float x) { return (float)(__bf16)x; }

DEV void smf_a(f32x4& c, u32x4 a, const u32x8& w, int idx) { asm volatile("v_smfmac_f32_16x16x64_bf16 %0, %1, %2, %3" : "+v"(c) : "v"(a), "a"(w), "v"(idx)); }
DEV void smf_v(f32x4& c, u32x4 a, const u32x8& w, int idx) { asm volatile("v_smfmac_f32_16x16x64_bf16 %0, %1, %2, %3" : "+v"(c) : "v"(a), "v"(w), "v"(idx)); }
template <int I> DEV void smf_w(f32x4& c, u32x4 a, const u32x8 (&w)[40], int idx) { if constexpr (I < 32) smf_a(c, a, w[I], idx); else smf_v(c, a, w[I], idx); }
DEV void mf_va0(f32x4& c, u32x4 a, u32x4 b) { asm volatile("v_mfma_f32_16x16x32_bf16 %0, %2, %1, 0" : "=&v"(c) : "v"(a), "a"(b)); }
DEV void mf_vv0(f32x4& c, u32x4 a, u32x4 b) { asm volatile("v_mfma_f32_16x16x32_bf16 %0, %1, %2, 0" : "=&v"(c) : "v"(a), "v"(b)); }
DEV void mf_va(f32x4& c, u32x4 a, u32x4 b) { asm volatile("v_mfma_f32_16x16x32_bf16 %0, %2, %1, %0" : "+v"(c) : "v"(a), "a"(b)); }
DEV void mf_vv(f32x4& c, u32x4 a, u32x4 b) { asm volatile("v_mfma_f32_16x16x32_bf16 %0, %1, %2, %0" : "+v"(c) : "v"(a), "v"(b)); }
template <bool A> DEV void pin_w(u32x8& x, u32x8& y) {
  if constexpr (A) asm volatile("s_waitcnt lgkmcnt(0)" : "+a"(x), "+a"(y) :: "memory"); else asm volatile("s_waitcnt lgkmcnt(0)" : "+v"(x), "+v"(y) :: "memory");
}
template <bool A> DEV void pin_w4(u32x4& x, u32x4& y, u32x4& z, u32x4& u) {
  if constexpr (A) asm volatile("s_waitcnt lgkmcnt(0)" : "+a"(x), "+a"(y), "+a"(z), "+a"(u) :: "memory"); else asm volatile("s_waitcnt lgkmcnt(0)" : "+v"(x), "+v"(y), "+v"(z), "+v"(u) :: "memory");
}
DEV void pin2(f32x4& x, f32x4& y) { asm volatile("" : "+v"(x), "+v"(y)); }
DEV void pin2n(f32x4& x, f32x4& y) { asm volatile("s_nop 3" : "+v"(x), "+v"(y)); }
DEV unsigned pin1(unsigned x) { asm volatile("" :: "v"(x)); return x; }
#define NOP(n) asm volatile("s_nop " #n)
#define PIN(x) asm volatile("" : "+v"(x))
#define PINS(x) asm volatile("" : "+s"(x))
DEV unsigned xcc_id() { return (unsigned)__builtin_amdgcn_s_getreg(20 | (3 << 11)) & 0xfu; }
DEV unsigned rtc() { unsigned long long t; asm volatile("s_memrealtime %0\n\ts_waitcnt lgkmcnt(0)" : "=s"(t) :: "memory"); return (unsigned)t; }
constexpr unsigned CTL_CUR = 65536 + 12288 + 1024;
constexpr unsigned OFF_REP = 8 * MB;
constexpr int MAXWAIT = 400 * 8;
#define D1_INIT 828
#define D2_INIT 500
struct Sched { int j0, jbad, rx; unsigned base; int T, D1; unsigned c0, v0; int R; unsigned ct, tt; };
DEV unsigned now8() { return rtc() << 3; }
DEV unsigned cycles() { unsigned long long t; asm volatile("s_memtime %0\n\ts_waitcnt lgkmcnt(0)" : "=s"(t) :: "memory"); return (unsigned)t; }
DEV void clk_init(Sched& c) { c.j0 = 0; c.jbad = 0; c.rx = 0; c.base = 0; c.T = D1_INIT + D2_INIT; c.D1 = D1_INIT; c.v0 = rtc(); c.c0 = cycles(); c.R = 24 * 256; c.ct = c.c0; c.tt = 0; }
DEV void clk_update(Sched& c) {
  const unsigned v = rtc() - c.v0, y = cycles() - c.c0;
  if (v > 300) c.R = __builtin_amdgcn_readfirstlane((int)((float)y * 256.f * __builtin_amdgcn_rcpf((float)v)));
}
#define RELAX 120
#define NRELAX0 6
#define RX0 4
#define NRELAX 2
DEV unsigned tau_of(const Sched& c, int j) { return c.base + (j >> 1) * c.T + ((j & 1) ? c.D1 : 0) + RELAX * min(max(j - c.rx, 0), c.rx <= RX0 ? NRELAX0 : NRELAX); }
DEV void adopt_cur(Sched& c, u32x4 v, int j) {
  const int j0 = __builtin_amdgcn_readfirstlane(v.x);
  if (j0 > c.j0 && j0 <= j) { c.j0 = j0; c.base = __builtin_amdgcn_readfirstlane(v.y); c.T = __builtin_amdgcn_readfirstlane(v.z); const int w = __builtin_amdgcn_readfirstlane(v.w); c.D1 = w & 0xffff; c.rx = w >> 16; }
}
DEV bool wait_until(unsigned tau) {
  int d = (int)(tau - now8());
  if (d <= 0 || d > MAXWAIT) return false;
  while ((int)(tau - now8()) > 0) {}
  return true;
}
#define NOP16 "s_nop 0\n\ts_nop 0\n\ts_nop 0\n\ts_nop 0\n\ts_nop 0\n\ts_nop 0\n\ts_nop 0\n\ts_nop 0\n\ts_nop 0\n\ts_nop 0\n\ts_nop 0\n\ts_nop 0\n\ts_nop 0\n\ts_nop 0\n\ts_nop 0\n\ts_nop 0\n\t"
#define FK 96
DEV unsigned est_cycle(unsigned tau, int R, unsigned c0, int best) {
  unsigned long long v, c; unsigned t; int rem;
  asm volatile(
    "1:\n\t"
    "s_memrealtime %0\n\ts_memtime %1\n\ts_waitcnt lgkmcnt(0)\n\t"
    "s_lshl_b32 %2, s94, 3\n\t"
    "s_sub_i32 %2, %5, %2\n\t"
    "s_mul_i32 %2, %2, %6\n\t"
    "s_ashr_i32 %2, %2, 11\n\t"
    "s_sub_i32 %3, s96, %7\n\t"
    "s_add_i32 %2, %2, %3\n\t"
    "s_min_i32 %4, %4, %2\n\t"
    "s_sub_i32 %2, %2, 100\n\t"
    "s_max_i32 %4, %4, %2\n\t"
    "s_sub_i32 %3, %4, %3\n\t"
    "s_cmp_gt_i32 %3, 520\n\t"
    "s_cbranch_scc1 1b"
    : "=&{s[94:95]}"(v), "=&{s[96:97]}"(c), "=&s"(t), "=&s"(rem), "+s"(best)
    : "s"(tau), "s"(R), "s"(c0) : "memory", "scc");
  return c0 + best;
}
DEV void release(unsigned cs) {
  unsigned long long c, pc; int rem;
  asm volatile(
    "1:\n\t"
    "s_memtime %0\n\ts_waitcnt lgkmcnt(0)\n\t"
    "s_sub_i32 %2, %3, s96\n\t"
    "s_cmp_gt_i32 %2, %4\n\t"
    "s_cbranch_scc1 1b\n\t"
    "s_sub_i32 %2, %2, %5\n\t"
    "s_max_i32 %2, %2, 0\n\t"
    "s_and_b32 %2, %2, -4\n\t"
    "s_sub_i32 %2, 140, %2\n\t"
    "s_getpc_b64 %1\n\t"
    "s_add_u32 s98, s98, %2\n\t"
    "s_addc_u32 s99, s99, 0\n\t"
    "s_setpc_b64 %1\n\t"
    NOP16 NOP16 "s_barrier"
    : "=&{s[96:97]}"(c), "=&{s[98:99]}"(pc), "=&s"(rem)
    : "s"(cs), "n"(FK + 124), "n"(FK) : "memory", "scc");
}
#define DRIFT 10
#define CSLEEP 3
#define LATE_THR 120
constexpr unsigned L_TM = 61440;
DEV void mark_overrun(Sched& c, int j, unsigned t) { if ((int)(t - tau_of(c, j + 1)) > LATE_THR + 40) c.jbad = c.j0; }
template <class F>
DEV unsigned trigger(rsrc_t rs, Sched& c, int j, unsigned hold, F&& poll) {
  unsigned cs, timed = 0;
  for (;;) {
    const unsigned tau = tau_of(c, j), now = now8();
    const int d = (int)(tau - now);
    const bool ok = c.j0 > c.jbad && d > -30 * 8 && d <= MAXWAIT;
    if (ok && d <= 0) { cs = cycles(); break; }
    if (ok && d <= 50 * 8) {
      const unsigned dt = tau - c.tt;
      const int prior = dt < 4096 ? (int)((dt * (unsigned)c.R) >> 11) + DRIFT : 0x7fffffff;
      cs = est_cycle(__builtin_amdgcn_readfirstlane(tau), __builtin_amdgcn_readfirstlane(c.R), __builtin_amdgcn_readfirstlane(c.ct), __builtin_amdgcn_readfirstlane(prior));
      c.ct = cs; c.tt = tau; timed = 1;
      break;
    }
    if (!ok && (unsigned)(hold - now) > (unsigned)MAXWAIT) { poll(); cs = cycles(); break; }
    if (d > 100 * 8 || !ok) { adopt_cur(c, ld128(rs, 0, OFF_CTL + CTL_CUR), j); CFENCE(); __builtin_amdgcn_s_sleep(CSLEEP); }
  }
  ldsw64(L_TM + (j & 2) * 8, (u32x2){cs, timed | (unsigned)c.R << 2});
  return __builtin_amdgcn_readfirstlane(cs);
}
#define STAMP(k)
#define STV(k) stdummy
#define STAMP_DECL unsigned stdummy
#define STAMP_OUT(e, s)

#define BMODE 0
#define KQMODE 0
constexpr unsigned BSTR = BMODE == 2 ? 8192 : 16384;
constexpr int ZBLK0 = BMODE == 0 ? 0 : BMODE == 1 ? 10240 : 5376;
DEV unsigned blk_addr(int blk, unsigned pol, unsigned far) { return OFF_BLK + blk * 16384 + (((pol >> ((blk >> 7) & 1)) ^ far) & 1) * 8192; }
DEV unsigned z_base(int s, unsigned pol) { const int pg = s / 3; return blk_addr(ZBLK0 + pg * 128 + (s - 3 * pg) * 40, pol, 0); }
DEV unsigned kq_addr(int isq, int seg, int h, unsigned pol) {
  if (KQMODE == 2) return OFF_BLK + (ZBLK0 + 3072) * 16384 + (isq * 2048 + seg * 32 + h) * 8192;
  return BMODE == 1 ? blk_addr(isq * 2048 + seg * 32 + h, pol, 1) : blk_addr(ZBLK0 + 3072 + isq * 2048 + seg * 32 + h, pol, 0);
}
DEV unsigned b_addr(int seg, int cb, unsigned pol) { return BMODE == 2 ? OFF_BLK + (seg * 40 + cb) * 32768 : blk_addr((seg * 40 + cb) * 4, pol, 1 - BMODE); }

#define HFAR 1
DEV constexpr unsigned spread(unsigned o) { return (o >> 13) * 16384 + (o & 8191); }
DEV unsigned h_base(int seg, unsigned pol) { return blk_addr(10240 + seg * 32, pol, HFAR); }
DEV unsigned pt_base(int seg, unsigned pol) { return blk_addr(10240 + seg * 32 + 16, pol, HFAR); }
#define EV(slot)
struct Args {
  const unsigned char* x; const unsigned char* mem0; const unsigned char* wq; const unsigned char* wk; const unsigned char* wv; const unsigned char* wo;
  const unsigned char* w1; const unsigned char* w3; const unsigned char* w2; unsigned char* out; unsigned char* ws;
};

constexpr unsigned CTL_EP = CTL_XP + 8192 + 512, CTL_HINT = CTL_EP + 128;
struct Boot { unsigned t0, t1, xoff, pol; };
DEV bool boot_hint(rsrc_t rs, Boot& bt, bool block) {
  for (int budget = SPIN_CAP; budget > 0; --budget) {
    const u32x4 v = ld128(rs, 0, OFF_CTL + CTL_HINT);
    CFENCE();
    if (__builtin_amdgcn_readfirstlane(v.x) == bt.t0 && __builtin_amdgcn_readfirstlane(v.y) == bt.t1) {
      const unsigned x = __builtin_amdgcn_readfirstlane(v.z) & 7;
      const bool same = x == bt.xoff;
      bt.xoff = x;
      return same;
    }
    if (!block) break;
    __builtin_amdgcn_s_sleep(2);
  }
  return true;
}
DEV bool boot_wait(rsrc_t rs, Boot& bt, int lane) {
  if (!boot_hint(rs, bt, true)) return false;
  u32x4 v;
  for (int budget = SPIN_CAP; budget > 0; --budget) {
    v = ld128(rs, lane * 128, OFF_CTL + CTL_XP);
    CFENCE();
    if (__all(v.x == bt.t0 && v.y == bt.t1)) break;
    __builtin_amdgcn_s_sleep(4);
  }
  const int d0 = __builtin_amdgcn_readlane(v.z, 60) + __builtin_amdgcn_readlane(v.z, 61), d1 = __builtin_amdgcn_readlane(v.z, 62) + __builtin_amdgcn_readlane(v.z, 63);
  bt.pol = d0 > d1 ? 1u : 2u;
  return true;
}
DEV int boot_probe(rsrc_t rs, int j, int lane) {
  const unsigned base = OFF_COMM + (j >> 1) * 2 * MB + (48 + (j & 1)) * 16384;
  st128(rs, (u32x4){1, 2, 3, 4}, (lane >> 5) * 8192 + (lane & 31) * 16, base);
  VMWAIT(0);
  unsigned best[2] = {~0u, ~0u};
#pragma unroll
  for (int r = 0; r < 6; ++r) {
    unsigned long long c0, c1; unsigned v;
    asm volatile("s_memtime %0\n\ts_waitcnt lgkmcnt(0)" : "=s"(c0) :: "memory");
    v = ld32(rs, (r & 1) * 8192 + (r >> 1) * 128, base);
    asm volatile("s_waitcnt vmcnt(0)\n\ts_memtime %0\n\ts_waitcnt lgkmcnt(0)" : "=s"(c1) : "v"(v) : "memory");
    best[r & 1] = min(best[r & 1], (unsigned)(c1 - c0));
  }
  return min(max((int)best[0] - (int)best[1], -300), 300);
}
DEV void boot_init(rsrc_t rs, int k, int wave, int lane) {
#pragma unroll
  for (int i = 0; i < 10; ++i) {
    const unsigned p = (k * 4 + wave) + i * 240;
    const unsigned so = p < 1024 ? OFF_COMM + (p >> 8) * MB + (p & 255) * 1024 : p < 1216 ? OFF_CTL + (p - 1024) * 1024 : OFF_REP + (p - 1216) * 1024;
    const unsigned f = p < 1024 ? SENT : 0u;
    st128(rs, (u32x4){f, f, f, f}, p < 2256 ? lane * 16 : OOB, so);
  }
}

DEV float rowsum16(float v) {
  v += __builtin_bit_cast(float, __builtin_amdgcn_update_dpp(0, __builtin_bit_cast(int, v), 0xB1, 0xf, 0xf, false));
  v += __builtin_bit_cast(float, __builtin_amdgcn_update_dpp(0, __builtin_bit_cast(int, v), 0x4E, 0xf, 0xf, false));
  v += __builtin_bit_cast(float, __builtin_amdgcn_update_dpp(0, __builtin_bit_cast(int, v), 0x124, 0xf, 0xf, false));
  v += __builtin_bit_cast(float, __builtin_amdgcn_update_dpp(0, __builtin_bit_cast(int, v), 0x128, 0xf, 0xf, false));
  return v;
}
DEV void ln_load(const Args& a, u32x4 (&v)[2][5], int seg, int wave, int lane) {
#pragma unroll
  for (int p = 0; p < 2; ++p)
#pragma unroll
    for (int i = 0; i < 5; ++i) v[p][i] = *(const GAS u32x4*)(a.x + (size_t)(seg * 32 + wave * 8 + p * 4 + (lane >> 4)) * 1280 + ((lane & 15) + 16 * i) * 16);
}
DEV void ln_compute(const u32x4 (&v)[2][5], unsigned dst, int wave, int lane) {
  const int sub = lane & 15;
#pragma unroll
  for (int p = 0; p < 2; ++p) {
    float f[40], sum = 0.f;
#pragma unroll
    for (int i = 0; i < 5; ++i)
#pragma unroll
      for (int j = 0; j < 4; ++j) { f[i * 8 + 2 * j] = __builtin_bit_cast(float, v[p][i][j] << 16); f[i * 8 + 2 * j + 1] = __builtin_bit_cast(float, v[p][i][j] & 0xFFFF0000u); sum += f[i * 8 + 2 * j] + f[i * 8 + 2 * j + 1]; }
    const float mean = rowsum16(sum) * (1.f / 640.f);
    float sq = 0.f;
#pragma unroll
    for (int i = 0; i < 40; ++i) { const float d = f[i] - mean; sq = fmaf(d, d, sq); }
    const float mb = rbf(mean), vb = rbf(rowsum16(sq) * (1.f / 640.f));
    const float inv = 1.f / rbf(sqrtf(rbf(vb + 1e-5f)));
    const int row = wave * 8 + p * 4 + (lane >> 4);
#pragma unroll
    for (int i = 0; i < 5; ++i) {
      u32x4 o;
#pragma unroll
      for (int j = 0; j < 4; ++j) o[j] = pk(rbf(f[i * 8 + 2 * j] - mb) * inv, rbf(f[i * 8 + 2 * j + 1] - mb) * inv);
      const int c = sub + 16 * i;
      ldsw128(dst + ((c >> 2) * 2 + (row >> 4)) * 1024 + ((c & 3) * 16 + (row & 15)) * 16, o);
    }
  }
}
DEV void ln_publish(rsrc_t rs, unsigned src, int seg, int wave, int lane, const Boot& bt) {
  BAR();
#pragma unroll
  for (int i = 0; i < 10; ++i) st128(rs, ldsr128(src + (wave * 10 + i) * 1024 + lane * 16), lane * 16, OFF_XN + seg * 40960 + (wave * 10 + i) * 1024);
  VMWAIT(0);
  BAR();
  if (wave == 0) st128(rs, (u32x4){bt.t0, bt.t1, 0, 0}, lane < 8 ? (seg < 60 ? seg : seg + 4) * 128 + lane * 16 : OOB, OFF_CTL + CTL_XP);
}

enum { K_P, K_V, K_QK, K_VB, K_F1 };
constexpr unsigned L_XB0 = 120 * 1024, L_XB1 = 80 * 1024;
template <int KIND, class WT, class LATE>
DEV void load_weights(WT& w, const unsigned char* baseA, const unsigned char* baseB, int wave, int lane, int cmask, LATE&& late) {
  constexpr int NG = 20;
  constexpr bool WO = KIND == K_V || KIND == K_VB;
  constexpr unsigned SA = KIND == K_F1 ? 3456 : 8192, SB = WO ? 1280 : SA;
  constexpr int MA = (KIND == K_P || KIND == K_V) ? 2 : 0, MBB = KIND == K_P ? 2 : KIND == K_VB ? 1 : 0;
  const int gB = lane >> 4, q = (lane >> 2) & 3, pp = lane & 3;
  unsigned vo[2][4], rd[2][2];
#pragma unroll
  for (int m = 0; m < 2; ++m) {
    const int mode = m ? MBB : MA;
#pragma unroll
    for (int p = 0; p < 4; ++p) {
      const int key = mode == 0 ? (gB | (p >> 1) << 2) : mode == 1 ? (gB | (p & 1) << 2) : ((gB >> 1) | p << 1);
      vo[m][p] = (wave * 16 + p * 4 + gB) * (m ? SB : SA) + (((lane & 15) ^ (key << 1)) & cmask) * 16;
    }
    const int key = mode == 2 ? ((gB & 1) | (q >> 1) << 1 | (q & 1) << 2) : (q | (gB & 1) << 2);
    const int row = mode == 0 ? 8 * gB + q : mode == 1 ? 4 * gB + q : 16 * (gB >> 1) + 4 * (q >> 1) + 2 * (gB & 1) + 8 * (q & 1);
#pragma unroll
    for (int nt = 0; nt < 2; ++nt) rd[m][nt] = row * 256 + ((4 * wave + 2 * nt + (pp >> 1)) ^ (key << 1)) * 16 + (pp & 1) * 8;
  }
  auto issue = [&](int g) {
    const unsigned char* src = g < 10 ? baseA + (size_t)g * 64 * SA : WO ? baseB + (size_t)((g - 10) & 1) * 64 * SB + ((g - 10) >> 1) * 256 : baseB + (size_t)(g - 10) * 64 * SB;
#pragma unroll
    for (int p = 0; p < 4; ++p) glds(src, vo[g >= 10][p], (g % 7) * 16384 + wave * 4096 + p * 1024);
  };
#pragma unroll
  for (int g = 0; g < 7; ++g) issue(g);
  static_for<NG>([&](auto G) {
    constexpr int g = G, mode = g < 10 ? MA : MBB;
    if constexpr (g == 0) vmwait<24>(); else if constexpr (g < NG - 5) vmwait<20>(); else if constexpr (g == NG - 5) late();
    if constexpr (g <= NG - 5) BAR();
    if constexpr (g >= 1 && g + 6 < NG) issue(g + 6);
    constexpr unsigned phs = mode == 2 ? 256 : mode == 1 ? 16 * 256 : 4 * 256, sl = (g % 7) * 16384;
#pragma unroll
    for (int nt = 0; nt < 2; ++nt) {
      const unsigned rb = rd[g >= 10][nt] + sl;
      u32x2 a = trread(rb), b = trread(rb + phs), c = trread(rb + 8192), d = trread(rb + 8192 + phs);
      if constexpr (KIND == K_P || KIND == K_V) w[g * 2 + nt] = cat8(cat4(a, b), cat4(c, d));
      else { w[g * 4 + nt * 2] = cat4(a, b); w[g * 4 + nt * 2 + 1] = cat4(c, d); }
    }
    if constexpr (KIND == K_P || KIND == K_V) pin_w<g < 16>(w[g * 2], w[g * 2 + 1]);
    else pin_w4<g < 16>(w[g * 4], w[g * 4 + 1], w[g * 4 + 2], w[g * 4 + 3]);
  });
}
DEV void load_w2(u32x4 (&w)[56], const unsigned char* base, int wave, int lane) {
  const int gB = lane >> 4, q = (lane >> 2) & 3, pp = lane & 3;
  const unsigned ring = wave * 32768;
  const unsigned vo = (lane >> 3) * 1280 + ((lane & 7) ^ (((lane >> 4) & 3) << 1)) * 16;
  unsigned rd[4];
#pragma unroll
  for (int c = 0; c < 4; ++c) rd[c] = ring + (4 * gB + q) * 128 + ((2 * c + (pp >> 1)) ^ ((((gB & 1) << 1) | (q >> 1)) << 1)) * 16 + (pp & 1) * 8;
  auto issue = [&](int g) {
    const unsigned char* src = base + (size_t)min(wave * 7 + g, 26) * 64 * 1280;
#pragma unroll
    for (int p = 0; p < 8; ++p) glds(src + p * 8 * 1280, vo, ring + (g & 3) * 8192 + p * 1024);
  };
#pragma unroll
  for (int g = 0; g < 4; ++g) issue(g);
  static_for<7>([&](auto G) {
    constexpr int g = G;
    vmwait<8 * (6 - g < 3 ? 6 - g : 3)>();
#pragma unroll
    for (int c = 0; c < 4; ++c) {
      const unsigned rb = rd[c] + (g & 3) * 8192;
      w[g * 8 + c * 2] = cat4(trread(rb), trread(rb + 2048)); w[g * 8 + c * 2 + 1] = cat4(trread(rb + 4096), trread(rb + 6144));
    }
    pin_w4<true>(w[g * 8], w[g * 8 + 1], w[g * 8 + 2], w[g * 8 + 3]); pin_w4<true>(w[g * 8 + 4], w[g * 8 + 5], w[g * 8 + 6], w[g * 8 + 7]);
    if constexpr (g + 4 < 7) issue(g + 4);
  });
}

#define QKN 8
#define VBN 16
DEV int first_open(int type, int par, int c) { const int n = type ? VBN : QKN; return c < n ? 2 * c + par : par ? NSEG : c + n; }
DEV unsigned prog_off(int type, int h, int par, int wave) { return OFF_CTL + (((type * 32 + h) * 2 + par) * 4 + wave) * 128; }
DEV unsigned hprog_off(int rep, int slice) { return OFF_CTL + CTL_HP + (rep * 56 + slice) * 128; }
DEV float silu(float x) { return x * __builtin_amdgcn_rcpf(1.f + __builtin_amdgcn_exp2f(-1.4426950408889634f * x)); }
DEV u32x2 pk4(f32x4 v) { return (u32x2){pk(v[0], v[1]), pk(v[2], v[3])}; }

template <int I, int HI, bool WA, bool FIRST, int N>
DEV void mf_w(f32x4& c, const u32x4 (&w)[N], u32x4 x) {
  const u32x4 f = w[2 * I + HI];
  if constexpr (FIRST) {
    if constexpr (I < 32) { if constexpr (WA) asm volatile("v_mfma_f32_16x16x32_bf16 %0, %1, %2, 0" : "=&v"(c) : "a"(f), "v"(x)); else asm volatile("v_mfma_f32_16x16x32_bf16 %0, %2, %1, 0" : "=&v"(c) : "a"(f), "v"(x)); }
    else { if constexpr (WA) asm volatile("v_mfma_f32_16x16x32_bf16 %0, %1, %2, 0" : "=&v"(c) : "v"(f), "v"(x)); else asm volatile("v_mfma_f32_16x16x32_bf16 %0, %2, %1, 0" : "=&v"(c) : "v"(f), "v"(x)); }
  } else {
    if constexpr (I < 32) { if constexpr (WA) asm volatile("v_mfma_f32_16x16x32_bf16 %0, %1, %2, %0" : "+v"(c) : "a"(f), "v"(x)); else asm volatile("v_mfma_f32_16x16x32_bf16 %0, %2, %1, %0" : "+v"(c) : "a"(f), "v"(x)); }
    else { if constexpr (WA) asm volatile("v_mfma_f32_16x16x32_bf16 %0, %1, %2, %0" : "+v"(c) : "v"(f), "v"(x)); else asm volatile("v_mfma_f32_16x16x32_bf16 %0, %2, %1, %0" : "+v"(c) : "v"(f), "v"(x)); }
  }
}

DEV void issue_x(const unsigned char* ws, int seg, unsigned buf, int wave, int lane) {
#pragma unroll
  for (int i = 0; i < 10; ++i) glds_sc1(ws + OFF_XN + (size_t)seg * 40960 + (wave * 10 + i) * 1024, lane * 16, buf + (wave * 10 + i) * 1024);
}
template <int NT, bool NEXT, class SIDE>
DEV void xproj(f32x4 (&acc)[NT][2], const u32x4 (&w)[80], u32x4 (&xr)[4][2], unsigned xa, unsigned xn, SIDE&& side) {
  static_for<20>([&](auto K) {
    constexpr int ks = K;
    static_for<2 * NT>([&](auto J) {
      constexpr int j = J, t = j >> 1, wi = ((t >> 1) * 10 + (ks >> 1)) * 2 + (t & 1);
      mf_w<wi, ks & 1, true, ks == 0>(acc[t][j & 1], w, xr[ks & 3][j & 1]);
      if constexpr (j == 1 || j == NT / 2 + 1) {
        constexpr int tt = j == 1 ? 0 : 1;
        if constexpr (ks < 18) { CFENCE(); xr[(ks + 2) & 3][tt] = ldsr128(xa + (ks + 2) * 2048 + tt * 1024); CFENCE(); }
        else if constexpr (NEXT) { CFENCE(); xr[ks - 18][tt] = ldsr128(xn + (ks - 18) * 2048 + tt * 1024); CFENCE(); }
      }
      side(IC<ks * 2 * NT + j>{});
    });
  });
}
template <int NT> DEV void acc_wait(f32x4 (&c)[NT][2]) {
  if constexpr (NT == 4) asm volatile("s_nop 7" : "+v"(c[0][0]), "+v"(c[0][1]), "+v"(c[1][0]), "+v"(c[1][1]), "+v"(c[2][0]), "+v"(c[2][1]), "+v"(c[3][0]), "+v"(c[3][1]));
  else asm volatile("s_nop 7" : "+v"(c[0][0]), "+v"(c[0][1]), "+v"(c[1][0]), "+v"(c[1][1]));
}
#define NFAST 2
DEV void pinf(float& x) { asm volatile("" : "+v"(x)); }
DEV void pinu(unsigned& x) { asm volatile("" : "+v"(x)); }

template <int KIND>
DEV bool role_x(const Args& a, rsrc_t rs, int id, int seg0, int sw, int lim, unsigned po, int wave, int lane, Boot& bt) {
  u32x4 w[80];
  bool agreed = true;
  auto late = [&]() {
    if constexpr (KIND != K_F1) agreed = boot_wait(rs, bt, lane);
    VMWAIT(0);
    issue_x(a.ws, seg0, L_XB0, wave, lane);
  };
  if constexpr (KIND == K_QK) load_weights<K_QK>(w, a.wq + id * 256, a.wk + id * 256, wave, lane, 15, late);
  else if constexpr (KIND == K_F1) load_weights<K_F1>(w, a.w1 + (id >> 2) * 256, a.w3 + (id >> 2) * 256, wave, lane, id >= 52 ? 7 : 15, late);
  else load_weights<K_VB>(w, a.wv + id * 256, a.wo + (size_t)id * 128 * 1280, wave, lane, 15, late);
  VMWAIT(0);
  BAR();
  if (!agreed) return false;
  const unsigned pol = bt.pol;
  EV(KIND == K_F1 ? 4 : 1);
  const unsigned l16 = lane * 16, fvo = lane < 32 ? po + lane * 4 : OOB;
  const unsigned svo = KIND == K_VB ? l16 + (id >> 3) * BSTR + (id & 7) * 1024 : (KIND == K_F1 && id > 53) ? OOB : l16;
  const unsigned hpos = spread(((id / 14) * 16 + id % 14) * 2048);
  u32x4 xr[4][2];
#pragma unroll
  for (int k = 0; k < 2; ++k) { xr[k][0] = ldsr128(L_XB0 + l16 + k * 2048); xr[k][1] = ldsr128(L_XB0 + l16 + k * 2048 + 1024); }
  int it = 0, prev = seg0;
  unsigned pvo = OOB;
  u32x4 pk4b[4];
  f32x4 c[3][2];
  u32x4 vf[4][2], o;
#pragma unroll
  for (int i = 0; i < 4; ++i) pk4b[i] = (u32x4){0, 0, 0, 0};
#pragma unroll
  for (int i = 0; i < 3; ++i) { c[i][0] = (f32x4){0, 0, 0, 0}; c[i][1] = c[i][0]; }
  float e0, e1, g0, g1;
  auto out_prev = [&](auto N) {
    constexpr int n = N;
    if constexpr (KIND == K_QK) {
      if constexpr (n < 4) { CFENCE(); st128(rs, pk4b[n], pvo, kq_addr(1 - (n >> 1), prev, id, pol) + ((n & 1) * 4 + wave) * 1024); CFENCE(); }
    } else if constexpr (KIND == K_F1) {
      constexpr int p = n / 10, st = n % 10, tt = p >> 2, q = p & 3;
      if constexpr (p < 8) {
        if constexpr (st == 0) { g0 = __builtin_bit_cast(float, pk4b[tt][q] << 16); g1 = __builtin_bit_cast(float, pk4b[tt][q] & 0xFFFF0000u); pinf(g0); pinf(g1); }
        if constexpr (st == 1) { e0 = -1.4426950408889634f * g0; e1 = -1.4426950408889634f * g1; pinf(e0); pinf(e1); }
        if constexpr (st == 2) { e0 = __builtin_amdgcn_exp2f(e0); pinf(e0); }
        if constexpr (st == 3) { e1 = __builtin_amdgcn_exp2f(e1); pinf(e1); }
        if constexpr (st == 4) { e0 += 1.f; e1 += 1.f; pinf(e0); pinf(e1); }
        if constexpr (st == 5) { e0 = __builtin_amdgcn_rcpf(e0); pinf(e0); }
        if constexpr (st == 6) { e1 = __builtin_amdgcn_rcpf(e1); pinf(e1); }
        if constexpr (st == 7) { unsigned u = pk(g0 * e0, g1 * e1); pinu(u); pk4b[tt][q] = u; }
        if constexpr (st == 8) { g0 = __builtin_bit_cast(float, pk4b[tt][q] << 16) * __builtin_bit_cast(float, pk4b[2 + tt][q] << 16); pinf(g0); }
        if constexpr (st == 9) {
          unsigned u = pk(g0, __builtin_bit_cast(float, pk4b[tt][q] & 0xFFFF0000u) * __builtin_bit_cast(float, pk4b[2 + tt][q] & 0xFFFF0000u)); pinu(u); pk4b[tt][q] = u;
          if constexpr (q == 3) { CFENCE(); st128(rs, pk4b[tt], pvo, h_base(prev, pol) + hpos + tt * 1024); CFENCE(); }
        }
      }
    }
  };
  auto vb_ep = [&](auto CT, auto KS, int seg, unsigned vo) {
    constexpr int ct = CT, ks = KS, e = ct % 3;
    if constexpr (ks == 0) pin2(c[e][0], c[e][1]);
    o[ks] = pk(c[e][ks >> 1][2 * (ks & 1)], c[e][ks >> 1][2 * (ks & 1) + 1]); pin1(o[ks]);
    if constexpr (ks == 3) { CFENCE(); st128(rs, o, vo, b_addr(seg, (ct >> 1) * 8 + wave * 2 + (ct & 1), pol)); CFENCE(); }
  };
  auto vb_p2 = [&](int seg, unsigned vo, auto&& side) {
    static_for<10>([&](auto CT) {
      static_for<4>([&](auto KS) {
        constexpr int ct = CT, ks = KS, wi = 20 + ((ct >> 1) * 2 + (ks >> 1)) * 2 + (ct & 1);
        mf_w<wi, ks & 1, false, ks == 0>(c[ct % 3][0], w, vf[ks][0]);
        side(IC<ct * 4 + ks>{});
        mf_w<wi, ks & 1, false, ks == 0>(c[ct % 3][1], w, vf[ks][1]);
        if constexpr (ct >= 2) vb_ep(IC<ct - 2>{}, KS, seg, vo);
      });
    });
  };
  for (int seg = seg0, nxt; seg < lim; seg = nxt, ++it) {
    nxt = seg + (seg < sw ? 2 : 1);
    if (it == 4)
      for (int budget = SPIN_CAP; budget > 0; --budget) { const u32x2 v = ld64(rs, (64 + (lane & 3)) * 128, OFF_CTL + CTL_XP); CFENCE(); if (__all(v.x == bt.t0 && v.y == bt.t1)) break; }
    const unsigned buf = (it & 1) ? L_XB1 : L_XB0, nbuf = L_XB0 + L_XB1 - buf;
    const unsigned char* xsrc = a.ws + OFF_XN + (size_t)(nxt < lim ? nxt : seg) * 40960 + wave * 10240;
    const unsigned xdst = nbuf + wave * 10240;
    const unsigned char *xs0 = xsrc, *xs1 = xsrc + 4096, *xs2 = xsrc + 8192;
    PINS(xs0); PINS(xs1); PINS(xs2);
    auto dma = [&](auto K) {
      if constexpr (K % 5 == 0 && K <= 10) set_m0(xdst + (K / 5) * 4096);
      if constexpr (K % 5 != 0 && K <= 12) glds_off<(K % 5 - 1) * 1024>(K < 5 ? xs0 : K < 10 ? xs1 : xs2, l16);
    };
    if constexpr (KIND != K_VB) {
      f32x4 acc[4][2];
      xproj<4, true>(acc, w, xr, buf + l16, nbuf + l16, [&](auto S) {
        constexpr int ks = S / 8, j = S % 8;
        if constexpr (j == 5) dma(IC<ks>{});
        if constexpr (KIND == K_QK && j == 7) out_prev(IC<ks>{});
        if constexpr (KIND == K_F1 && (j == 0 || j == 2 || j == 4 || j >= 6)) out_prev(IC<ks * 5 + (j < 6 ? j / 2 : j - 3)>{});
        if constexpr (KIND == K_QK && ks == 11 && j == 7) { vmwait<6>(); st32(rs, it, it > NFAST ? fvo : OOB, 0); }
        if constexpr (ks == 18 && j == 0) {
          if constexpr (KIND == K_QK) { vmwait<0>(); BAR(); } else { vmwait<1>(); BAR(); st32(rs, it - 1, it > 1 ? fvo : OOB, 0); }
        }
      });
      acc_wait(acc);
#pragma unroll
      for (int m = 0; m < 2; ++m)
#pragma unroll
        for (int tt = 0; tt < 2; ++tt) pk4b[2 * m + tt] = cat4(pk4(acc[2 * m][tt]), pk4(acc[2 * m + 1][tt]));
      prev = seg; pvo = svo;
      if (KIND == K_QK && it < NFAST) {
        static_for<4>([&](auto N) { out_prev(N); });
        vmwait<0>(); st32(rs, it + 1, fvo, 0); pvo = OOB; if (it == 0) EV(2);
      }
    } else {
      f32x4 acc[2][2];
      xproj<2, false>(acc, w, xr, buf + l16, 0, [&](auto S) {
        constexpr int ks = S / 4, j = S % 4;
        if constexpr (j == 0 && ks >= 2) dma(IC<ks - 2>{});
        if constexpr (j == 3 && ks < 8) vb_ep(IC<8 + ks / 4>{}, IC<ks % 4>{}, prev, pvo);
        if constexpr (j == 3 && ks == 17) { vmwait<6>(); st32(rs, it, it > NFAST ? fvo : OOB, 0); }
      });
      acc_wait(acc);
#pragma unroll
      for (int tt = 0; tt < 2; ++tt) ldsw128(l16 + (wave * 2 + tt) * 1024, cat4(pk4(acc[0][tt]), pk4(acc[1][tt])));
      BAR();
#pragma unroll
      for (int k = 0; k < 8; ++k) vf[k >> 1][k & 1] = ldsr128(l16 + k * 1024);
      prev = seg; pvo = svo;
      vb_p2(seg, svo, [&](auto S) {
        constexpr int n = S;
        if constexpr (n == 32) { vmwait<6>(); BAR(); }
        if constexpr (n >= 33 && n < 37) { CFENCE(); xr[(n - 33) >> 1][(n - 33) & 1] = ldsr128(nbuf + l16 + (n - 33) * 1024); CFENCE(); }
      });
      if (it < NFAST) {
        NOP(7);
        static_for<8>([&](auto N) { vb_ep(IC<8 + N / 4>{}, IC<N % 4>{}, prev, pvo); });
        vmwait<0>(); st32(rs, it + 1, fvo, 0); pvo = OOB; if (it == 0) EV(2);
      }
    }
  }
  if constexpr (KIND == K_QK) static_for<4>([&](auto N) { out_prev(N); });
  else if constexpr (KIND == K_F1) static_for<80>([&](auto N) { out_prev(N); });
  else {
    NOP(7);
    static_for<8>([&](auto N) { vb_ep(IC<8 + N / 4>{}, IC<N % 4>{}, prev, pvo); });
  }
  VMWAIT(0);
  st32(rs, it, fvo, 0);
  EV(KIND == K_F1 ? 6 : 3);
  BAR();
  return true;
}

constexpr int REPL = 4;
DEV unsigned slot_base(int s, unsigned pol) { return OFF_COMM + (s & 3) * MB + ((pol >> ((s >> 1) & 1)) & 1) * 8192; }
DEV int wmax(int v) {
  v = max(v, __builtin_amdgcn_update_dpp(v, v, 0xB1, 0xf, 0xf, false));
  v = max(v, __builtin_amdgcn_update_dpp(v, v, 0x4E, 0xf, 0xf, false));
  v = max(v, __builtin_amdgcn_update_dpp(v, v, 0x124, 0xf, 0xf, false));
  v = max(v, __builtin_amdgcn_update_dpp(v, v, 0x128, 0xf, 0xf, false));
  return max(max(__builtin_amdgcn_readlane(v, 0), __builtin_amdgcn_readlane(v, 16)), max(__builtin_amdgcn_readlane(v, 32), __builtin_amdgcn_readlane(v, 48)));
}
DEV int wmin(int v) { return -wmax(-v); }
DEV void report(rsrc_t rs, int e, int s, unsigned flags, int lane) {
  unsigned long long v, c;
  asm volatile("s_memrealtime %0\n\ts_memtime %1\n\ts_waitcnt lgkmcnt(0)" : "=s"(v), "=s"(c) :: "memory");
  const u32x2 tm = *(volatile LAS u32x2*)(uintptr_t)(L_TM + (s & 1) * 16);
  st128(rs, (u32x4){(unsigned)s + 1, (unsigned)v << 3, (((unsigned)c - tm.x) << 2) | flags | (tm.y & 1), tm.y >> 2}, lane < 8 ? lane * 16 : OOB, OFF_REP + (s * 128 + e) * 128);
}
DEV void sleep_until_turn(rsrc_t rs, Sched& c, int j, int off) {
  for (;;) {
    adopt_cur(c, ld128(rs, 0, OFF_CTL + CTL_CUR), j + 1); CFENCE();
    const int d = (int)(tau_of(c, j) + off - now8());
    if (c.j0 == 0 || d > 200 * MAXWAIT) return;
    if (d < 150 * 8) { wait_until(tau_of(c, j) + off); return; }
    if (d > 500 * 8) __builtin_amdgcn_s_sleep(127); else __builtin_amdgcn_s_sleep(40);
  }
}
DEV int probe_seg(int s, int lane) { return min(s + (3 << (lane & 3)), NSEG - 1); }
DEV unsigned probe_ld(rsrc_t rs, int s, int lane) { return ld32(rs, lane < 4 ? probe_seg(s, lane) * 128 : OOB, OFF_CTL + CTL_RL); }
DEV int probe_rdy(unsigned v, int s, int lane) { return wmax(v != 0 ? probe_seg(s, lane) + 1 : 0); }
DEV void role_agg(rsrc_t rs, int lane) {
  int done = 0, budget = SPIN_CAP;
  while (done < NSEG && --budget > 0) {
    int m = NSEG;
#pragma unroll
    for (int i = 0; i < 8; ++i) m = min(m, first_open(i >> 2, (lane >> 2) & 1, ld32(rs, (i * 64 + lane) * 128, OFF_CTL)));
    CFENCE();
    m = min(wmin(m), NSEG);
    for (; done < m; done = min(done + 8, m)) st128(rs, (u32x4){1, 1, 1, 1}, done + (lane >> 3) < m ? done * 128 + lane * 16 : OOB, OFF_CTL + CTL_RL);
    __builtin_amdgcn_s_sleep(16);
  }
}
DEV unsigned rep_off(int j, int lane) { return ((j >> 1) * 128 + ((j & 1) ? 64 + min(lane, 39) : lane)) * 128; }
#define PTSLEEP 2
#define PSLEEP 2
#define HOLDP 720
#define HOLDC 1080
#define NOPM_(n) asm volatile("s_nop " #n ::: "memory")
#define NOPM(n) NOPM_(n)
#define VSLACK 48
#define PACE 11
#define NOPV(n, x) asm volatile("s_nop " #n : "+v"(x))
template <int N> DEV void zero_acc(f32x4 (&c)[N]) {
#pragma unroll
  for (int t = 0; t < N; ++t) { c[t] = (f32x4){0, 0, 0, 0}; PIN(c[t]); }
}
DEV unsigned memoff(int j) { return j < 8 ? j * 1024 : C_MEM1 + (j - 8) * 1024; }
DEV bool has_nan(f32x4 v) { return __any(__builtin_isunordered(v[0], v[1]) || __builtin_isunordered(v[2], v[3])); }
template <int N> DEV bool has_sent(const u32x4 (&a)[N]) {
  unsigned m = 0;
#pragma unroll
  for (int k = 0; k < N; ++k) m = max(m, mx4(a[k]));
  return __any(m == SENT);
}
DEV int mrow(int i, int half) { return 4 * (i >> 1) + 2 * half + (i & 1); }
DEV unsigned foldpk(f32x4 v) { return pk(v[0] + v[2], v[1] + v[3]); }
DEV void ldsw32(unsigned a, unsigned v) { *(LAS unsigned*)(uintptr_t)a = v; }
DEV void mem_wait(rsrc_t rs, unsigned sb, int lane, int& budget) {
  for (;;) {
    const unsigned u = ld32(rs, lane * 128, sb), v = ld32(rs, C_MEM1 + (lane & 15) * 128, sb);
    CFENCE();
    if (__all(u != SENT && v != SENT) || --budget <= 0) break;
    __builtin_amdgcn_s_sleep(PSLEEP);
  }
}
template <int NT, int NK>
DEV void project(rsrc_t rs, const unsigned (&vo)[10], unsigned sb, const u32x8 (&w)[40], int idx, u32x4 (&am)[10], f32x4 (&acc)[NT]) {
#pragma unroll
  for (int k = 0; k < 8; ++k) {
    am[k] = ld128(rs, vo[k], sb);
    if (k < 7) NOPM(PACE); else CFENCE();
  }
  static_for<NK>([&](auto K) {
    constexpr int ks = K;
    smf_w<ks * 2>(acc[0], am[ks], w, idx); smf_w<ks * 2 + 1>(acc[1], am[ks], w, idx);
    if constexpr (ks < 2) { CFENCE(); am[8 + ks] = ld128(rs, vo[8 + ks], sb); CFENCE(); }
    if constexpr (NT == 4) { smf_w<20 + ks * 2>(acc[2], am[ks], w, idx); smf_w<20 + ks * 2 + 1>(acc[3], am[ks], w, idx); }
  });
}
#define DPL " row_ror:8 row_mask:0xf bank_mask:0x3\n\t"
#define DPH " row_ror:8 row_mask:0xf bank_mask:0xc\n\t"
#define DPA " quad_perm:[2,3,0,1] row_mask:0xf bank_mask:0xf\n\t"
#define FADD2(d0, d1, c) asm volatile("v_add_f32 %0, %2, %4\n\tv_add_f32 %1, %3, %5" : "=&v"(d0), "=&v"(d1) : "v"(c[0]), "v"(c[1]), "v"(c[2]), "v"(c[3]))

constexpr unsigned P_RING = 16384;
#define ROWRED(op) "s_nop 1\n\t" op "_dpp %7, %6, %6" DPA op "_dpp %8, %6, %6" DPA "s_nop 1\n\tv_permlane16_swap_b32 %7, %8\n\t" op " %6, %7, %8\n\t" op " %7, %7, %8\n\t" \
                   "s_nop 1\n\tv_permlane32_swap_b32 %6, %7\n\t" op " %6, %6, %7\n\t"
#define SM_SELM "v_cndmask_b32 %4, %24, %13, %26\n\tv_cndmask_b32 %5, %25, %14, %26\n\t"
#define SM_SELX "v_cndmask_b32 %0, %20, %15, %26\n\tv_cndmask_b32 %1, %21, %16, %26\n\tv_cndmask_b32 %2, %22, %17, %26\n\tv_cndmask_b32 %3, %23, %18, %26\n\t"
#define SM_EXPM "v_fma_f32 %4, %4, %19, -%12\n\tv_fma_f32 %5, %5, %19, -%12\n\tv_exp_f32 %4, %4\n\tv_exp_f32 %5, %5\n\t"
#define SM_EXPX "v_fma_f32 %0, %0, %19, -%12\n\tv_fma_f32 %1, %1, %19, -%12\n\tv_exp_f32 %0, %0\n\tv_fma_f32 %2, %2, %19, -%12\n\tv_exp_f32 %1, %1\n\tv_fma_f32 %3, %3, %19, -%12\n\tv_exp_f32 %2, %2\n\t" \
                "v_add_f32 %6, %4, %5\n\tv_exp_f32 %3, %3\n\tv_add_f32 %7, %0, %1\n\tv_add_f32 %6, %6, %2\n\tv_add_f32 %7, %7, %3\n\tv_add_f32 %6, %6, %7\n\t"
#define SM_OUT "v_rcp_f32 %7, %6\n\tv_med3_f32 %8, %6, %27, %28\n\t" \
               "v_mul_f32 %0, %0, %7\n\tv_mul_f32 %1, %1, %7\n\tv_cmp_eq_f32 %11, %8, %6\n\tv_mul_f32 %2, %2, %7\n\tv_mul_f32 %3, %3, %7\n\t" \
               "v_cvt_pk_bf16_f32 v248, |%0|, |%1|\n\tv_cvt_pk_bf16_f32 v250, |%0|, |%1|\n\tv_cvt_pk_bf16_f32 v249, |%2|, |%3|\n\tv_cvt_pk_bf16_f32 v251, |%2|, |%3|\n\t" \
               "v_permlane32_swap_b32 v248, v250\n\tv_mul_f32 %4, %4, %7\n\tv_permlane32_swap_b32 v249, v251\n\tv_mul_f32 %5, %5, %7"
#define SM_OPS : "=&v"(x0), "=&v"(x1), "=&v"(x2), "=&v"(x3), "=&v"(y0), "=&v"(y1), "=&v"(sum), "=&v"(t1), "=&v"(t2), "=&{v[248:251]}"(ox), "=&v"(t3), "=&s"(okm), "+v"(mref) \
               : "v"(s0[2]), "v"(s0[3]), "v"(s2[0]), "v"(s2[1]), "v"(s2[2]), "v"(s2[3]), "s"(cs), "v"(s1[0]), "v"(s1[1]), "v"(s1[2]), "v"(s1[3]), "v"(s0[0]), "v"(s0[1]), \
                 "s"(0xCCCCCCCCCCCCCCCCull), "v"(lo), "v"(hi)
template <int CLS>
DEV void p_loop(const Args& a, rsrc_t rs, int h, int wave, int lane, unsigned pol, const u32x8 (&w)[40], Sched& sc) {
  const int n = lane & 15, g = lane >> 4;
  const int idx = (lane & 2) ? 0xEEEE : 0x4444;
  const float NEG = -3.0e38f;
  const unsigned l16 = lane * 16, cpy = (h & (REPL - 1)) * 65536;
  unsigned vo[10];
#pragma unroll
  for (int k = 0; k < 10; ++k) { vo[k] = l16 + memoff(k); PIN(vo[k]); }
  f32x4 i1, i2;
#pragma unroll
  for (int r = 0; r < 4; ++r) {
    const bool tri = (4 * g + r) <= n;
    i1[r] = (wave == 1 && !tri) ? NEG : 0.f;
    i2[r] = (wave == 1 || (wave == 2 && !tri)) ? NEG : 0.f;
  }
  unsigned kvo[2];
#pragma unroll
  for (int tl = 0; tl < 2; ++tl) {
    int tok = 16 * tl + n;
    if (CLS == 0) { const int gp = n >> 2, r = n & 3; tok = 16 * (r >> 1) + 8 * (gp & 1) + 4 * (gp >> 1) + 2 * tl + (r & 1); }
    kvo[tl] = P_RING + (tok >> 4) * 4096 + (g * 16 + (tok & 15)) * 16;
  }
  const unsigned qvo = P_RING + 8192 + (wave == 2 ? 4096 : 0) + l16;
  unsigned wra = (32 * wave + n) * 16 + g * 4, wrq = 2 * wra;
  unsigned tra = (4 * g + ((lane >> 2) & 3)) * 16 + (lane & 1) * 8, trq = (4 * g + ((lane >> 2) & 3)) * 32 + (lane & 3) * 8;
  PIN(wra); PIN(wrq); PIN(tra); PIN(trq);
  unsigned vox[2], vom[2];
#pragma unroll
  for (int c = 0; c < 2; ++c) {
    if (CLS == 0) {
      vox[c] = (2 * c + (lane >> 5)) * 65536 + C_PX + (h >> 4) * 16384 + (h & 15) * 512 + (lane & 31) * 16;
      vom[c] = lane < 32 ? (2 * c + (lane >> 4)) * 65536 + C_PM + h * 128 + (lane & 15) * 8 : OOB;
      PIN(vox[c]); PIN(vom[c]);
    } else { vox[c] = spread((h * 2 + (wave - 1)) * 1024) + l16; vom[c] = lane < 16 ? (8 + wave - 1) * 16384 + h * 256 + l16 : OOB; }
  }
  const unsigned pvo = prog_off(0, h, (lane >> 2) & 1, lane & 3);
  const float cs = 0.08838834764831845f * 1.4426950408889634f;
  float mref = 0.f, lo = 0x1p-64f, hi = 0x1p64f;
  PIN(lo); PIN(hi);
  int budget = SPIN_CAP, rdy = 0;
  auto fetch = [&](int seg) {
    while (rdy <= seg && --budget > 0) { rdy = wmin(first_open(0, (lane >> 2) & 1, ld32(rs, pvo, 0))); CFENCE(); }
    static_for<4>([&](auto J) {
      const unsigned char* src = a.ws + kq_addr(J >> 1, seg, h, pol) + (J & 1) * 4096;
      set_m0(P_RING + (seg & 1) * 16384 + J * 4096); NOP(0);
      glds_off<0>(src, l16); glds_off<1024>(src, l16); glds_off<2048>(src, l16); glds_off<3072>(src, l16);
    });
  };
  u32x4 kx[2][4], qf[4];
  auto pull = [&](int seg) {
    const unsigned rb = (seg & 1) * 16384;
#pragma unroll
    for (int ks = 0; ks < 4; ++ks) { kx[0][ks] = ldsr128(kvo[0] + rb + ks * 1024); kx[1][ks] = ldsr128(kvo[1] + rb + ks * 1024); if (CLS == 1) qf[ks] = ldsr128(qvo + rb + ks * 1024); }
  };
#pragma unroll
  for (int ks = 0; ks < 4; ++ks) { kx[0][ks] = (u32x4){0, 0, 0, 0}; kx[1][ks] = kx[0][ks]; qf[ks] = kx[0][ks]; }
  unsigned hold = now8(), pend = 0;
  for (int s = 0; s < NSEG; ++s) {
    unsigned sb = slot_base(s, pol), sbr = sb + cpy;
    const float n0 = g >= 2 ? NEG : 0.f;
    f32x4 s0 = {n0, n0, n0, n0}, s1 = i1, s2 = i2;
    PINS(sb); PINS(sbr);
    if (CLS < 2) {
      PIN(s0); PIN(s1); PIN(s2);
#pragma unroll
      for (int ks = 0; ks < 4; ++ks) { PIN(kx[0][ks]); PIN(kx[1][ks]); }
    }
    if (CLS == 1) {
      NOP(1);
#pragma unroll
      for (int ks = 0; ks < 4; ++ks) { mf_vv(s1, kx[0][ks], qf[ks]); mf_vv(s2, kx[1][ks], qf[ks]); }
    }
    unsigned cst = 0;
    if (CLS == 2) cst = trigger(rs, sc, 2 * s, hold, [&]() { mem_wait(rs, sbr, lane, budget); });
    f32x4 acc[4];
    zero_acc(acc);
    STAMP_DECL;
    unsigned good = 2;
    if (CLS == 2) release(cst); else BAR();
    STAMP(0);
    for (;;) {
      u32x4 am[10];
      project<4, 8>(rs, vo, sbr, w, idx, am, acc);
      float a0, a1, b0, b1;
      smf_w<16>(acc[0], am[8], w, idx); smf_w<17>(acc[1], am[8], w, idx); smf_w<18>(acc[0], am[9], w, idx); smf_w<19>(acc[1], am[9], w, idx);
      smf_w<36>(acc[2], am[8], w, idx); smf_w<37>(acc[3], am[8], w, idx); smf_w<38>(acc[2], am[9], w, idx);
      FADD2(a0, a1, acc[0]);
      smf_w<39>(acc[3], am[9], w, idx);
      FADD2(b0, b1, acc[1]);
      STAMP(1);
      asm volatile("ds_write2_b32 %0, %1, %1 offset0:0 offset1:1" :: "v"(wrq), "v"(pk(a0, a1)) : "memory");
      asm volatile("ds_write2_b32 %0, %1, %1 offset0:128 offset1:129" :: "v"(wrq), "v"(pk(b0, b1)) : "memory");
      const bool bad = __any(__builtin_isunordered(a0, a1));
      PIN(acc[2]);
      const unsigned f2 = foldpk(acc[2]);
      pin1(f2);
      PIN(acc[3]);
      ldsw32(wra + 4096, f2); ldsw32(wra + 4096 + 256, foldpk(acc[3]));
      if (__builtin_expect(!bad, 1)) break;
      if ((!(good & 2) && !has_sent(am)) || --budget <= 0) break;
      good = 0;
      zero_acc(acc);
      mem_wait(rs, sbr, lane, budget);
    }
    if (__builtin_expect(s == 0, 0)) {
      if (CLS == 2) { fetch(0); VMWAIT(0); }
      BAR();
      EV(2);
      if (CLS < 2) pull(0);
      if (CLS == 1) {
#pragma unroll
        for (int ks = 0; ks < 4; ++ks) { mf_vv(s1, kx[0][ks], qf[ks]); mf_vv(s2, kx[1][ks], qf[ks]); }
      }
    }
    BAR();
    STAMP(2);
    u32x4 km[4];
#define TRQ(ks) qf[ks] = cat4(trread(trq + (ks) * 1024), trread(trq + (ks) * 1024 + 512))
#define TRK(ks) km[ks] = cat4(trread(tra + 4096 + (ks) * 512), trread(tra + 4096 + (ks) * 512 + 256))
    if (CLS == 0) {
      TRQ(0); TRQ(1); TRK(0); TRK(1); CFENCE();
      mf_vv(s1, kx[0][0], qf[0]); CFENCE(); TRQ(2); CFENCE(); mf_vv(s2, kx[1][0], qf[0]); CFENCE(); TRK(2); CFENCE();
      mf_vv(s1, kx[0][1], qf[1]); CFENCE(); TRQ(3); CFENCE(); mf_vv(s2, kx[1][1], qf[1]); CFENCE(); TRK(3); CFENCE();
      mf_vv(s0, km[0], qf[0]); mf_vv(s0, km[1], qf[1]); mf_vv(s0, km[2], qf[2]); mf_vv(s0, km[3], qf[3]);
      mf_vv(s1, kx[0][2], qf[2]); mf_vv(s2, kx[1][2], qf[2]); mf_vv(s1, kx[0][3], qf[3]); mf_vv(s2, kx[1][3], qf[3]);
      STAMP(3);
      float x0, x1, x2, x3, y0, y1, sum, t1, t2, t3;
      u32x4 ox; u32x2 om; unsigned long long okm;
      asm volatile(SM_SELM SM_EXPM "s_nop 1\n\t" SM_SELX SM_EXPX ROWRED("v_add_f32") SM_OUT SM_OPS);
      if (__builtin_expect(okm != ~0ull, 0))
        asm volatile(SM_SELM SM_SELX "v_max3_f32 %6, %4, %5, %0\n\tv_max3_f32 %7, %1, %2, %3\n\tv_max_f32 %6, %6, %7\n\t" ROWRED("v_max_f32") "v_mul_f32 %12, %19, %6\n\t"
                     SM_EXPM SM_EXPX ROWRED("v_add_f32") SM_OUT SM_OPS);
      st128(rs, ox, vox[0], sb); st128(rs, ox, vox[1], sb);
      asm volatile("v_cvt_pk_bf16_f32 v252, |%1|, |%2|\n\tv_cvt_pk_bf16_f32 v253, |%1|, |%2|\n\ts_nop 1\n\tv_permlane16_swap_b32 v252, v253" : "=&{v[252:253]}"(om) : "v"(y0), "v"(y1) : "memory");
      st64(rs, om, vom[0], sb); st64(rs, om, vom[1], sb);
      CFENCE();
      STAMP(4);
      STAMP_OUT(h, s);
      report(rs, h, s, good, lane);
      mref += __builtin_amdgcn_logf(sum);
      if (s >= 1) {
        const unsigned rb = slot_base(s - 1, pol);
#pragma unroll
        for (int c = 0; c < 2; ++c) { st128(rs, (u32x4){SENT, SENT, SENT, SENT}, vox[c], rb); st64(rs, (u32x2){SENT, SENT}, vom[c], rb); }
      }
      if (s == 0) BAR();
      if (s + 1 < NSEG) pull(s + 1);
    } else if (CLS == 1) {
      TRK(0); TRK(1); TRK(2); TRK(3);
#pragma unroll
      for (int ks = 0; ks < 4; ++ks) mf_vv(s0, km[ks], qf[ks]);
      asm volatile("s_nop 7" : "+v"(s0), "+v"(s1), "+v"(s2));
      float mxv = fmaxf(fmaxf(fmaxf(s1[0], s1[1]), fmaxf(s1[2], s1[3])), fmaxf(fmaxf(s2[0], s2[1]), fmaxf(s2[2], s2[3])));
      mxv = fmaxf(fmaxf(fmaxf(s0[0], s0[1]), fmaxf(s0[2], s0[3])), mxv);
      mxv = xmax32(xmax16(mxv));
      const float mb = -mxv * cs;
      float sum = 0.f;
#pragma unroll
      for (int r = 0; r < 4; ++r) {
        s0[r] = __builtin_amdgcn_exp2f(fmaf(s0[r], cs, mb)); s1[r] = __builtin_amdgcn_exp2f(fmaf(s1[r], cs, mb)); s2[r] = __builtin_amdgcn_exp2f(fmaf(s2[r], cs, mb));
        sum += s0[r] + s1[r] + s2[r];
      }
      sum = xadd32(xadd16(sum));
      const float inv = __builtin_amdgcn_rcpf(sum);
      s0 *= inv; s1 *= inv; s2 *= inv;
      const unsigned so = pt_base(s, pol);
      const u32x2 pmv = pk4(s0);
      auto r0 = __builtin_amdgcn_permlane16_swap(pmv.x, pmv.x, false, false);
      auto r1 = __builtin_amdgcn_permlane16_swap(pmv.y, pmv.y, false, false);
      const u32x4 ptx = cat4(pk4(s1), pk4(s2));
      if (s == 0) BAR();
      if (s + 1 < NSEG) { pull(s + 1); __builtin_amdgcn_s_sleep(PTSLEEP); }
      st128(rs, ptx, vox[0], so);
      st128(rs, (u32x4){(unsigned)r0[0], (unsigned)r1[0], (unsigned)r0[1], (unsigned)r1[1]}, vom[0], so);
      STAMP(4);
      STAMP_OUT(h, s);
    } else {
      const unsigned tend = now8() + 24 * 8;
      if (s == 0) { fetch(1); VMWAIT(0); BAR(); }
      const u32x4 cur = ld128(rs, 0, OFF_CTL + CTL_CUR);
      rdy = max(rdy, probe_rdy(pend, s, lane));
      adopt_cur(sc, cur, 2 * s + 2);
      mark_overrun(sc, 2 * s, tend);
      if (s + 2 < NSEG) fetch(s + 2);
      pend = 0;
      if (rdy <= s + 5) pend = probe_ld(rs, s + 1, lane);
      STAMP_OUT(h, s);
      hold = tend + HOLDP; clk_update(sc);
    }
  }
  VMWAIT(0);
  BAR();
  if (CLS == 2) st128(rs, (u32x4){1, 1, 1, 1}, lane < 8 ? lane * 16 : OOB, OFF_REP + (NSEG * 128 + h) * 128);
  EV(3);
}
DEV bool role_p(const Args& a, rsrc_t rs, int h, int wave, int lane, Boot& bt) {
  u32x8 w[40];
  Sched sc; clk_init(sc);
  load_weights<K_P>(w, a.wq + h * 256, a.wk + h * 256, wave, lane, 15, []() { VMWAIT(0); });
  BAR();
  if (!boot_wait(rs, bt, lane)) return false;
  const unsigned pol = bt.pol;
  EV(1);
  if (wave == 0) p_loop<0>(a, rs, h, wave, lane, pol, w, sc);
  else if (wave < 3) p_loop<1>(a, rs, h, wave, lane, pol, w, sc);
  else p_loop<2>(a, rs, h, wave, lane, pol, w, sc);
  return true;
}

#define VLDS 1
constexpr unsigned L_VG = 16384;
DEV bool role_v(const Args& a, rsrc_t rs, int h, int wave, int lane, Boot& bt) {
  u32x8 w[40];
  Sched sc; clk_init(sc);
  load_weights<K_V>(w, a.wv + h * 256, a.wo + (size_t)h * 128 * 1280, wave, lane, 15, []() { VMWAIT(0); });
  BAR();
  if (!boot_wait(rs, bt, lane)) return false;
  const unsigned pol = bt.pol;
  EV(1);
  const int n = lane & 15, g = lane >> 4;
  const int idx = (lane & 2) ? 0xEEEE : 0x4444;
  unsigned wra = (16 * wave + (n >> 2) * 2 + (n & 1)) * 32 + g * 8 + ((n >> 1) & 1) * 4, tra = (8 * g + ((lane >> 2) & 3)) * 32 + (lane & 3) * 8;
  unsigned zv0 = g < 2 ? (wave * 2 + g) * 16384 + h * 256 + n * 16 : OOB;
  unsigned zv1 = (8 + (g >> 1) * 8 + wave * 2 + (g & 1)) * 16384 + h * 256 + n * 16, zv2 = zv1 + 16 * 16384;
  unsigned vg0 = lane * 16 + wave * 1024, vg1 = vg0 + 4096, vg2 = lane * 16 + C_MEM1 + (wave & 1) * 1024, vga = L_VG + lane * 16;
  PIN(wra); PIN(tra); PIN(zv0); PIN(zv1); PIN(zv2); PIN(vg0); PIN(vg1); PIN(vg2); PIN(vga);
  unsigned vo[10];
#pragma unroll
  for (int k = 0; k < 10; ++k) { vo[k] = lane * 16 + memoff(k); PIN(vo[k]); }
  int budget = SPIN_CAP;
  unsigned hold = now8();
  for (int s = 0; s < NSEG; ++s) {
    unsigned sb = slot_base(s, pol) + (h & (REPL - 1)) * 65536, zb = z_base(s, pol);
    const unsigned char* gsrc = a.ws + sb;
    PINS(sb); PINS(zb); PINS(gsrc);
    f32x4 c[10], acc[2];
    zero_acc(c); zero_acc(acc);
    unsigned cst;
    if (wave == 3) cst = trigger(rs, sc, 2 * s, hold, [&]() { mem_wait(rs, sb, lane, budget); }); else cst = cycles();
    STAMP_DECL;
    unsigned good = 2;
    release(__builtin_amdgcn_readfirstlane(cst));
    STAMP(0);
    for (;;) {
      u32x4 am[10];
      if (good & 2) {
        set_m0(L_VG + wave * 1024); NOP(0);
        glds_off<0>(gsrc, vg0); set_m0(L_VG + 4096 + wave * 1024); NOP(0);
        glds_off<0>(gsrc, vg1); set_m0(L_VG + 8192 + wave * 1024); NOP(0);
        glds_off<0>(gsrc, vg2);
        asm volatile("s_waitcnt vmcnt(0)\n\ts_barrier" ::: "memory");
#pragma unroll
        for (int k = 0; k < 10; ++k) am[k] = ldsr128(vga + k * 1024);
      } else {
#pragma unroll
        for (int k = 0; k < 10; ++k) am[k] = ld128(rs, vo[k], sb);
      }
      static_for<10>([&](auto K) { smf_w<K * 2>(acc[0], am[K], w, idx); smf_w<K * 2 + 1>(acc[1], am[K], w, idx); });
      STAMP(1);
      asm volatile("s_nop 7" : "+v"(acc[0]), "+v"(acc[1]));
      ldsw32(wra, foldpk(acc[0])); ldsw32(wra + 256, foldpk(acc[1]));
      if (__builtin_expect(!has_nan(acc[0]), 1)) break;
      if ((!(good & 2) && !has_sent(am)) || --budget <= 0) break;
      good = 0;
      zero_acc(acc);
      mem_wait(rs, sb, lane, budget);
    }
    BAR();
    STAMP(2);
    u32x4 va[2];
#pragma unroll
    for (int ks = 0; ks < 2; ++ks) va[ks] = cat4(trread(tra + ks * 1024), trread(tra + ks * 1024 + 128));
    CFENCE(); const u32x4 cur = ld128(rs, 0, OFF_CTL + CTL_CUR); CFENCE();
    u32x4 O0, O1, O2;
#define VMM(t, k) smf_w<20 + ((t) >> 1) * 4 + 2 * (k) + ((t) & 1)>(c[t], va[k], w, idx)
#define VEA(t, T) FADD2(T##0, T##1, c[t])
#define VEC2(R, ra, rb, O) asm volatile("v_cvt_pk_bf16_f32 " ra ", %1, %2\n\tv_cvt_pk_bf16_f32 " rb ", %3, %4" : "+{" R "}"(O) : "v"(ta0), "v"(ta1), "v"(tb0), "v"(tb1))
#define VSW(txt, R, O) asm volatile(txt : "+{" R "}"(O))
    float ta0, ta1, tb0, tb1;
    asm volatile("" : "={v[232:235]}"(O0), "={v[236:239]}"(O1), "={v[240:243]}"(O2));
    VMM(0, 0); VMM(1, 0); VMM(0, 1); VMM(1, 1);
    VMM(2, 0); VMM(3, 0); VMM(2, 1); VMM(3, 1);
    VMM(4, 0); NOP(1); VMM(5, 0); VEA(0, ta); VMM(4, 1); VEA(1, tb); VMM(5, 1); VEC2("v[232:235]", "v232", "v233", O0);
    VMM(6, 0); VEA(2, ta); VMM(7, 0); VEA(3, tb); VMM(6, 1); VSW("v_permlane16_swap_b32 v232, v233", "v[232:235]", O0); VMM(7, 1); VEC2("v[236:239]", "v236", "v237", O1);
    VMM(8, 0); VSW("v_permlane32_swap_b32 v232, v234", "v[232:235]", O0); VMM(9, 0); VSW("v_permlane32_swap_b32 v233, v235", "v[232:235]", O0);
    VMM(8, 1); VEA(4, ta); VMM(9, 1); CFENCE(); st128(rs, O0, zv0, zb); CFENCE(); VEA(5, tb);
    VEC2("v[236:239]", "v238", "v239", O1);
    VEA(6, ta); VEA(7, tb);
    VSW("v_permlane32_swap_b32 v236, v238\n\tv_permlane32_swap_b32 v237, v239", "v[236:239]", O1);
    VEC2("v[240:243]", "v240", "v241", O2);
    VSW("v_permlane16_swap_b32 v236, v237\n\tv_permlane16_swap_b32 v238, v239", "v[236:239]", O1);
    CFENCE(); st128(rs, O1, zv1, zb); CFENCE();
    VEA(8, ta); VEA(9, tb); VEC2("v[240:243]", "v242", "v243", O2);
    VSW("s_nop 0\n\tv_permlane32_swap_b32 v240, v242\n\tv_permlane32_swap_b32 v241, v243\n\ts_nop 1\n\tv_permlane16_swap_b32 v240, v241\n\tv_permlane16_swap_b32 v242, v243", "v[240:243]", O2);
    CFENCE(); st128(rs, O2, zv2, zb);
    CFENCE();
    STAMP(4);
    STAMP_OUT(32 + h, s);
    if (wave == 0) report(rs, 32 + h, s, good, lane);
    if (wave == 3) { const unsigned tend = now8(); hold = tend + HOLDP; adopt_cur(sc, cur, 2 * s + 2); mark_overrun(sc, 2 * s, tend - VSLACK); clk_update(sc); }
  }
  VMWAIT(0);
  BAR();
  if (wave == 0) st128(rs, (u32x4){1, 1, 1, 1}, lane < 8 ? lane * 16 : OOB, OFF_REP + (NSEG * 128 + 32 + h) * 128);
  return true;
}

#define CLOB_A "a0", "a1", "a2", "a3", "a4", "a5", "a6", "a7", "a8", "a9", "a10", "a11", "a12", "a13", "a14", "a15", "a16", "a17", "a18", "a19", "a20", "a21", "a22", "a23", "a24", "a25", "a26", "a27", "a28", "a29", "a30", "a31", "a32", "a33", "a34", "a35", "a36", "a37", "a38", "a39", "a40", "a41", "a42", "a43", "a44", "a45", "a46", "a47", "a48", "a49", "a50", "a51", "a52", "a53", "a54", "a55", "a56", "a57", "a58", "a59", "a60", "a61", "a62", "a63", "a64", "a65", "a66", "a67", "a68", "a69", "a70", "a71", "a72", "a73", "a74", "a75", "a76", "a77", "a78", "a79", "a80", "a81", "a82", "a83", "a84", "a85", "a86", "a87", "a88", "a89", "a90", "a91", "a92", "a93", "a94", "a95", "a96", "a97", "a98", "a99", "a100", "a101", "a102", "a103", "a104", "a105", "a106", "a107", "a108", "a109", "a110", "a111", "a112", "a113", "a114", "a115", "a116", "a117", "a118", "a119", "a120", "a121", "a122", "a123", "a124", "a125", "a126", "a127"
template <int J> DEV void c_ldb(unsigned l16, const unsigned char* src) {
  asm volatile("global_load_dwordx4 a[%c2:%c2+3], %0, %1 sc1\n\tglobal_load_dwordx4 a[%c2+4:%c2+7], %0, %1 offset:1024 sc1\n\t"
               "global_load_dwordx4 a[%c2+8:%c2+11], %0, %1 offset:2048 sc1\n\tglobal_load_dwordx4 a[%c2+12:%c2+15], %0, %1 offset:3072 sc1"
               :: "v"(l16), "s"(src + (J >> 1) * BSTR + (J & 1) * 4096), "n"(J * 16) : "memory", CLOB_A);
}
template <int J> DEV void c_smf(f32x4& acc, u32x4 p, int idx) { asm volatile("v_smfmac_f32_16x16x64_bf16 %0, %1, a[%c3:%c3+7], %2" : "+{v[248:251]}"(acc) : "v"(p), "v"(idx), "n"(J * 8)); }
#define ZFIRST 0
#define ASLEEP -1
#define BFREE 3
#define BDELAY 30
DEV void c_arm(rsrc_t rs, int s, int cb, int lane, unsigned pol) {
  const unsigned zn = z_base(s, pol) + cb * 16384;
#pragma unroll
  for (int j = 0; j < 8; ++j) st128(rs, (u32x4){SENT, SENT, SENT, SENT}, lane * 16 + (j & 3) * 1024, zn + (j >> 2) * 4096);
}
DEV void role_c(const Args& a, rsrc_t rs, int cb, int wave, int lane, unsigned pol) {
  Sched sc; clk_init(sc);
  c_arm(rs, wave, cb, lane, pol);
  VMWAIT(0);
  BAR();
  const unsigned pvo = prog_off(1, lane >> 1, lane & 1, (cb >> 1) & 3);
  if (wave == 3) {
    int rdy = 0;
    for (int s = 0; s < NSEG - 1; ++s) {
      BAR(); BAR();
      unsigned v = 0;
      if (rdy <= s + 4) v = probe_ld(rs, s, lane);
      BAR();
      rdy = max(rdy, probe_rdy(v, s, lane));
      ldsw32(L_TM + 32, rdy);
    }
    return;
  }
  if (wave == 2) {
    const unsigned rvo = (lane >> 4) < REPL ? (lane >> 4) * 65536 + (cb < 32 ? cb * 256 : C_MEM1 + (cb - 32) * 256) + (lane & 15) * 16 : OOB;
    for (int s = 0; s < NSEG - 1; ++s) {
      BAR(); VMWAIT(0); BAR();
      if (ASLEEP >= 0) { __builtin_amdgcn_s_sleep(ASLEEP >= 0 ? ASLEEP : 0); if (s + 4 < NSEG) c_arm(rs, s + 4, cb, lane, pol); }
      BAR();
      st128(rs, (u32x4){SENT, SENT, SENT, SENT}, rvo, slot_base(s, pol));
      if (ASLEEP < 0 && s + 4 < NSEG) c_arm(rs, s + 4, cb, lane, pol);
    }
    return;
  }
  int budget = SPIN_CAP;
  const unsigned cpy = ((cb / 10) & (REPL - 1)) * 65536;
  auto c_wait = [&](unsigned sb, unsigned zb) {
    for (;;) {
      const unsigned u = ld32(rs, C_PM + (lane & 31) * 128, sb), v = ld32(rs, cb * 16384 + lane * 128, zb);
      CFENCE();
      if (__all(u != SENT && v != SENT) || --budget <= 0) break;
      __builtin_amdgcn_s_sleep(PSLEEP);
    }
  };
  if (wave == 1) {
    unsigned hold = now8();
    for (int s = 0; s < NSEG - 1; ++s) {
      release(trigger(rs, sc, 2 * s + 1, hold, [&]() { c_wait(slot_base(s, pol) + cpy, z_base(s, pol)); }));
      clk_update(sc);
      BAR();
      hold = now8();
      adopt_cur(sc, ld128(rs, 0, OFF_CTL + CTL_CUR), 2 * s + 3);
      mark_overrun(sc, 2 * s + 1, hold);
      hold += HOLDC;
      if (sc.j0 > sc.jbad && sc.rx <= 2 * s + 2 && s >= BFREE) wait_until(tau_of(sc, 2 * s + 2) + BDELAY * 8);
      BAR();
    }
    return;
  }
  const int n = lane & 15, g = lane >> 4;
  const int idx = (lane & 2) ? 0xEEEE : 0x4444;
  const unsigned l16 = lane * 16;
  unsigned pxvo[4], zvo[2], pmvo = C_PM + 2 * g * 128 + n * 8;
#pragma unroll
  for (int i = 0; i < 4; ++i) { pxvo[i] = C_PX + (i >> 1) * 16384 + (i & 1) * 4096 + l16; PIN(pxvo[i]); }
#pragma unroll
  for (int i = 0; i < 2; ++i) { zvo[i] = cb * 16384 + i * 4096 + l16; PIN(zvo[i]); }
  const unsigned mvo = (cb < 32 ? cb * 256 : C_MEM1 + (cb - 32) * 256) + mrow(2 * g + (n >> 3), (n & 7) >> 2) * 16 + (n & 3) * 4;
  unsigned oob = OOB; PIN(oob); PIN(pmvo);
  {
    const GAS unsigned short* m0 = (const GAS unsigned short*)a.mem0 + (2 * g + (n >> 3)) * 640 + cb * 16 + (n & 7);
    const unsigned v = min(m0[0] | (unsigned)m0[8] << 16, 0xFFFFFFFEu);
#pragma unroll
    for (int c = 0; c < REPL; ++c) st32(rs, v, mvo, slot_base(0, pol) + c * 65536);
  }
  int rdy = 0;
  auto fetch = [&](int seg) {
    while (rdy <= seg && --budget > 0) { rdy = wmin(first_open(1, lane & 1, ld32(rs, pvo, 0))); CFENCE(); }
    const unsigned char* src = a.ws + b_addr(seg, cb, pol);
    static_for<8>([&](auto J) { c_ldb<J>(l16, src); });
  };
  fetch(0);
  for (int s = 0; s < NSEG - 1; ++s) {
    unsigned sb = slot_base(s, pol) + cpy, zb = z_base(s, pol), sbn[REPL];
#pragma unroll
    for (int c = 0; c < REPL; ++c) { sbn[c] = slot_base(s + 1, pol) + c * 65536; PINS(sbn[c]); }
    PINS(sb); PINS(zb);
    STAMP_DECL;
    unsigned good = 2;
    BAR();
    STAMP(0);
    for (;;) {
      u32x4 z[8], pm[4], px[16];
#pragma unroll
      for (int j = 0; j < ZFIRST; ++j) { z[j] = ld128(rs, zvo[j >> 2] + (j & 3) * 1024, zb); CFENCE(); }
#pragma unroll
      for (int j = 0; j < 16; ++j) px[j] = ld128(rs, pxvo[j >> 2] + (j & 3) * 1024, sb);
      CFENCE();
#pragma unroll
      for (int j = 0; j < 4; ++j) pm[j] = cat4(ld64(rs, pmvo + j * 1024, sb), ld64(rs, pmvo + j * 1024 + 128, sb));
      CFENCE();
#pragma unroll
      for (int j = ZFIRST; j < 8; ++j) { z[j] = ld128(rs, zvo[j >> 2] + (j & 3) * 1024, zb); CFENCE(); }
      f32x4 acc = {0, 0, 0, 0};
      asm volatile("s_nop 1" : "+{v[248:251]}"(acc));
      static_for<16>([&](auto J) { c_smf<J>(acc, px[J], idx); });
#pragma unroll
      for (int j = 0; j < 4; ++j) asm volatile("v_smfmac_f32_16x16x64_bf16 %0, %1, %2, %3" : "+{v[248:251]}"(acc) : "v"(pm[j]), "v"(cat8(z[2 * j], z[2 * j + 1])), "v"(idx));
      STAMP(1);
      unsigned o, vo2; unsigned long long okm;
      asm volatile(
        "s_nop 7\n\t"
        "v_add_f32 v248, v248, v250\n\t"
        "v_add_f32 v250, v249, v251\n\t"
        "v_add_f32 v249, v249, v251\n\t"
        "v_mov_b32_dpp v250, v248" DPL
        "s_nop 0\n\t"
        "v_mov_b32_dpp v248, v249" DPH
        "v_cmp_o_f32 %3, v248, v250\n\t"
        "v_cvt_pk_bf16_f32 %1, v248, v250\n\t"
        "s_nop 0\n\t"
        "v_cndmask_b32 %2, %5, %4, %3"
        : "+{v[248:251]}"(acc), "=&v"(o), "=&v"(vo2), "=&s"(okm) : "v"(mvo), "v"(oob));
#pragma unroll
      for (int c = 0; c < REPL; ++c) st32(rs, o, vo2, sbn[c]);
      CFENCE();
      if (__builtin_expect(okm == ~0ull, 1)) break;
      if ((!(good & 2) && !(has_sent(z) || has_sent(pm) || has_sent(px))) || --budget <= 0) {
#pragma unroll
        for (int c = 0; c < REPL; ++c) st32(rs, min(o, 0xFFFFFFFEu), mvo, sbn[c]);
        break;
      }
      good = 0;
      c_wait(sb, zb);
    }
    STAMP(4);
    STAMP_OUT(64 + cb, s);
    report(rs, 64 + cb, s, good, lane);
    BAR();
    if (s == 0) EV(2); if (s == NSEG - 2) EV(3);
    if (s > 0) rdy = max(rdy, (int)__builtin_amdgcn_readfirstlane(*(volatile LAS unsigned*)(uintptr_t)(L_TM + 32)));
    BAR();
    if (s + 2 < NSEG) fetch(s + 1);
  }
}

template <int IMG, int C0, int C1> DEV void u_scatter(const f32x4 (&acc)[4][2], unsigned base) {
  PIN(base);
  LAS char* p = (LAS char*)(uintptr_t)base;
#pragma unroll
  for (int c = C0; c < C1; ++c)
#pragma unroll
    for (int tt = 0; tt < 2; ++tt) *(LAS f32x4*)(p + IMG + tt * 16 * 272 + c * 64) = acc[c][tt];
}
template <int IMG> DEV void u_gather(f32x4& v0, f32x4& v1, unsigned base) {
  PIN(base);
  LAS char* p = (LAS char*)(uintptr_t)base;
  v0 = (f32x4){0, 0, 0, 0}; v1 = v0;
#pragma unroll
  for (int ww = 0; ww < 4; ++ww) { v0 += *(LAS f32x4*)(p + IMG + ww * 8704); v1 += *(LAS f32x4*)(p + IMG + ww * 8704 + 16); }
}
#define UOFF 240
#define UOFF_LAST 320
#define USLEEP 0
#define UPACE 0
constexpr unsigned CTL_UT = 65536 + 16384;
constexpr unsigned L_UT = 69632;
DEV void role_u(const Args& a, rsrc_t rs, int cq, int wave, int lane, unsigned pol) {
  const int tid = wave * 64 + lane;
  const unsigned l16 = lane * 16;
  u32x4 w[56];
  auto ticket = [&]() -> unsigned {
    unsigned t = 0;
    if (lane == 0) t = __hip_atomic_fetch_add((__attribute__((address_space(1))) unsigned*)(a.ws + OFF_CTL + CTL_UT + cq * 128), 1u, __ATOMIC_RELAXED, __HIP_MEMORY_SCOPE_AGENT);
    return t;
  };
  load_w2(w, a.w2 + cq * 128, wave, lane);
  BAR();
  if (wave == 0) { const unsigned t = ticket(); if (lane == 0) ldsw32(L_UT, t); }
  BAR();
  const int seg0 = __builtin_amdgcn_readfirstlane(*(volatile LAS unsigned*)(uintptr_t)L_UT);
  if (seg0 >= NSEG) return;
  EV(7);
  const int nks = wave < 3 ? 14 : 12;
  const unsigned sca = wave * 8704 + (lane & 15) * 272 + (lane >> 4) * 16, gaa = (tid >> 3) * 272 + (tid & 7) * 32;
  int budget = SPIN_CAP;
  Sched sc; clk_init(sc);
  auto rep_vo = [&](int seg) { return (lane >= 16 && lane < (seg == NSEG - 1 ? 32 : 24)) ? OFF_REP + ((seg + 1) * 128 + ((lane >> 3) & 1) * 32 + wave * 8 + (lane & 7)) * 128 : OOB; };
  auto chk_vo = [&](int seg) {
    const bool past = sc.j0 != 0 && (int)(now8() - tau_of(sc, 2 * seg + 3)) > 200 * 8;
    return lane < nks ? hprog_off(seg & 1, wave * 14 + lane) : lane == 14 ? OFF_CTL + CTL_RL + seg * 128 : past ? rep_vo(seg) : OOB;
  };
  auto need_of = [&](int seg) -> unsigned { return lane < nks ? (seg >> 1) + 1 : lane == 14 ? 1 : 0; };
  u32x4 hf[14][2];
  auto start = [&](int seg, unsigned& have, bool on) {
    const unsigned need = need_of(seg);
    while (!__all(have >= need) && --budget > 0) { __builtin_amdgcn_s_sleep(16); have = ld32(rs, chk_vo(seg), 0); CFENCE(); }
    const unsigned hb = h_base(seg, pol) + wave * 65536, va = on ? l16 : OOB, vb = (on && wave < 3) ? l16 : OOB;
#pragma unroll
    for (int k = 0; k < 14; ++k)
#pragma unroll
      for (int tt = 0; tt < 2; ++tt) hf[k][tt] = ld128(rs, (k < 12 ? va : vb) + tt * 1024, hb + spread(k * 2048));
    CFENCE();
  };
  adopt_cur(sc, ld128(rs, 0, OFF_CTL + CTL_CUR), 2 * NSEG);
  unsigned have = ld32(rs, chk_vo(seg0), 0);
  start(seg0, have, true);
  const unsigned tvo = tid == 0 ? OFF_CTL + CTL_UT + cq * 128 : OOB;
  for (int seg = seg0;;) {
    const bool last = seg == NSEG - 1;
    const unsigned rvo = rep_vo(seg);
    const bool ract = rvo != OOB, chain_ok = __all(!ract || have != 0);
    unsigned tk;
    f32x4 acc[4][2];
    u32x4 bx[4][8];
    unsigned ba[4];
#pragma unroll
    for (int c = 0; c < 4; ++c) ba[c] = b_addr(seg, cq * 4 + c, pol) + wave * BSTR;
    unsigned so = pt_base(seg, pol), sx = so + wave * 32768, sm = so + 8 * 16384 + wave * 2048, zo = z_base(seg, pol) + cq * 65536 + wave * 2048;
    PINS(sx); PINS(sm); PINS(zo);
    static_for<112>([&](auto N) {
      constexpr int n = N, k = n >> 3, c = (n >> 1) & 3, wi = ((k >> 1) * 2 + (c >> 1)) * 2 + (c & 1);
      mf_w<wi, k & 1, true, k == 0>(acc[c][n & 1], w, hf[k][n & 1]);
      if constexpr (n == 2) { CFENCE(); tk = __builtin_amdgcn_raw_ptr_buffer_atomic_add_i32(chain_ok ? 1 : 0, rs, tvo, 0, 0); CFENCE(); }
      if constexpr (n % 7 == 1 || n % 7 == 4) {
        constexpr int q = (n / 7) * 2 + (n % 7 == 4), cc = q >> 3, hh = q & 7;
        CFENCE(); bx[cc][hh] = ld128(rs, l16 + (hh & 3) * 1024, ba[cc] + (hh & 4) * 1024); CFENCE();
      }
    });
    int nxt; bool late;
    u32x4 gp;
    auto gate = [&]() {
      if (tid == 0) ldsw64(L_UT + 8, (u32x2){tk, chain_ok ? 0u : 1u});
      BAR();
      const u32x2 t = *(volatile LAS u32x2*)(uintptr_t)(L_UT + 8);
      nxt = __builtin_amdgcn_readfirstlane(t.x); late = __builtin_amdgcn_readfirstlane(t.y);
      f32x4 g0, g1;
      u_gather<0>(g0, g1, gaa);
#pragma unroll
      for (int r = 0; r < 4; ++r) { g0[r] = silu(rbf(g0[r])); g1[r] = silu(rbf(g1[r])); }
      gp = cat4(pk4(g0), pk4(g1));
      PIN(gp);
    };
    const unsigned oo = (seg * 32 + (tid >> 3)) * 1280 + cq * 128 + (tid & 7) * 16;
    u32x4 px[8][2], xv;
    acc_wait(acc);
#pragma unroll
    for (int hh = 0; hh < 8; ++hh) asm volatile("" : "+a"(bx[0][hh]));
    if (!chain_ok) {
      u_scatter<0, 0, 4>(acc, sca);
      gate();
      sleep_until_turn(rs, sc, last ? 2 * seg + 1 : 2 * seg + 3, last ? UOFF_LAST : UOFF);
      for (;;) { const unsigned v = ld32(rs, rvo, 0); CFENCE(); if (__all(!ract || v != 0) || --budget <= 0) break; if (last) __builtin_amdgcn_s_sleep(2); else __builtin_amdgcn_s_sleep(16); }
    }
    EV(8);
    static_for<4>([&](auto C) {
#pragma unroll
      for (int q = 0; q < 4; ++q) px[2 * C + (q >> 1)][q & 1] = ld128(rs, l16 + (q & 1) * 1024, sx + spread((2 * C + (q >> 1)) * 2048));
      CFENCE();
      if (chain_ok) u_scatter<0, C, C + 1>(acc, sca);
      CFENCE();
    });
    if (chain_ok) gate();
    const bool more = !late && nxt < NSEG;
    const int pre = more ? nxt : seg;
    have = ld32(rs, chk_vo(pre), 0);
    bool on = false;
    unsigned va = OOB, vb = OOB;
    unsigned hb = h_base(pre, pol) + wave * 65536;
    PINS(hb);
    {
      u32x4 pm[2][2], z[4][2], cur;
      static_for<80>([&](auto N) {
        constexpr int n = N, hh = n >> 3, c = (n >> 1) & 3, tt = n & 1;
        if constexpr (n < 8) { if constexpr (c == 0) mf_va0(acc[c][tt], px[0][tt], bx[c][0]); else mf_vv0(acc[c][tt], bx[c][0], px[0][tt]); }
        else if constexpr (hh < 8) { if constexpr (c == 0) mf_va(acc[c][tt], px[hh][tt], bx[c][hh]); else mf_vv(acc[c][tt], bx[c][hh], px[hh][tt]); }
        else mf_vv(acc[c][tt], z[c][hh - 8], pm[hh - 8][tt]);
        if constexpr ((n & 1) == 1) {
          constexpr int q = n >> 1;
          CFENCE();
          if constexpr (q < 4) pm[q >> 1][q & 1] = ld128(rs, l16 + (q >> 1) * 1024, sm + (q & 1) * 16384);
          else if constexpr (q < 12) z[(q - 4) >> 1][q & 1] = ld128(rs, l16 + (q & 1) * 1024, zo + ((q - 4) >> 1) * 16384);
          else if constexpr (q == 12) { cur = ld128(rs, 0, OFF_CTL + CTL_CUR); xv = *(const GAS u32x4*)(a.x + oo); }
          else if constexpr (q == 13) { on = more && __all(have >= need_of(pre)); va = on ? l16 : OOB; vb = (on && wave < 3) ? l16 : OOB; }
          else if constexpr (q - 14 < 26) hf[(q - 14) >> 1][q & 1] = ld128(rs, ((q - 14) < 24 ? va : vb) + (q & 1) * 1024, hb + spread(((q - 14) >> 1) * 2048));
          CFENCE();
        }
      });
      hf[13][0] = ld128(rs, vb, hb + spread(13 * 2048)); hf[13][1] = ld128(rs, vb + 1024, hb + spread(13 * 2048));
      adopt_cur(sc, cur, 2 * NSEG);
    }
    acc_wait(acc);
    u_scatter<34816, 0, 4>(acc, sca);
    BAR();
    f32x4 s0, s1;
    u_gather<34816>(s0, s1, gaa);
    u32x4 o;
    const u32x4 sp = cat4(pk4(s0), pk4(s1));
#pragma unroll
    for (int j = 0; j < 4; ++j)
      o[j] = pk(blo(xv[j]) + rbf(blo(sp[j]) * blo(gp[j])), bhi(xv[j]) + rbf(bhi(sp[j]) * bhi(gp[j])));
    *(GAS u32x4*)(a.out + oo) = o;
    EV(9);
    if (nxt >= NSEG) return;
    if (late) {
      if (wave == 0) { const unsigned t = ticket(); if (lane == 0) ldsw32(L_UT, t); }
      BAR();
      nxt = __builtin_amdgcn_readfirstlane(*(volatile LAS unsigned*)(uintptr_t)L_UT);
      if (nxt >= NSEG) return;
      have = ld32(rs, chk_vo(nxt), 0);
    }
    if (!on) start(nxt, have, true);
    seg = nxt;
  }
}

#define WDELAY 18
#define WPACE 0
#define WBLAG 1
#define WAUX 16
#define WAHEAD 3
DEV void role_w(rsrc_t rs, int xq, int k, int wave, int lane, unsigned pol) {
  const int cb0 = 10 * xq + 2 * k;
  unsigned sink = 0;
  int budget = SPIN_CAP;
  Sched sc; clk_init(sc);
  for (int seg = 1 + wave; seg < NSEG; seg += 4) {
    const int s = seg - WAHEAD;
    if (s >= 2) {
      while (sc.j0 == 0 && --budget > 0) { adopt_cur(sc, ld128(rs, 0, OFF_CTL + CTL_CUR), 2 * NSEG); CFENCE(); __builtin_amdgcn_s_sleep(8); }
      sleep_until_turn(rs, sc, 2 * s + 1, WDELAY * 8 - 240);
    }
    while (__builtin_amdgcn_readfirstlane(ld32(rs, seg * 128, OFF_CTL + CTL_RL)) == 0 && --budget > 0) { CFENCE(); __builtin_amdgcn_s_sleep(8); }
    if (s >= 2) wait_until(tau_of(sc, 2 * s + 1) + WDELAY * 8);
#define WLANES 64
    constexpr int NI = 64 / WLANES;
    unsigned v[12 * NI];
    const unsigned wvo = lane < WLANES ? lane * 128 : OOB;
#pragma unroll
    for (int j = 0; j < 12 * NI; ++j) {
      const int b = j / NI, u = min(k + 5 * (b - 8), 15);
      const unsigned so = b < 8 ? b_addr(max(seg - WBLAG, 0), cb0 + (b >> 2), pol) + (b & 3) * BSTR : kq_addr(u & 1, seg, (u >> 1) * 4 + xq, pol);
      v[j] = __builtin_amdgcn_raw_buffer_load_b32(rs, wvo, so + (j % NI) * WLANES * 128, WAUX); CFENCE(); __builtin_amdgcn_s_sleep(WPACE);
    }
#pragma unroll
    for (int j = 0; j < 12 * NI; ++j) sink += v[j];
    CFENCE();
  }
  asm volatile("" :: "v"(sink));
}

#define LEAD1 78
#define LEAD2 60
#define GEXTRA 300
#define ATOL 8
#define NADAPT 4
#define TKLAG 900
#define TKOFF 200
#define TKSLEEP 2
#define GPOLL 2600
#define GPOLL0 2700
DEV void role_tk(rsrc_t rs, int lane) {
  Sched sc; clk_init(sc);
  Sched old = sc;
  int budget = SPIN_CAP, n1c = 0, n2c = 0, m1 = 0, m2 = 0, relock = 0, jp1 = -9, jp2 = -9;
  unsigned Mp1 = 0, Mp2 = 0;
  bool locked = false;
  const unsigned tref = now8();
  auto tau_at = [&](int j) { return sc.j0 <= j ? tau_of(sc, j) : tau_of(old, j); };
  auto publish = [&](int j) {
    st128(rs, (u32x4){(unsigned)sc.j0, sc.base, (unsigned)sc.T, (unsigned)sc.D1 | (unsigned)sc.rx << 16}, lane < 8 ? lane * 16 : OOB, OFF_CTL + CTL_CUR);
    n1c = n2c = m1 = m2 = 0;
  };
  for (int j = 0; j < 2 * NSEG - 6; ++j) {
    if (locked) {
      while ((int)(now8() - tau_at(j + 2)) > TKOFF - 160) ++j;
      if (j >= 2 * NSEG - 6) break;
      wait_until(tau_at(j + 1) + TKOFF);
    }
    const unsigned vo = rep_off(j, lane);
    u32x4 e;
    for (;;) { e = ld128(rs, vo, OFF_REP); CFENCE(); if (__all(e.x != 0) || --budget <= 0) break; __builtin_amdgcn_s_sleep(TKSLEEP); }
    const bool zrow = !(j & 1) && lane >= 32;
    const unsigned M = tref + (unsigned)wmax((int)(e.y - tref) - (zrow ? VSLACK : 0));
    locked = __all(e.z & 1) && j >= relock;
    if (j + 1 >= relock) {
      const bool off = sc.j0 == 0 || (int)(M - tau_at(j + 1)) > LATE_THR || (j >= relock && !__any(e.z & 1));
      if (off) locked = false;
      if (off && (int)(now8() - M) < TKLAG) {
        int gap = sc.j0 == 0 ? GPOLL0 : GPOLL;
        if (j <= relock && jp2 == j - 2) gap = max(gap, min((int)(M - Mp2) + GEXTRA, 3600));
        sc.rx = j + 3;
        sc.base += M + gap - tau_of(sc, j + 3); sc.j0 = relock = j + 3;
        publish(j);
      }
    }
    Mp2 = Mp1; Mp1 = M; jp2 = jp1; jp1 = j;
    if (locked && sc.j0 <= j && __all((e.z & 3) == 3)) {
      const int d = wmax((int)((float)(int)(e.z >> 2) * 2048.f * __builtin_amdgcn_rcpf((float)e.w)) - (zrow ? VSLACK : 0));
      if (j & 1) { if (n2c < NADAPT) { m2 += d; ++n2c; } } else if (n1c < NADAPT) { m1 += d; ++n1c; }
      if (n1c == NADAPT && n2c == NADAPT) {
        const int n1 = m1 / NADAPT + LEAD1, n2 = m2 / NADAPT + LEAD2, sn = (j >> 1) + 3;
        if (abs(n1 - sc.D1) > ATOL || abs(n2 - (sc.T - sc.D1)) > ATOL) {
          old = sc;
          sc.base += sn * (sc.T - (n1 + n2)); sc.T = n1 + n2; sc.D1 = n1; sc.j0 = 2 * sn;
          publish(j);
        }
        n1c = n2c = m1 = m2 = 0;
      }
    }
  }
}

DEV bool run_roles(Args a, Boot& bt, int b, int ix, int wave, int lane) {
    PIN(lane); PINS(wave); PINS(ix); PINS(a.ws); PINS(a.x); PINS(a.wq); PINS(a.wk); PINS(a.wv); PINS(a.wo); PINS(a.w1); PINS(a.w3); PINS(a.w2); PINS(a.out);
    const rsrc_t rs = __builtin_amdgcn_make_buffer_rsrc(a.ws, 0, WS_BYTES, 0x00020000);
    const int xq = (b + bt.xoff) & 7;
    if (xq < 4) {
      const int r = ix * 4 + xq;
      if (r < 32) { if (!role_p(a, rs, r, wave, lane, bt)) return false; }
      else if (r < 64) { if (!role_v(a, rs, r - 32, wave, lane, bt)) return false; }
      else {
        if (r >= 124) {
          int extra = 0;
          if (wave == 0) extra = boot_probe(rs, r - 124, lane);
          BAR();
          if (!boot_hint(rs, bt, true)) return false;
          if (wave == 0) st128(rs, (u32x4){bt.t0, bt.t1, (unsigned)extra, 0}, lane < 8 ? (r - 64) * 128 + lane * 16 : OOB, OFF_CTL + CTL_XP);
        }
        u32x4 xv[2][5];
        ln_load(a, xv, r - 64, wave, lane);
        if (!boot_hint(rs, bt, true)) return false;
        if (r < 124) boot_init(rs, r - 64, wave, lane);
        ln_compute(xv, 0, wave, lane);
        ln_publish(rs, 0, r - 64, wave, lane, bt);
        EV(1);
        if (r >= 126 || (r >= 124 && wave > 0)) return true;
        boot_wait(rs, bt, lane);
        if (r < 104) role_c(a, rs, 10 * xq + ((r - 64) >> 2), wave, lane, bt.pol);
        else if (r < 124) role_w(rs, xq, (r - 104) >> 2, wave, lane, bt.pol);
        else if (r == 124) { role_tk(rs, lane); st64(rs, (u32x2){bt.t0, bt.t1}, lane < 16 ? lane * 8 : OOB, OFF_CTL + CTL_EP); }
        else role_agg(rs, lane);
      }
    } else {
      const int pid = (xq - 4) * 16 + (ix >> 1), par = ix & 1;
      const int h = pid >> 1;
      int cq;
      if (pid & 1) {
        if (!role_x<K_VB>(a, rs, h, par, 2 * VBN, par ? 2 * VBN : NSEG, prog_off(1, h, par, wave), wave, lane, bt)) return false;
        cq = par ? (h < 20 ? h % 10 : 4 + (h - 20) % 6) : h % 10;
      } else {
        if (!role_x<K_QK>(a, rs, h, par, 2 * QKN, par ? 2 * QKN : NSEG, prog_off(0, h, par, wave), wave, lane, bt)) return false;
        if (par && h < 28) role_x<K_F1>(a, rs, (h % 14) * 4 + wave, h / 14, NSEG, NSEG, hprog_off(h / 14, (h % 14) * 4 + wave), wave, lane, bt);
        cq = par ? (h < 20 ? h % 10 : h & 3) : h % 10;
      }
      role_u(a, rs, cq, wave, lane, bt.pol);
    }
    return true;
}

__global__ void __launch_bounds__(256, 1) k_main(Args a) {
  int lane = threadIdx.x & 63, wave = __builtin_amdgcn_readfirstlane(threadIdx.x >> 6);
  const rsrc_t rs = __builtin_amdgcn_make_buffer_rsrc(a.ws, 0, WS_BYTES, 0x00020000);
  EV(10);
  {
    unsigned long long pc; unsigned junk; int len;
    asm volatile("s_getpc_b64 %0\ncode_here_%=:\n\ts_mov_b32 %1, code_end_mark-code_here_%=" : "=s"(pc), "=s"(len));
    const unsigned off = min((int)((blockIdx.x >> 3) * 64 + lane) * 128, max(len, 0));
    if (wave == 3) asm volatile("global_load_dword %0, %1, %2\n\ts_waitcnt vmcnt(0)" : "=&v"(junk) : "v"(off), "s"(pc) : "memory");
  }
  const int b = blockIdx.x;
  int ix = b >> 3;
  Boot bt;
  {
    const u32x2 ep = ld64(rs, 0, OFF_CTL + CTL_EP);
    bt.t0 = __builtin_amdgcn_readfirstlane(ep.x) + 1; bt.t1 = __builtin_amdgcn_readfirstlane(ep.y);
    bt.xoff = (xcc_id() - b) & 7; bt.pol = 0;
    if (b == 0 && wave == 0) st128(rs, (u32x4){bt.t0, bt.t1, bt.xoff, 0}, lane < 8 ? lane * 16 : OOB, OFF_CTL + CTL_HINT);
  }
  EV(0);
  if (__builtin_expect(!run_roles(a, bt, b, ix, wave, lane), 0)) run_roles(a, bt, b, ix, wave, lane);
}
__global__ void k_end() { asm volatile("code_end_mark:"); }

extern "C" void kernel_launch(void* const* d_in, const int* in_sizes, int n_in, void* d_out, int out_size, void* d_ws, size_t ws_size, hipStream_t stream) {
  Args a;
  a.x = (const unsigned char*)d_in[0]; a.mem0 = (const unsigned char*)d_in[1]; a.wq = (const unsigned char*)d_in[2]; a.wk = (const unsigned char*)d_in[3];
  a.wv = (const unsigned char*)d_in[4]; a.wo = (const unsigned char*)d_in[5]; a.w1 = (const unsigned char*)d_in[8]; a.w3 = (const unsigned char*)d_in[9];
  a.w2 = (const unsigned char*)d_in[10]; a.out = (unsigned char*)d_out; a.ws = (unsigned char*)d_ws;
  (void)hipFuncSetAttribute((const void*)k_main, hipFuncAttributeMaxDynamicSharedMemorySize, 163840);
  k_main<<<256, 256, 163840, stream>>>(a);
}
```
